```python
import torch

def _softmax(x, axis=-1):
    return torch.softmax(x, dim=axis)
def _gelu(x):
    return torch.nn.functional.gelu(x, approximate="tanh")
def _silu(x):
    return torch.nn.functional.silu(x)
def _sigmoid(x):
    return torch.sigmoid(x)
def _layernorm(x, axis=-1, eps=1e-5):
    m = x.mean(dim=axis, keepdim=True)
    v = x.var(dim=axis, keepdim=True, unbiased=False)
    return (x - m) / torch.sqrt(v + eps)
def _scatter_add(tgt, idx, src, axis=0):
    return tgt.clone().index_add_(axis, idx, src)
def _segment_sum(src, seg, n):
    out = torch.zeros((n,) + src.shape[1:], dtype=src.dtype)
    return out.index_add_(0, seg, src)
def _pad(x, pw):
    flat = tuple(v for pair in reversed(pw) for v in pair)
    return torch.nn.functional.pad(x, flat)

kernel_name = 'concat_heads__gqa_block_cached__gqa_block_cached__near_ridge'

def setup_inputs():

    _w = (lambda a: (lambda t: t.to(torch.bfloat16) if t.is_floating_point() else t)(torch.as_tensor(a)))
    def _A_setup():
        B, Sq, D = 8, 144, 640
        Hq, Hkv, Sk, Dh = 32, 32, 1024, 128
        assert Hq % Hkv == 0, (Hq, Hkv)
        return {
            "x": _w((lambda *s: torch.randn(*s, dtype=torch.bfloat16) * 0.8)(B, Sq, D)),
            "k_cache": _w((lambda *s: torch.randn(*s, dtype=torch.bfloat16) * 0.8)(B, Hkv, Sk, Dh)),
            "v_cache": _w((lambda *s: torch.randn(*s, dtype=torch.bfloat16) * 0.05)(B, Hkv, Sk, Dh)),
            "wq": _w((lambda *s: torch.randn(*s, dtype=torch.bfloat16) * 0.8)(D, Hq * Dh) * (D ** -0.5)),
            "wo": _w((lambda *s: torch.randn(*s, dtype=torch.bfloat16) * 0.05)(Hq * Dh, D) * ((Hq * Dh) ** -0.5)),
        }
    _A_in = _A_setup()

    def _B_setup():
        B, Sq, D = 8, 144, 640
        Hq, Hkv, Sk, Dh = 32, 32, 1024, 128
        assert Hq % Hkv == 0, (Hq, Hkv)
        return {
            "x": _w((lambda *s: torch.randn(*s, dtype=torch.bfloat16) * 0.8)(B, Sq, D)),
            "k_cache": _w((lambda *s: torch.randn(*s, dtype=torch.bfloat16) * 0.8)(B, Hkv, Sk, Dh)),
            "v_cache": _w((lambda *s: torch.randn(*s, dtype=torch.bfloat16) * 0.05)(B, Hkv, Sk, Dh)),
            "wq": _w((lambda *s: torch.randn(*s, dtype=torch.bfloat16) * 0.8)(D, Hq * Dh) * (D ** -0.5)),
            "wo": _w((lambda *s: torch.randn(*s, dtype=torch.bfloat16) * 0.05)(Hq * Dh, D) * ((Hq * Dh) ** -0.5)),
        }
    _B_in = _B_setup()

    return {
        "A_x": _A_in["x"],
        "A_k_cache": _A_in["k_cache"],
        "A_v_cache": _A_in["v_cache"],
        "A_wq": _A_in["wq"],
        "A_wo": _A_in["wo"],
        "B_x": _B_in["x"],
        "B_k_cache": _B_in["k_cache"],
        "B_v_cache": _B_in["v_cache"],
        "B_wq": _B_in["wq"],
        "B_wo": _B_in["wo"],
    }

def reference(A_x, A_k_cache, A_v_cache, A_wq, A_wo, B_x, B_k_cache, B_v_cache, B_wq, B_wo):
    def _A_ref(x, k_cache, v_cache, wq, wo):
        B, Sq, D = x.shape
        Hq, Dh = 32, 128
        Hkv = k_cache.shape[1]
        rep = Hq // Hkv
        q = (lambda x, *d: x.permute(*d))((x @ wq).reshape(B, Sq, Hq, Dh), 0, 2, 1, 3)
        k = (lambda x, n, axis: torch.repeat_interleave(x, n, dim=axis))(k_cache, rep, axis=1)
        v = (lambda x, n, axis: torch.repeat_interleave(x, n, dim=axis))(v_cache, rep, axis=1)
        scores = torch.einsum("bhqd,bhkd->bhqk", q, k) * (Dh ** -0.5)
        p = _softmax(scores, axis=-1)
        o = torch.einsum("bhqk,bhkd->bhqd", p, v)
        return (lambda x, *d: x.permute(*d))(o, 0, 2, 1, 3).reshape(B, Sq, Hq * Dh) @ wo

    def _B_ref(x, k_cache, v_cache, wq, wo):
        B, Sq, D = x.shape
        Hq, Dh = 32, 128
        Hkv = k_cache.shape[1]
        rep = Hq // Hkv
        q = (lambda x, *d: x.permute(*d))((x @ wq).reshape(B, Sq, Hq, Dh), 0, 2, 1, 3)
        k = (lambda x, n, axis: torch.repeat_interleave(x, n, dim=axis))(k_cache, rep, axis=1)
        v = (lambda x, n, axis: torch.repeat_interleave(x, n, dim=axis))(v_cache, rep, axis=1)
        scores = torch.einsum("bhqd,bhkd->bhqk", q, k) * (Dh ** -0.5)
        p = _softmax(scores, axis=-1)
        o = torch.einsum("bhqk,bhkd->bhqd", p, v)
        return (lambda x, *d: x.permute(*d))(o, 0, 2, 1, 3).reshape(B, Sq, Hq * Dh) @ wo

    a = _A_ref(A_x, A_k_cache, A_v_cache, A_wq, A_wo)
    b = _B_ref(B_x, B_k_cache, B_v_cache, B_wq, B_wo)
    return torch.concatenate([a, b], axis=-1)
```

```cpp
#include <hip/hip_runtime.h>
#include <hip/hip_bf16.h>

typedef unsigned short u16;

constexpr int SQ = 144, DM = 640, NH = 32, SK = 1024, DH = 128;

__device__ __forceinline__ float bf2f(u16 v) { return __uint_as_float((unsigned)v << 16); }
__device__ __forceinline__ u16 f2bf(float f) { __bf16 b = (__bf16)f; return __builtin_bit_cast(u16, b); }

__device__ __forceinline__ void slow_item(const u16* __restrict__ x, const u16* __restrict__ w, const u16* __restrict__ k, const u16* __restrict__ v,
                                          u16* __restrict__ o, int wid, int lane, float* lds) {
  float* qs = lds;
  float* ps = lds + SQ * DH + wid * SK;
  const int tid = wid * 64 + lane;
  for (int q = tid >> 7; q < SQ; q += 2) {
    const int d = tid & 127; float acc = 0.f;
    for (int kk = 0; kk < DM; ++kk) acc = fmaf(bf2f(x[q * DM + kk]), bf2f(w[(size_t)kk * (NH * DH) + d]), acc);
    qs[q * DH + d] = bf2f(f2bf(acc));
  }
  __syncthreads();
  for (int q = wid; q < SQ; q += 4) {
    float m = -__builtin_inff();
    for (int i = 0; i < SK / 64; ++i) {
      const u16* kr = k + (size_t)(i * 64 + lane) * DH; float s = 0.f;
      for (int d = 0; d < DH; ++d) s = fmaf(qs[q * DH + d], bf2f(kr[d]), s);
      s *= 0.08838834764831845f;
      ps[i * 64 + lane] = s; m = fmaxf(m, s);
    }
    for (int off = 32; off; off >>= 1) m = fmaxf(m, __shfl_xor(m, off, 64));
    float l = 0.f;
    for (int i = 0; i < SK / 64; ++i) { const float p = __expf(ps[i * 64 + lane] - m); ps[i * 64 + lane] = p; l += p; }
    for (int off = 32; off; off >>= 1) l += __shfl_xor(l, off, 64);
    __builtin_amdgcn_fence(__ATOMIC_RELEASE, "wavefront"); __builtin_amdgcn_wave_barrier(); __builtin_amdgcn_fence(__ATOMIC_ACQUIRE, "wavefront");
    float a0 = 0.f, a1 = 0.f;
    for (int kk = 0; kk < SK; ++kk) { const float p = ps[kk]; a0 = fmaf(p, bf2f(v[(size_t)kk * DH + 2 * lane]), a0); a1 = fmaf(p, bf2f(v[(size_t)kk * DH + 2 * lane + 1]), a1); }
    const float rl = 1.f / l;
    const int d = 2 * lane, dd = d & 31, g = (dd & 15) >> 2, j = (dd & 3) + (dd >> 4) * 4;
    __hip_atomic_store((unsigned*)(o + ((size_t)(q >> 4) * 128 + (d >> 5)) * 512 + ((q & 15) + 16 * g) * 8 + j),
                       (unsigned)f2bf(a0 * rl) | (unsigned)f2bf(a1 * rl) << 16, __ATOMIC_RELAXED, __HIP_MEMORY_SCOPE_AGENT);
    __builtin_amdgcn_fence(__ATOMIC_RELEASE, "wavefront"); __builtin_amdgcn_wave_barrier(); __builtin_amdgcn_fence(__ATOMIC_ACQUIRE, "wavefront");
  }
  asm volatile("s_waitcnt vmcnt(0)" ::: "memory");
  __syncthreads();
}

__global__ __launch_bounds__(256, 1) void attn_kernel(const u16* __restrict__ xA, const u16* __restrict__ kA, const u16* __restrict__ vA, const u16* __restrict__ wA,
                                                      const u16* __restrict__ xB, const u16* __restrict__ kB, const u16* __restrict__ vB, const u16* __restrict__ wB,
                                                      const u16* __restrict__ woA, const u16* __restrict__ woB, u16* __restrict__ ows, u16* __restrict__ out) {
  extern __shared__ __attribute__((aligned(16))) char lds[];
  typedef const u16* const __attribute__((address_space(4)))* kargs_t;
  kargs_t ka = (kargs_t)__builtin_amdgcn_kernarg_segment_ptr();
  int g = blockIdx.x;
  const unsigned wid = __builtin_amdgcn_readfirstlane(threadIdx.x >> 6);
  unsigned bad; unsigned long long tag;
  asm volatile(R"ASM(
  s_bfe_u32 s50, %[gid], 0x10002
  s_lshl_b32 s51, s50, 5
  s_lshl_b32 s52, s50, 3
  s_add_u32 s52, s52, 64
  s_load_dwordx8 s[68:75], %[ka], s51
  s_load_dwordx2 s[80:81], %[ka], s52
  s_load_dwordx4 s[88:91], %[ka], 0x50
  s_mov_b32 s36, %[wid]
  s_nop 0
  s_and_b32 s53, %[gid], 3
  s_lshl_b32 s53, s53, 3
  s_bfe_u32 s54, %[gid], 0x20003
  s_add_u32 s53, s53, s54
  s_lshr_b32 s54, %[gid], 5
  s_lshl_b32 s55, s54, 5
  s_add_u32 s55, s55, s53
  s_lshl_b32 s55, s55, 18
  s_mul_i32 s51, s54, 184320
  s_lshl_b32 s52, s53, 8
  s_lshl_b32 s50, s50, 3
  s_add_u32 s50, s50, s54
  s_mul_i32 s50, s50, 1152
  s_lshl_b32 s53, s53, 2
  s_add_u32 s50, s50, s53
  s_lshl_b32 s50, s50, 10
  s_mov_b32 s18, 1311744
  s_mov_b32 s19, 0x20000
  s_mov_b32 s22, 1311744
  s_mov_b32 s23, 0x20000
  s_mov_b32 s26, 184320
  s_mov_b32 s27, 0x20000
  s_mov_b32 s30, 5235968
  s_mov_b32 s31, 0x20000
  s_mov_b32 s66, 1069056
  s_mov_b32 s67, 0x20000
  s_waitcnt lgkmcnt(0)
  s_add_u32 s28, s74, s52
  s_addc_u32 s29, s75, 0
  s_add_u32 s24, s68, s51
  s_addc_u32 s25, s69, 0
  s_add_u32 s16, s70, s55
  s_addc_u32 s17, s71, 0
  s_add_u32 s20, s72, s55
  s_addc_u32 s21, s73, 0
  s_add_u32 s64, s88, s50
  s_addc_u32 s65, s89, 0
  s_sub_u32 s16, s16, 1024
  s_subb_u32 s17, s17, 0
  s_sub_u32 s20, s20, 1024
  s_subb_u32 s21, s21, 0
  s_add_u32 s92, s88, 19922944
  s_addc_u32 s93, s89, 0
  v_mov_b32_e32 v61, 0xff00
  global_load_dwordx2 v[62:63], v61, s[92:93] sc1
  v_mbcnt_lo_u32_b32 v27, -1, 0
  v_mbcnt_hi_u32_b32 v27, -1, v27
  v_lshlrev_b32_e32 v0, 4, v27
  s_mov_b32 s46, 0
  s_mov_b32 s45, 0
  s_mov_b32 s48, 0
  s_mov_b32 s95, 2
  v_lshrrev_b32_e32 v28, 4, v27
  v_and_b32_e32 v29, 15, v27
  s_and_b32 s50, s36, 1
  s_lshl_b32 s51, s50, 2
  v_or_b32_e32 v30, s51, v28
  v_lshlrev_b32_e32 v30, 1, v30
  v_xor_b32_e32 v30, v30, v29
  v_lshlrev_b32_e32 v30, 4, v30
  v_lshl_add_u32 v30, v28, 13, v30
  s_lshl_b32 s53, s36, 16
  v_add_u32_e32 v11, s53, v30
  v_add_u32_e32 v12, 31744, v11
  s_lshl_b32 s52, s36, 11
  s_add_u32 s52, s52, 83968
  v_lshrrev_b32_e32 v28, 3, v27
  v_and_b32_e32 v29, 7, v27
  v_lshrrev_b32_e32 v30, 1, v28
  v_xor_b32_e32 v29, v29, v30
  s_mul_i32 s50, s36, 5
  s_min_u32 s50, s50, 13
  s_and_b32 s51, s50, 1
  s_lshl_b32 s51, s51, 2
  v_xor_b32_e32 v29, s51, v29
  v_xor_b32_e32 v30, 4, v29
  v_mul_u32_u24_e32 v28, 0x500, v28
  s_mul_i32 s51, s50, 10240
  s_lshl_b32 s55, s50, 10
  v_add_u32_e32 v28, s51, v28
  v_lshl_add_u32 v29, v29, 4, v28
  v_lshl_add_u32 v30, v30, 4, v28
  v_mov_b32_e32 v32, v29
  v_add_u32_e32 v33, 9216, v30
  v_add_u32_e32 v34, 18432, v29
  v_add_u32_e32 v35, 27648, v30
  v_add_u32_e32 v36, 40960, v29
  s_getpc_b64 s[68:69]
.Lcw%=:
  s_mov_b32 s70, .Lend%=-.Lcw%=
  s_mov_b32 s71, 0x20000
  s_lshr_b32 s76, %[gid], 3
  s_lshl_b32 s76, s76, 2
  s_add_u32 s76, s76, s36
  s_lshl_b32 s76, s76, 7
  v_lshlrev_b32_e32 v28, 14, v27
  s_nop 1
  buffer_load_dword v25, v28, s[68:71], s76 offen
  s_mov_b32 s49, 0
  s_add_u32 m0, s52, 16384
  s_nop 0
  buffer_load_dwordx4 v11, s[28:31], s49 offen lds
  buffer_load_dwordx4 v12, s[28:31], s49 offen offset:1024 lds
  s_mov_b32 s53, 1024
  s_add_u32 m0, s52, 24576
  s_nop 0
  buffer_load_dwordx4 v11, s[28:31], s53 offen lds
  buffer_load_dwordx4 v12, s[28:31], s53 offen offset:1024 lds
  s_mov_b32 s54, 0
  s_add_u32 m0, s55, 65536
  s_nop 0
  buffer_load_dwordx4 v32, s[24:27], s54 offen lds
  buffer_load_dwordx4 v33, s[24:27], s54 offen offset:1024 lds
  buffer_load_dwordx4 v34, s[24:27], s54 offen offset:2048 lds
  buffer_load_dwordx4 v35, s[24:27], s54 offen offset:3072 lds
  s_add_u32 m0, s55, 69632
  s_nop 0
  buffer_load_dwordx4 v36, s[24:27], s54 offen lds
  s_mov_b32 s49, 262144
  s_add_u32 m0, s52, 32768
  s_nop 0
  buffer_load_dwordx4 v11, s[28:31], s49 offen lds
  buffer_load_dwordx4 v12, s[28:31], s49 offen offset:1024 lds
  s_mov_b32 s53, 263168
  s_add_u32 m0, s52, 40960
  s_nop 0
  buffer_load_dwordx4 v11, s[28:31], s53 offen lds
  buffer_load_dwordx4 v12, s[28:31], s53 offen offset:1024 lds
  s_lshr_b32 s76, %[gid], 3
  s_and_b32 s77, s76, 3
  s_lshl_b32 s77, s77, 8
  s_sub_u32 s68, s28, s77
  s_subb_u32 s69, s29, 0
  s_mov_b32 s70, 5236736
  s_mov_b32 s71, 0x20000
  s_lshr_b32 s77, s76, 2
  s_mul_i32 s77, s77, 184320
  s_sub_u32 s72, s24, s77
  s_subb_u32 s73, s25, 0
  s_mov_b32 s74, 1474560
  s_mov_b32 s75, 0x20000
  s_lshl_b32 s76, s76, 2
  s_add_u32 s76, s76, s36
  s_mul_i32 s77, s76, 11520
  v_mul_u32_u24_e32 v30, 0x500, v27
  v_add_u32_e32 v30, s77, v30
  v_mov_b32_e32 v31, 0x7f000000
  v_cmp_gt_u32_e32 vcc, 9, v27
  s_lshr_b32 s77, s76, 1
  s_lshl_b32 s77, s77, 13
  v_cndmask_b32_e32 v26, v31, v30, vcc
  s_and_b32 s78, s76, 1
  s_lshl_b32 s78, s78, 10
  s_add_u32 s77, s77, s78
  v_lshlrev_b32_e32 v29, 7, v27
  v_add_u32_e32 v30, s77, v29
  v_cmp_gt_u32_e32 vcc, 8, v27
  s_nop 1
  v_cndmask_b32_e32 v24, v31, v30, vcc
  v_lshrrev_b32_e32 v28, 4, v27
  v_and_b32_e32 v29, 15, v27
  v_lshrrev_b32_e32 v30, 1, v29
  v_xor_b32_e32 v30, v30, v28
  v_lshlrev_b32_e32 v31, 7, v29
  v_add_u32_e32 v31, 81920, v31
  s_lshl_b32 s63, s36, 12
  s_sub_u32 s63, s63, 16384
  v_lshl_add_u32 v15, v30, 4, v31
  v_xor_b32_e32 v30, 4, v30
  v_lshl_add_u32 v16, v30, 4, v31
  v_add_u32_e32 v13, s63, v15
  v_add_u32_e32 v14, s63, v16
  v_add_u32_e32 v37, 67584, v13
  v_add_u32_e32 v38, 67584, v14
  v_add_u32_e32 v39, 67584, v15
  v_add_u32_e32 v40, 67584, v16
  v_bfe_u32 v29, v27, 2, 2
  v_lshl_add_u32 v30, v28, 3, v29
  v_lshlrev_b32_e32 v30, 8, v30
  v_and_b32_e32 v31, 1, v27
  v_lshl_add_u32 v30, v31, 3, v30
  v_add_u32_e32 v30, 83968, v30
  v_and_b32_e32 v31, 1, v28
  v_lshl_or_b32 v29, v31, 2, v29
  v_lshlrev_b32_e32 v29, 1, v29
  v_bfe_u32 v31, v27, 1, 1
  v_xor_b32_e32 v29, v29, v31
  s_lshl_b32 s63, s36, 2
  s_add_u32 s47, s63, 0
  s_and_b32 s47, s47, 15
  v_xor_b32_e32 v31, s47, v29
  v_lshl_add_u32 v41, v31, 4, v30
  s_add_u32 s47, s63, 2
  s_and_b32 s47, s47, 15
  v_xor_b32_e32 v31, s47, v29
  v_lshl_add_u32 v42, v31, 4, v30
  s_add_u32 s47, s63, 4
  s_and_b32 s47, s47, 15
  v_xor_b32_e32 v31, s47, v29
  v_lshl_add_u32 v43, v31, 4, v30
  s_add_u32 s47, s63, 6
  s_and_b32 s47, s47, 15
  v_xor_b32_e32 v31, s47, v29
  v_lshl_add_u32 v44, v31, 4, v30
  s_add_u32 s47, s63, 8
  s_and_b32 s47, s47, 15
  v_xor_b32_e32 v31, s47, v29
  v_lshl_add_u32 v45, v31, 4, v30
  s_add_u32 s47, s63, 10
  s_and_b32 s47, s47, 15
  v_xor_b32_e32 v31, s47, v29
  v_lshl_add_u32 v46, v31, 4, v30
  s_add_u32 s47, s63, 12
  s_and_b32 s47, s47, 15
  v_xor_b32_e32 v31, s47, v29
  v_lshl_add_u32 v47, v31, 4, v30
  s_add_u32 s47, s63, 14
  s_and_b32 s47, s47, 15
  v_xor_b32_e32 v31, s47, v29
  v_lshl_add_u32 v48, v31, 4, v30
  s_mov_b32 s54, 128
  s_add_u32 m0, s55, 133120
  s_nop 0
  buffer_load_dwordx4 v32, s[24:27], s54 offen lds
  buffer_load_dwordx4 v33, s[24:27], s54 offen offset:1024 lds
  buffer_load_dwordx4 v34, s[24:27], s54 offen offset:2048 lds
  buffer_load_dwordx4 v35, s[24:27], s54 offen offset:3072 lds
  s_add_u32 m0, s55, 137216
  s_nop 0
  buffer_load_dwordx4 v36, s[24:27], s54 offen lds
  s_mov_b32 s49, 524288
  s_mov_b32 m0, s52
  s_nop 0
  buffer_load_dwordx4 v11, s[28:31], s49 offen lds
  buffer_load_dwordx4 v12, s[28:31], s49 offen offset:1024 lds
  s_mov_b32 s53, 525312
  s_add_u32 m0, s52, 8192
  s_nop 0
  buffer_load_dwordx4 v11, s[28:31], s53 offen lds
  buffer_load_dwordx4 v12, s[28:31], s53 offen offset:1024 lds
  s_mov_b32 s78, 256
  s_nop 0
  buffer_load_dword v25, v26, s[72:75], s78 offen
  s_mov_b32 s79, 1048576
  s_nop 0
  buffer_load_dword v25, v24, s[68:71], s79 offen
  s_mov_b32 s78, 384
  s_nop 0
  buffer_load_dword v25, v26, s[72:75], s78 offen
  s_mov_b32 s79, 1572864
  s_nop 0
  buffer_load_dword v25, v24, s[68:71], s79 offen
  s_waitcnt vmcnt(17)
  s_barrier
  v_readfirstlane_b32 s82, v62
  v_readfirstlane_b32 s83, v63
  s_nop 0
  s_add_u32 s82, s82, 1
  s_addc_u32 s83, s83, 0
  s_bfe_u32 s47, %[gid], 0x30003
.Lstag%=:
  s_cmp_eq_u32 s47, 0
  s_cbranch_scc1 .Lstag2%=
  s_sleep 4
  s_sub_u32 s47, s47, 1
  s_branch .Lstag%=
.Lstag2%=:
  ds_read_b128 a[8:11], v15
  ds_read_b64_tr_b16 a[24:25], v41 offset:16384
  ds_read_b64_tr_b16 a[26:27], v41 offset:17408
  ds_read_b64_tr_b16 a[28:29], v42 offset:16384
  ds_read_b64_tr_b16 a[30:31], v42 offset:17408
  ds_read_b64_tr_b16 a[56:57], v41 offset:24576
  ds_read_b64_tr_b16 a[58:59], v41 offset:25600
  ds_read_b64_tr_b16 a[60:61], v42 offset:24576
  ds_read_b64_tr_b16 a[62:63], v42 offset:25600
  ds_read_b128 a[0:3], v13
  ds_read_b64_tr_b16 a[32:33], v43 offset:16384
  ds_read_b64_tr_b16 a[34:35], v43 offset:17408
  ds_read_b64_tr_b16 a[36:37], v44 offset:16384
  ds_read_b64_tr_b16 a[38:39], v44 offset:17408
  ds_read_b64_tr_b16 a[40:41], v45 offset:16384
  ds_read_b64_tr_b16 a[42:43], v45 offset:17408
  ds_read_b64_tr_b16 a[44:45], v46 offset:16384
  ds_read_b64_tr_b16 a[46:47], v46 offset:17408
  ds_read_b64_tr_b16 a[48:49], v47 offset:16384
  ds_read_b64_tr_b16 a[50:51], v47 offset:17408
  ds_read_b64_tr_b16 a[52:53], v48 offset:16384
  ds_read_b64_tr_b16 a[54:55], v48 offset:17408
  ds_read_b128 a[4:7], v13 offset:2048
  ds_read_b64_tr_b16 a[64:65], v43 offset:24576
  ds_read_b64_tr_b16 a[66:67], v43 offset:25600
  ds_read_b64_tr_b16 a[68:69], v44 offset:24576
  ds_read_b64_tr_b16 a[70:71], v44 offset:25600
  ds_read_b64_tr_b16 a[72:73], v45 offset:24576
  ds_read_b64_tr_b16 a[74:75], v45 offset:25600
  ds_read_b64_tr_b16 a[76:77], v46 offset:24576
  ds_read_b64_tr_b16 a[78:79], v46 offset:25600
  ds_read_b64_tr_b16 a[80:81], v47 offset:24576
  ds_read_b64_tr_b16 a[82:83], v47 offset:25600
  ds_read_b64_tr_b16 a[84:85], v48 offset:24576
  ds_read_b64_tr_b16 a[86:87], v48 offset:25600
  s_waitcnt vmcnt(13) lgkmcnt(0)
  s_barrier
  v_mfma_f32_16x16x32_bf16 v[112:115], a[24:27], a[8:11], 0
  s_mov_b32 s49, 786432
  s_add_u32 m0, s52, 16384
  ds_read_b128 a[20:23], v16
  ds_read_b64_tr_b16 a[88:89], v41 offset:32768
  v_mfma_f32_16x16x32_bf16 v[116:119], a[28:31], a[8:11], 0
  ds_read_b64_tr_b16 a[90:91], v41 offset:33792
  buffer_load_dwordx4 v11, s[28:31], s49 offen lds
  v_mfma_f32_16x16x32_bf16 v[120:123], a[56:59], a[8:11], 0
  ds_read_b64_tr_b16 a[92:93], v42 offset:32768
  v_mfma_f32_16x16x32_bf16 v[124:127], a[60:63], a[8:11], 0
  ds_read_b64_tr_b16 a[94:95], v42 offset:33792
  v_mfma_f32_16x16x32_bf16 v[128:131], a[24:27], a[0:3], 0
  ds_read_b64_tr_b16 a[120:121], v41 offset:40960
  v_mfma_f32_16x16x32_bf16 v[132:135], a[28:31], a[0:3], 0
  ds_read_b64_tr_b16 a[122:123], v41 offset:41984
  v_mfma_f32_16x16x32_bf16 v[136:139], a[32:35], a[0:3], 0
  ds_read_b64_tr_b16 a[124:125], v42 offset:40960
  buffer_load_dwordx4 v12, s[28:31], s49 offen offset:1024 lds
  v_mfma_f32_16x16x32_bf16 v[140:143], a[36:39], a[0:3], 0
  ds_read_b64_tr_b16 a[126:127], v42 offset:41984
  v_mfma_f32_16x16x32_bf16 v[144:147], a[40:43], a[0:3], 0
  ds_read_b128 a[12:15], v14
  v_mfma_f32_16x16x32_bf16 v[148:151], a[44:47], a[0:3], 0
  ds_read_b64_tr_b16 a[96:97], v43 offset:32768
  v_mfma_f32_16x16x32_bf16 v[152:155], a[48:51], a[0:3], 0
  s_mov_b32 s53, 787456
  s_add_u32 m0, s52, 24576
  ds_read_b64_tr_b16 a[98:99], v43 offset:33792
  v_mfma_f32_16x16x32_bf16 v[156:159], a[52:55], a[0:3], 0
  ds_read_b64_tr_b16 a[100:101], v44 offset:32768
  buffer_load_dwordx4 v11, s[28:31], s53 offen lds
  v_mfma_f32_16x16x32_bf16 v[160:163], a[24:27], a[4:7], 0
  ds_read_b64_tr_b16 a[102:103], v44 offset:33792
  v_mfma_f32_16x16x32_bf16 v[164:167], a[28:31], a[4:7], 0
  ds_read_b64_tr_b16 a[104:105], v45 offset:32768
  v_mfma_f32_16x16x32_bf16 v[168:171], a[32:35], a[4:7], 0
  ds_read_b64_tr_b16 a[106:107], v45 offset:33792
  v_mfma_f32_16x16x32_bf16 v[172:175], a[36:39], a[4:7], 0
  ds_read_b64_tr_b16 a[108:109], v46 offset:32768
  v_mfma_f32_16x16x32_bf16 v[176:179], a[40:43], a[4:7], 0
  ds_read_b64_tr_b16 a[110:111], v46 offset:33792
  ds_read_b64_tr_b16 a[112:113], v47 offset:32768
  buffer_load_dwordx4 v12, s[28:31], s53 offen offset:1024 lds
  v_mfma_f32_16x16x32_bf16 v[180:183], a[44:47], a[4:7], 0
  ds_read_b64_tr_b16 a[114:115], v47 offset:33792
  v_mfma_f32_16x16x32_bf16 v[184:187], a[48:51], a[4:7], 0
  ds_read_b64_tr_b16 a[116:117], v48 offset:32768
  s_sleep 4
  v_mfma_f32_16x16x32_bf16 v[188:191], a[52:55], a[4:7], 0
  ds_read_b64_tr_b16 a[118:119], v48 offset:33792
  v_mfma_f32_16x16x32_bf16 v[192:195], a[56:59], a[0:3], 0
  s_mov_b32 s78, 512
  ds_read_b128 a[16:19], v14 offset:2048
  v_mfma_f32_16x16x32_bf16 v[196:199], a[60:63], a[0:3], 0
  ds_read_b64_tr_b16 a[128:129], v43 offset:40960
  buffer_load_dword v25, v26, s[72:75], s78 offen
  v_mfma_f32_16x16x32_bf16 v[200:203], a[64:67], a[0:3], 0
  ds_read_b64_tr_b16 a[130:131], v43 offset:41984
  v_mfma_f32_16x16x32_bf16 v[204:207], a[68:71], a[0:3], 0
  ds_read_b64_tr_b16 a[132:133], v44 offset:40960
  v_mfma_f32_16x16x32_bf16 v[208:211], a[72:75], a[0:3], 0
  ds_read_b64_tr_b16 a[134:135], v44 offset:41984
  v_mfma_f32_16x16x32_bf16 v[212:215], a[76:79], a[0:3], 0
  s_mov_b32 s79, 2097152
  ds_read_b64_tr_b16 a[136:137], v45 offset:40960
  v_mfma_f32_16x16x32_bf16 v[216:219], a[80:83], a[0:3], 0
  ds_read_b64_tr_b16 a[138:139], v45 offset:41984
  buffer_load_dword v25, v24, s[68:71], s79 offen
  v_mfma_f32_16x16x32_bf16 v[220:223], a[84:87], a[0:3], 0
  ds_read_b64_tr_b16 a[140:141], v46 offset:40960
  v_mfma_f32_16x16x32_bf16 v[224:227], a[56:59], a[4:7], 0
  ds_read_b64_tr_b16 a[142:143], v46 offset:41984
  v_mfma_f32_16x16x32_bf16 v[228:231], a[60:63], a[4:7], 0
  ds_read_b64_tr_b16 a[144:145], v47 offset:40960
  v_mfma_f32_16x16x32_bf16 v[232:235], a[64:67], a[4:7], 0
  ds_read_b64_tr_b16 a[146:147], v47 offset:41984
  v_mfma_f32_16x16x32_bf16 v[236:239], a[68:71], a[4:7], 0
  ds_read_b64_tr_b16 a[148:149], v48 offset:40960
  v_bfe_u32 v28, v27, 2, 3
  v_lshlrev_b32_e32 v29, 8, v28
  v_lshrrev_b32_e32 v30, 5, v27
  v_lshl_add_u32 v29, v30, 6, v29
  v_mfma_f32_16x16x32_bf16 v[240:243], a[72:75], a[4:7], 0
  ds_read_b64_tr_b16 a[150:151], v48 offset:41984
  s_lshl_b32 s37, s36, 11
  s_add_u32 s50, s37, 1024
  s_and_b32 s51, s36, 1
  v_add_u32_e32 v29, s50, v29
  v_mfma_f32_16x16x32_bf16 v[244:247], a[76:79], a[4:7], 0
  v_and_b32_e32 v30, 3, v27
  v_xor_b32_e32 v31, s51, v30
  v_lshl_add_u32 v1, v31, 4, v29
  v_lshrrev_b32_e32 v28, 2, v28
  v_mfma_f32_16x16x32_bf16 v[248:251], a[80:83], a[4:7], 0
  v_lshlrev_b32_e32 v28, 1, v28
  v_xor_b32_e32 v30, v30, v28
  v_lshl_add_u32 v3, v30, 4, v29
  v_subrev_u32_e32 v2, 0x380, v1
  v_mfma_f32_16x16x32_bf16 v[252:255], a[84:87], a[4:7], 0
  v_subrev_u32_e32 v4, 0x380, v3
  v_bfe_u32 v28, v27, 3, 1
  v_and_b32_e32 v29, 7, v27
  s_waitcnt vmcnt(10) lgkmcnt(0)
  s_barrier
  v_mfma_f32_16x16x32_bf16 v[112:115], a[88:91], a[20:23], v[112:115]
  s_mov_b32 s54, 256
  s_add_u32 m0, s55, 65536
  ds_read_b128 a[8:11], v39
  ds_read_b64_tr_b16 a[24:25], v41
  v_mfma_f32_16x16x32_bf16 v[116:119], a[92:95], a[20:23], v[116:119]
  ds_read_b64_tr_b16 a[26:27], v41 offset:1024
  buffer_load_dwordx4 v32, s[24:27], s54 offen lds
  v_mfma_f32_16x16x32_bf16 v[120:123], a[120:123], a[20:23], v[120:123]
  ds_read_b64_tr_b16 a[28:29], v42
  v_mfma_f32_16x16x32_bf16 v[124:127], a[124:127], a[20:23], v[124:127]
  ds_read_b64_tr_b16 a[30:31], v42 offset:1024
  v_mfma_f32_16x16x32_bf16 v[128:131], a[88:91], a[12:15], v[128:131]
  ds_read_b64_tr_b16 a[56:57], v41 offset:8192
  buffer_load_dwordx4 v33, s[24:27], s54 offen offset:1024 lds
  v_mfma_f32_16x16x32_bf16 v[132:135], a[92:95], a[12:15], v[132:135]
  ds_read_b64_tr_b16 a[58:59], v41 offset:9216
  v_mfma_f32_16x16x32_bf16 v[136:139], a[96:99], a[12:15], v[136:139]
  ds_read_b64_tr_b16 a[60:61], v42 offset:8192
  v_mfma_f32_16x16x32_bf16 v[140:143], a[100:103], a[12:15], v[140:143]
  ds_read_b64_tr_b16 a[62:63], v42 offset:9216
  v_mfma_f32_16x16x32_bf16 v[144:147], a[104:107], a[12:15], v[144:147]
  ds_read_b128 a[0:3], v37
  buffer_load_dwordx4 v34, s[24:27], s54 offen offset:2048 lds
  v_mfma_f32_16x16x32_bf16 v[148:151], a[108:111], a[12:15], v[148:151]
  ds_read_b64_tr_b16 a[32:33], v43
  v_mfma_f32_16x16x32_bf16 v[152:155], a[112:115], a[12:15], v[152:155]
  ds_read_b64_tr_b16 a[34:35], v43 offset:1024
  v_mfma_f32_16x16x32_bf16 v[156:159], a[116:119], a[12:15], v[156:159]
  ds_read_b64_tr_b16 a[36:37], v44
  v_mfma_f32_16x16x32_bf16 v[160:163], a[88:91], a[16:19], v[160:163]
  ds_read_b64_tr_b16 a[38:39], v44 offset:1024
  buffer_load_dwordx4 v35, s[24:27], s54 offen offset:3072 lds
  v_mfma_f32_16x16x32_bf16 v[164:167], a[92:95], a[16:19], v[164:167]
  ds_read_b64_tr_b16 a[40:41], v45
  v_mfma_f32_16x16x32_bf16 v[168:171], a[96:99], a[16:19], v[168:171]
  ds_read_b64_tr_b16 a[42:43], v45 offset:1024
  v_mfma_f32_16x16x32_bf16 v[172:175], a[100:103], a[16:19], v[172:175]
  s_add_u32 m0, s55, 69632
  ds_read_b64_tr_b16 a[44:45], v46
  v_mfma_f32_16x16x32_bf16 v[176:179], a[104:107], a[16:19], v[176:179]
  ds_read_b64_tr_b16 a[46:47], v46 offset:1024
  ds_read_b64_tr_b16 a[48:49], v47
  buffer_load_dwordx4 v36, s[24:27], s54 offen lds
  v_mfma_f32_16x16x32_bf16 v[180:183], a[108:111], a[16:19], v[180:183]
  ds_read_b64_tr_b16 a[50:51], v47 offset:1024
  v_mfma_f32_16x16x32_bf16 v[184:187], a[112:115], a[16:19], v[184:187]
  s_mov_b32 s49, 1048576
  s_add_u32 m0, s52, 32768
  ds_read_b64_tr_b16 a[52:53], v48
  s_sleep 4
  v_mfma_f32_16x16x32_bf16 v[188:191], a[116:119], a[16:19], v[188:191]
  ds_read_b64_tr_b16 a[54:55], v48 offset:1024
  buffer_load_dwordx4 v11, s[28:31], s49 offen lds
  v_mfma_f32_16x16x32_bf16 v[192:195], a[120:123], a[12:15], v[192:195]
  ds_read_b128 a[4:7], v37 offset:2048
  v_mfma_f32_16x16x32_bf16 v[196:199], a[124:127], a[12:15], v[196:199]
  ds_read_b64_tr_b16 a[64:65], v43 offset:8192
  v_mfma_f32_16x16x32_bf16 v[200:203], a[128:131], a[12:15], v[200:203]
  ds_read_b64_tr_b16 a[66:67], v43 offset:9216
  v_mfma_f32_16x16x32_bf16 v[204:207], a[132:135], a[12:15], v[204:207]
  ds_read_b64_tr_b16 a[68:69], v44 offset:8192
  buffer_load_dwordx4 v12, s[28:31], s49 offen offset:1024 lds
  v_mfma_f32_16x16x32_bf16 v[208:211], a[136:139], a[12:15], v[208:211]
  ds_read_b64_tr_b16 a[70:71], v44 offset:9216
  v_mfma_f32_16x16x32_bf16 v[212:215], a[140:143], a[12:15], v[212:215]
  ds_read_b64_tr_b16 a[72:73], v45 offset:8192
  v_mfma_f32_16x16x32_bf16 v[216:219], a[144:147], a[12:15], v[216:219]
  s_mov_b32 s53, 1049600
  s_add_u32 m0, s52, 40960
  ds_read_b64_tr_b16 a[74:75], v45 offset:9216
  v_mfma_f32_16x16x32_bf16 v[220:223], a[148:151], a[12:15], v[220:223]
  ds_read_b64_tr_b16 a[76:77], v46 offset:8192
  buffer_load_dwordx4 v11, s[28:31], s53 offen lds
  v_mfma_f32_16x16x32_bf16 v[224:227], a[120:123], a[16:19], v[224:227]
  ds_read_b64_tr_b16 a[78:79], v46 offset:9216
  v_mfma_f32_16x16x32_bf16 v[228:231], a[124:127], a[16:19], v[228:231]
  ds_read_b64_tr_b16 a[80:81], v47 offset:8192
  v_mfma_f32_16x16x32_bf16 v[232:235], a[128:131], a[16:19], v[232:235]
  ds_read_b64_tr_b16 a[82:83], v47 offset:9216
  v_mfma_f32_16x16x32_bf16 v[236:239], a[132:135], a[16:19], v[236:239]
  ds_read_b64_tr_b16 a[84:85], v48 offset:8192
  v_lshlrev_b32_e32 v29, 6, v29
  v_lshl_add_u32 v29, v28, 11, v29
  v_bfe_u32 v30, v27, 4, 1
  v_lshrrev_b32_e32 v31, 5, v27
  buffer_load_dwordx4 v12, s[28:31], s53 offen offset:1024 lds
  v_mfma_f32_16x16x32_bf16 v[240:243], a[136:139], a[16:19], v[240:243]
  ds_read_b64_tr_b16 a[86:87], v48 offset:9216
  v_lshl_add_u32 v30, v30, 1, v31
  v_xor_b32_e32 v30, v30, v28
  v_lshl_add_u32 v5, v30, 4, v29
  v_lshlrev_b32_e32 v31, 11, v31
  v_mfma_f32_16x16x32_bf16 v[244:247], a[140:143], a[16:19], v[244:247]
  v_bfe_u32 v28, v27, 2, 3
  v_lshl_add_u32 v31, v28, 6, v31
  v_and_b32_e32 v28, 1, v27
  v_lshl_add_u32 v31, v28, 3, v31
  v_mfma_f32_16x16x32_bf16 v[248:251], a[144:147], a[16:19], v[248:251]
  v_add_u32_e32 v31, 0x2000, v31
  v_bfe_u32 v28, v27, 4, 1
  v_bfe_u32 v29, v27, 1, 1
  v_lshl_or_b32 v28, v28, 1, v29
  v_mfma_f32_16x16x32_bf16 v[252:255], a[148:151], a[16:19], v[252:255]
  v_lshl_add_u32 v6, v28, 4, v31
  v_xor_b32_e32 v28, 2, v28
  v_lshl_add_u32 v7, v28, 4, v31
  s_add_u32 s84, s36, 1
  s_waitcnt vmcnt(11) lgkmcnt(0)
  s_barrier
  v_mfma_f32_16x16x32_bf16 v[112:115], a[24:27], a[8:11], v[112:115]
  s_mov_b32 s49, 1310720
  s_mov_b32 m0, s52
  ds_read_b128 a[20:23], v40
  ds_read_b64_tr_b16 a[88:89], v41 offset:16384
  v_mfma_f32_16x16x32_bf16 v[116:119], a[28:31], a[8:11], v[116:119]
  ds_read_b64_tr_b16 a[90:91], v41 offset:17408
  buffer_load_dwordx4 v11, s[28:31], s49 offen lds
  v_mfma_f32_16x16x32_bf16 v[120:123], a[56:59], a[8:11], v[120:123]
  ds_read_b64_tr_b16 a[92:93], v42 offset:16384
  v_mfma_f32_16x16x32_bf16 v[124:127], a[60:63], a[8:11], v[124:127]
  ds_read_b64_tr_b16 a[94:95], v42 offset:17408
  v_mfma_f32_16x16x32_bf16 v[128:131], a[24:27], a[0:3], v[128:131]
  ds_read_b64_tr_b16 a[120:121], v41 offset:24576
  v_mfma_f32_16x16x32_bf16 v[132:135], a[28:31], a[0:3], v[132:135]
  ds_read_b64_tr_b16 a[122:123], v41 offset:25600
  v_mfma_f32_16x16x32_bf16 v[136:139], a[32:35], a[0:3], v[136:139]
  ds_read_b64_tr_b16 a[124:125], v42 offset:24576
  buffer_load_dwordx4 v12, s[28:31], s49 offen offset:1024 lds
  v_mfma_f32_16x16x32_bf16 v[140:143], a[36:39], a[0:3], v[140:143]
  ds_read_b64_tr_b16 a[126:127], v42 offset:25600
  v_mfma_f32_16x16x32_bf16 v[144:147], a[40:43], a[0:3], v[144:147]
  ds_read_b128 a[12:15], v38
  v_mfma_f32_16x16x32_bf16 v[148:151], a[44:47], a[0:3], v[148:151]
  ds_read_b64_tr_b16 a[96:97], v43 offset:16384
  v_mfma_f32_16x16x32_bf16 v[152:155], a[48:51], a[0:3], v[152:155]
  s_mov_b32 s53, 1311744
  s_add_u32 m0, s52, 8192
  ds_read_b64_tr_b16 a[98:99], v43 offset:17408
  v_mfma_f32_16x16x32_bf16 v[156:159], a[52:55], a[0:3], v[156:159]
  ds_read_b64_tr_b16 a[100:101], v44 offset:16384
  buffer_load_dwordx4 v11, s[28:31], s53 offen lds
  v_mfma_f32_16x16x32_bf16 v[160:163], a[24:27], a[4:7], v[160:163]
  ds_read_b64_tr_b16 a[102:103], v44 offset:17408
  v_mfma_f32_16x16x32_bf16 v[164:167], a[28:31], a[4:7], v[164:167]
  ds_read_b64_tr_b16 a[104:105], v45 offset:16384
  v_mfma_f32_16x16x32_bf16 v[168:171], a[32:35], a[4:7], v[168:171]
  ds_read_b64_tr_b16 a[106:107], v45 offset:17408
  v_mfma_f32_16x16x32_bf16 v[172:175], a[36:39], a[4:7], v[172:175]
  ds_read_b64_tr_b16 a[108:109], v46 offset:16384
  v_mfma_f32_16x16x32_bf16 v[176:179], a[40:43], a[4:7], v[176:179]
  ds_read_b64_tr_b16 a[110:111], v46 offset:17408
  ds_read_b64_tr_b16 a[112:113], v47 offset:16384
  buffer_load_dwordx4 v12, s[28:31], s53 offen offset:1024 lds
  v_mfma_f32_16x16x32_bf16 v[180:183], a[44:47], a[4:7], v[180:183]
  ds_read_b64_tr_b16 a[114:115], v47 offset:17408
  v_mfma_f32_16x16x32_bf16 v[184:187], a[48:51], a[4:7], v[184:187]
  ds_read_b64_tr_b16 a[116:117], v48 offset:16384
  s_sleep 4
  v_mfma_f32_16x16x32_bf16 v[188:191], a[52:55], a[4:7], v[188:191]
  ds_read_b64_tr_b16 a[118:119], v48 offset:17408
  v_mfma_f32_16x16x32_bf16 v[192:195], a[56:59], a[0:3], v[192:195]
  s_mov_b32 s78, 640
  ds_read_b128 a[16:19], v38 offset:2048
  v_mfma_f32_16x16x32_bf16 v[196:199], a[60:63], a[0:3], v[196:199]
  ds_read_b64_tr_b16 a[128:129], v43 offset:24576
  buffer_load_dword v25, v26, s[72:75], s78 offen
  v_mfma_f32_16x16x32_bf16 v[200:203], a[64:67], a[0:3], v[200:203]
  ds_read_b64_tr_b16 a[130:131], v43 offset:25600
  v_mfma_f32_16x16x32_bf16 v[204:207], a[68:71], a[0:3], v[204:207]
  ds_read_b64_tr_b16 a[132:133], v44 offset:24576
  v_mfma_f32_16x16x32_bf16 v[208:211], a[72:75], a[0:3], v[208:211]
  ds_read_b64_tr_b16 a[134:135], v44 offset:25600
  v_mfma_f32_16x16x32_bf16 v[212:215], a[76:79], a[0:3], v[212:215]
  s_mov_b32 s79, 2621440
  ds_read_b64_tr_b16 a[136:137], v45 offset:24576
  v_mfma_f32_16x16x32_bf16 v[216:219], a[80:83], a[0:3], v[216:219]
  ds_read_b64_tr_b16 a[138:139], v45 offset:25600
  buffer_load_dword v25, v24, s[68:71], s79 offen
  v_mfma_f32_16x16x32_bf16 v[220:223], a[84:87], a[0:3], v[220:223]
  ds_read_b64_tr_b16 a[140:141], v46 offset:24576
  v_mfma_f32_16x16x32_bf16 v[224:227], a[56:59], a[4:7], v[224:227]
  ds_read_b64_tr_b16 a[142:143], v46 offset:25600
  v_mfma_f32_16x16x32_bf16 v[228:231], a[60:63], a[4:7], v[228:231]
  ds_read_b64_tr_b16 a[144:145], v47 offset:24576
  v_mfma_f32_16x16x32_bf16 v[232:235], a[64:67], a[4:7], v[232:235]
  ds_read_b64_tr_b16 a[146:147], v47 offset:25600
  v_mfma_f32_16x16x32_bf16 v[236:239], a[68:71], a[4:7], v[236:239]
  ds_read_b64_tr_b16 a[148:149], v48 offset:24576
  s_and_b32 s84, s84, 3
  s_lshl_b32 s84, s84, 14
  s_add_u32 s96, s36, 0
  s_and_b32 s96, s96, 3
  v_mfma_f32_16x16x32_bf16 v[240:243], a[72:75], a[4:7], v[240:243]
  ds_read_b64_tr_b16 a[150:151], v48 offset:25600
  s_lshl_b32 s96, s96, 9
  s_add_u32 s85, s36, 2
  s_and_b32 s85, s85, 3
  s_lshl_b32 s85, s85, 14
  v_mfma_f32_16x16x32_bf16 v[244:247], a[76:79], a[4:7], v[244:247]
  s_add_u32 s97, s36, 1
  s_and_b32 s97, s97, 3
  s_lshl_b32 s97, s97, 9
  s_add_u32 s86, s36, 3
  v_mfma_f32_16x16x32_bf16 v[248:251], a[80:83], a[4:7], v[248:251]
  s_and_b32 s86, s86, 3
  s_lshl_b32 s86, s86, 14
  s_add_u32 s98, s36, 2
  s_and_b32 s98, s98, 3
  v_mfma_f32_16x16x32_bf16 v[252:255], a[84:87], a[4:7], v[252:255]
  s_lshl_b32 s98, s98, 9
  s_add_u32 s87, s36, 4
  s_and_b32 s87, s87, 3
  s_lshl_b32 s87, s87, 14
  s_waitcnt vmcnt(6) lgkmcnt(0)
  s_barrier
  v_mfma_f32_16x16x32_bf16 v[112:115], a[88:91], a[20:23], v[112:115]
  s_mov_b32 s54, 384
  s_add_u32 m0, s55, 133120
  ds_read_b128 a[8:11], v15
  ds_read_b64_tr_b16 a[24:25], v41 offset:32768
  v_mfma_f32_16x16x32_bf16 v[116:119], a[92:95], a[20:23], v[116:119]
  ds_read_b64_tr_b16 a[26:27], v41 offset:33792
  buffer_load_dwordx4 v32, s[24:27], s54 offen lds
  v_mfma_f32_16x16x32_bf16 v[120:123], a[120:123], a[20:23], v[120:123]
  ds_read_b64_tr_b16 a[28:29], v42 offset:32768
  v_mfma_f32_16x16x32_bf16 v[124:127], a[124:127], a[20:23], v[124:127]
  ds_read_b64_tr_b16 a[30:31], v42 offset:33792
  v_mfma_f32_16x16x32_bf16 v[128:131], a[88:91], a[12:15], v[128:131]
  ds_read_b64_tr_b16 a[56:57], v41 offset:40960
  buffer_load_dwordx4 v33, s[24:27], s54 offen offset:1024 lds
  v_mfma_f32_16x16x32_bf16 v[132:135], a[92:95], a[12:15], v[132:135]
  ds_read_b64_tr_b16 a[58:59], v41 offset:41984
  v_mfma_f32_16x16x32_bf16 v[136:139], a[96:99], a[12:15], v[136:139]
  ds_read_b64_tr_b16 a[60:61], v42 offset:40960
  v_mfma_f32_16x16x32_bf16 v[140:143], a[100:103], a[12:15], v[140:143]
  ds_read_b64_tr_b16 a[62:63], v42 offset:41984
  v_mfma_f32_16x16x32_bf16 v[144:147], a[104:107], a[12:15], v[144:147]
  ds_read_b128 a[0:3], v13
  buffer_load_dwordx4 v34, s[24:27], s54 offen offset:2048 lds
  v_mfma_f32_16x16x32_bf16 v[148:151], a[108:111], a[12:15], v[148:151]
  ds_read_b64_tr_b16 a[32:33], v43 offset:32768
  v_mfma_f32_16x16x32_bf16 v[152:155], a[112:115], a[12:15], v[152:155]
  ds_read_b64_tr_b16 a[34:35], v43 offset:33792
  v_mfma_f32_16x16x32_bf16 v[156:159], a[116:119], a[12:15], v[156:159]
  ds_read_b64_tr_b16 a[36:37], v44 offset:32768
  v_mfma_f32_16x16x32_bf16 v[160:163], a[88:91], a[16:19], v[160:163]
  ds_read_b64_tr_b16 a[38:39], v44 offset:33792
  buffer_load_dwordx4 v35, s[24:27], s54 offen offset:3072 lds
  v_mfma_f32_16x16x32_bf16 v[164:167], a[92:95], a[16:19], v[164:167]
  ds_read_b64_tr_b16 a[40:41], v45 offset:32768
  v_mfma_f32_16x16x32_bf16 v[168:171], a[96:99], a[16:19], v[168:171]
  ds_read_b64_tr_b16 a[42:43], v45 offset:33792
  v_mfma_f32_16x16x32_bf16 v[172:175], a[100:103], a[16:19], v[172:175]
  s_add_u32 m0, s55, 137216
  ds_read_b64_tr_b16 a[44:45], v46 offset:32768
  v_mfma_f32_16x16x32_bf16 v[176:179], a[104:107], a[16:19], v[176:179]
  ds_read_b64_tr_b16 a[46:47], v46 offset:33792
  ds_read_b64_tr_b16 a[48:49], v47 offset:32768
  buffer_load_dwordx4 v36, s[24:27], s54 offen lds
  v_mfma_f32_16x16x32_bf16 v[180:183], a[108:111], a[16:19], v[180:183]
  ds_read_b64_tr_b16 a[50:51], v47 offset:33792
  v_mfma_f32_16x16x32_bf16 v[184:187], a[112:115], a[16:19], v[184:187]
  s_mov_b32 s49, 1572864
  s_add_u32 m0, s52, 16384
  ds_read_b64_tr_b16 a[52:53], v48 offset:32768
  s_sleep 4
  v_mfma_f32_16x16x32_bf16 v[188:191], a[116:119], a[16:19], v[188:191]
  ds_read_b64_tr_b16 a[54:55], v48 offset:33792
  buffer_load_dwordx4 v11, s[28:31], s49 offen lds
  v_mfma_f32_16x16x32_bf16 v[192:195], a[120:123], a[12:15], v[192:195]
  ds_read_b128 a[4:7], v13 offset:2048
  v_mfma_f32_16x16x32_bf16 v[196:199], a[124:127], a[12:15], v[196:199]
  ds_read_b64_tr_b16 a[64:65], v43 offset:40960
  v_mfma_f32_16x16x32_bf16 v[200:203], a[128:131], a[12:15], v[200:203]
  ds_read_b64_tr_b16 a[66:67], v43 offset:41984
  v_mfma_f32_16x16x32_bf16 v[204:207], a[132:135], a[12:15], v[204:207]
  ds_read_b64_tr_b16 a[68:69], v44 offset:40960
  buffer_load_dwordx4 v12, s[28:31], s49 offen offset:1024 lds
  v_mfma_f32_16x16x32_bf16 v[208:211], a[136:139], a[12:15], v[208:211]
  ds_read_b64_tr_b16 a[70:71], v44 offset:41984
  v_mfma_f32_16x16x32_bf16 v[212:215], a[140:143], a[12:15], v[212:215]
  ds_read_b64_tr_b16 a[72:73], v45 offset:40960
  v_mfma_f32_16x16x32_bf16 v[216:219], a[144:147], a[12:15], v[216:219]
  s_mov_b32 s53, 1573888
  s_add_u32 m0, s52, 24576
  ds_read_b64_tr_b16 a[74:75], v45 offset:41984
  v_mfma_f32_16x16x32_bf16 v[220:223], a[148:151], a[12:15], v[220:223]
  ds_read_b64_tr_b16 a[76:77], v46 offset:40960
  buffer_load_dwordx4 v11, s[28:31], s53 offen lds
  v_mfma_f32_16x16x32_bf16 v[224:227], a[120:123], a[16:19], v[224:227]
  ds_read_b64_tr_b16 a[78:79], v46 offset:41984
  v_mfma_f32_16x16x32_bf16 v[228:231], a[124:127], a[16:19], v[228:231]
  ds_read_b64_tr_b16 a[80:81], v47 offset:40960
  v_mfma_f32_16x16x32_bf16 v[232:235], a[128:131], a[16:19], v[232:235]
  ds_read_b64_tr_b16 a[82:83], v47 offset:41984
  v_mfma_f32_16x16x32_bf16 v[236:239], a[132:135], a[16:19], v[236:239]
  ds_read_b64_tr_b16 a[84:85], v48 offset:40960
  s_add_u32 s99, s36, 3
  s_and_b32 s99, s99, 3
  s_lshl_b32 s99, s99, 9
  s_lshr_b32 s62, %[gid], 3
  buffer_load_dwordx4 v12, s[28:31], s53 offen offset:1024 lds
  v_mfma_f32_16x16x32_bf16 v[240:243], a[136:139], a[16:19], v[240:243]
  ds_read_b64_tr_b16 a[86:87], v48 offset:41984
  s_lshr_b32 s50, %[gid], 6
  s_add_u32 s62, s62, s50
  s_and_b32 s62, s62, 7
  s_mov_b32 s39, -1
  v_mfma_f32_16x16x32_bf16 v[244:247], a[140:143], a[16:19], v[244:247]
  s_xor_b32 s37, s37, 0x10000
  s_add_u32 s39, s39, 1
  s_lshr_b32 s50, s39, 3
  s_lshl_b32 s50, s50, 20
  v_mfma_f32_16x16x32_bf16 v[248:251], a[144:147], a[16:19], v[248:251]
  s_add_u32 s51, s39, s62
  s_and_b32 s51, s51, 7
  s_lshl_b32 s51, s51, 15
  s_add_u32 s40, s50, s51
  v_mfma_f32_16x16x32_bf16 v[252:255], a[148:151], a[16:19], v[252:255]
  s_add_u32 s41, s40, 8192
  s_add_u32 s42, s40, 16384
  s_add_u32 s43, s40, 24576
  s_xor_b32 s37, s37, 0x10000
  s_waitcnt vmcnt(11) lgkmcnt(0)
  s_barrier
  v_mfma_f32_16x16x32_bf16 v[112:115], a[24:27], a[8:11], v[112:115]
  s_mov_b32 s49, 1835008
  s_add_u32 m0, s52, 32768
  ds_read_b128 a[20:23], v16
  ds_read_b64_tr_b16 a[88:89], v41
  v_mfma_f32_16x16x32_bf16 v[116:119], a[28:31], a[8:11], v[116:119]
  ds_read_b64_tr_b16 a[90:91], v41 offset:1024
  buffer_load_dwordx4 v11, s[28:31], s49 offen lds
  v_mfma_f32_16x16x32_bf16 v[120:123], a[56:59], a[8:11], v[120:123]
  ds_read_b64_tr_b16 a[92:93], v42
  v_mfma_f32_16x16x32_bf16 v[124:127], a[60:63], a[8:11], v[124:127]
  ds_read_b64_tr_b16 a[94:95], v42 offset:1024
  v_mfma_f32_16x16x32_bf16 v[128:131], a[24:27], a[0:3], v[128:131]
  ds_read_b64_tr_b16 a[120:121], v41 offset:8192
  v_mfma_f32_16x16x32_bf16 v[132:135], a[28:31], a[0:3], v[132:135]
  ds_read_b64_tr_b16 a[122:123], v41 offset:9216
  buffer_load_dwordx4 v12, s[28:31], s49 offen offset:1024 lds
  v_mfma_f32_16x16x32_bf16 v[136:139], a[32:35], a[0:3], v[136:139]
  ds_read_b64_tr_b16 a[124:125], v42 offset:8192
  v_mfma_f32_16x16x32_bf16 v[140:143], a[36:39], a[0:3], v[140:143]
  ds_read_b64_tr_b16 a[126:127], v42 offset:9216
  v_mfma_f32_16x16x32_bf16 v[144:147], a[40:43], a[0:3], v[144:147]
  s_mov_b32 s53, 1836032
  s_add_u32 m0, s52, 40960
  ds_read_b128 a[12:15], v14
  v_mfma_f32_16x16x32_bf16 v[148:151], a[44:47], a[0:3], v[148:151]
  ds_read_b64_tr_b16 a[96:97], v43
  buffer_load_dwordx4 v11, s[28:31], s53 offen lds
  v_mfma_f32_16x16x32_bf16 v[152:155], a[48:51], a[0:3], v[152:155]
  ds_read_b64_tr_b16 a[98:99], v43 offset:1024
  v_mfma_f32_16x16x32_bf16 v[156:159], a[52:55], a[0:3], v[156:159]
  ds_read_b64_tr_b16 a[100:101], v44
  v_mfma_f32_16x16x32_bf16 v[160:163], a[24:27], a[4:7], v[160:163]
  ds_read_b64_tr_b16 a[102:103], v44 offset:1024
  v_mfma_f32_16x16x32_bf16 v[164:167], a[28:31], a[4:7], v[164:167]
  ds_read_b64_tr_b16 a[104:105], v45
  buffer_load_dwordx4 v12, s[28:31], s53 offen offset:1024 lds
  v_mfma_f32_16x16x32_bf16 v[168:171], a[32:35], a[4:7], v[168:171]
  ds_read_b64_tr_b16 a[106:107], v45 offset:1024
  v_mfma_f32_16x16x32_bf16 v[172:175], a[36:39], a[4:7], v[172:175]
  ds_read_b64_tr_b16 a[108:109], v46
  v_mfma_f32_16x16x32_bf16 v[176:179], a[40:43], a[4:7], v[176:179]
  ds_read_b64_tr_b16 a[110:111], v46 offset:1024
  ds_read_b64_tr_b16 a[112:113], v47
  v_mfma_f32_16x16x32_bf16 v[180:183], a[44:47], a[4:7], v[180:183]
  s_mov_b32 s78, 768
  ds_read_b64_tr_b16 a[114:115], v47 offset:1024
  v_mfma_f32_16x16x32_bf16 v[184:187], a[48:51], a[4:7], v[184:187]
  ds_read_b64_tr_b16 a[116:117], v48
  buffer_load_dword v25, v26, s[72:75], s78 offen
  s_sleep 4
  v_mfma_f32_16x16x32_bf16 v[188:191], a[52:55], a[4:7], v[188:191]
  ds_read_b64_tr_b16 a[118:119], v48 offset:1024
  v_mfma_f32_16x16x32_bf16 v[192:195], a[56:59], a[0:3], v[192:195]
  ds_read_b128 a[16:19], v14 offset:2048
  v_mfma_f32_16x16x32_bf16 v[196:199], a[60:63], a[0:3], v[196:199]
  s_mov_b32 s79, 3145728
  ds_read_b64_tr_b16 a[128:129], v43 offset:8192
  v_mfma_f32_16x16x32_bf16 v[200:203], a[64:67], a[0:3], v[200:203]
  ds_read_b64_tr_b16 a[130:131], v43 offset:9216
  buffer_load_dword v25, v24, s[68:71], s79 offen
  v_mfma_f32_16x16x32_bf16 v[204:207], a[68:71], a[0:3], v[204:207]
  ds_read_b64_tr_b16 a[132:133], v44 offset:8192
  v_mfma_f32_16x16x32_bf16 v[208:211], a[72:75], a[0:3], v[208:211]
  ds_read_b64_tr_b16 a[134:135], v44 offset:9216
  v_mfma_f32_16x16x32_bf16 v[212:215], a[76:79], a[0:3], v[212:215]
  s_mov_b32 m0, s37
  ds_read_b64_tr_b16 a[136:137], v45 offset:8192
  v_mfma_f32_16x16x32_bf16 v[216:219], a[80:83], a[0:3], v[216:219]
  ds_read_b64_tr_b16 a[138:139], v45 offset:9216
  buffer_load_dwordx4 v1, s[16:19], s40 offen nt lds
  v_mfma_f32_16x16x32_bf16 v[220:223], a[84:87], a[0:3], v[220:223]
  ds_read_b64_tr_b16 a[140:141], v46 offset:8192
  v_mfma_f32_16x16x32_bf16 v[224:227], a[56:59], a[4:7], v[224:227]
  ds_read_b64_tr_b16 a[142:143], v46 offset:9216
  v_mfma_f32_16x16x32_bf16 v[228:231], a[60:63], a[4:7], v[228:231]
  ds_read_b64_tr_b16 a[144:145], v47 offset:8192
  v_mfma_f32_16x16x32_bf16 v[232:235], a[64:67], a[4:7], v[232:235]
  ds_read_b64_tr_b16 a[146:147], v47 offset:9216
  buffer_load_dwordx4 v2, s[16:19], s40 offen offset:1024 nt lds
  v_mfma_f32_16x16x32_bf16 v[236:239], a[68:71], a[4:7], v[236:239]
  ds_read_b64_tr_b16 a[148:149], v48 offset:8192
  v_mfma_f32_16x16x32_bf16 v[240:243], a[72:75], a[4:7], v[240:243]
  ds_read_b64_tr_b16 a[150:151], v48 offset:9216
  v_mfma_f32_16x16x32_bf16 v[244:247], a[76:79], a[4:7], v[244:247]
  v_mfma_f32_16x16x32_bf16 v[248:251], a[80:83], a[4:7], v[248:251]
  v_mfma_f32_16x16x32_bf16 v[252:255], a[84:87], a[4:7], v[252:255]
  s_waitcnt vmcnt(8) lgkmcnt(0)
  s_barrier
  v_mfma_f32_16x16x32_bf16 v[112:115], a[88:91], a[20:23], v[112:115]
  s_mov_b32 s54, 512
  s_add_u32 m0, s55, 65536
  ds_read_b128 a[8:11], v39
  ds_read_b64_tr_b16 a[24:25], v41 offset:16384
  v_mfma_f32_16x16x32_bf16 v[116:119], a[92:95], a[20:23], v[116:119]
  ds_read_b64_tr_b16 a[26:27], v41 offset:17408
  buffer_load_dwordx4 v32, s[24:27], s54 offen lds
  v_mfma_f32_16x16x32_bf16 v[120:123], a[120:123], a[20:23], v[120:123]
  ds_read_b64_tr_b16 a[28:29], v42 offset:16384
  v_mfma_f32_16x16x32_bf16 v[124:127], a[124:127], a[20:23], v[124:127]
  ds_read_b64_tr_b16 a[30:31], v42 offset:17408
  v_mfma_f32_16x16x32_bf16 v[128:131], a[88:91], a[12:15], v[128:131]
  ds_read_b64_tr_b16 a[56:57], v41 offset:24576
  buffer_load_dwordx4 v33, s[24:27], s54 offen offset:1024 lds
  v_mfma_f32_16x16x32_bf16 v[132:135], a[92:95], a[12:15], v[132:135]
  ds_read_b64_tr_b16 a[58:59], v41 offset:25600
  v_mfma_f32_16x16x32_bf16 v[136:139], a[96:99], a[12:15], v[136:139]
  ds_read_b64_tr_b16 a[60:61], v42 offset:24576
  v_mfma_f32_16x16x32_bf16 v[140:143], a[100:103], a[12:15], v[140:143]
  ds_read_b64_tr_b16 a[62:63], v42 offset:25600
  v_mfma_f32_16x16x32_bf16 v[144:147], a[104:107], a[12:15], v[144:147]
  ds_read_b128 a[0:3], v37
  buffer_load_dwordx4 v34, s[24:27], s54 offen offset:2048 lds
  v_mfma_f32_16x16x32_bf16 v[148:151], a[108:111], a[12:15], v[148:151]
  ds_read_b64_tr_b16 a[32:33], v43 offset:16384
  v_mfma_f32_16x16x32_bf16 v[152:155], a[112:115], a[12:15], v[152:155]
  ds_read_b64_tr_b16 a[34:35], v43 offset:17408
  v_mfma_f32_16x16x32_bf16 v[156:159], a[116:119], a[12:15], v[156:159]
  ds_read_b64_tr_b16 a[36:37], v44 offset:16384
  v_mfma_f32_16x16x32_bf16 v[160:163], a[88:91], a[16:19], v[160:163]
  ds_read_b64_tr_b16 a[38:39], v44 offset:17408
  buffer_load_dwordx4 v35, s[24:27], s54 offen offset:3072 lds
  v_mfma_f32_16x16x32_bf16 v[164:167], a[92:95], a[16:19], v[164:167]
  ds_read_b64_tr_b16 a[40:41], v45 offset:16384
  v_mfma_f32_16x16x32_bf16 v[168:171], a[96:99], a[16:19], v[168:171]
  ds_read_b64_tr_b16 a[42:43], v45 offset:17408
  v_mfma_f32_16x16x32_bf16 v[172:175], a[100:103], a[16:19], v[172:175]
  s_add_u32 m0, s55, 69632
  ds_read_b64_tr_b16 a[44:45], v46 offset:16384
  v_mfma_f32_16x16x32_bf16 v[176:179], a[104:107], a[16:19], v[176:179]
  ds_read_b64_tr_b16 a[46:47], v46 offset:17408
  ds_read_b64_tr_b16 a[48:49], v47 offset:16384
  buffer_load_dwordx4 v36, s[24:27], s54 offen lds
  v_mfma_f32_16x16x32_bf16 v[180:183], a[108:111], a[16:19], v[180:183]
  ds_read_b64_tr_b16 a[50:51], v47 offset:17408
  v_mfma_f32_16x16x32_bf16 v[184:187], a[112:115], a[16:19], v[184:187]
  s_mov_b32 s49, 2097152
  s_mov_b32 m0, s52
  ds_read_b64_tr_b16 a[52:53], v48 offset:16384
  s_sleep 4
  v_mfma_f32_16x16x32_bf16 v[188:191], a[116:119], a[16:19], v[188:191]
  ds_read_b64_tr_b16 a[54:55], v48 offset:17408
  buffer_load_dwordx4 v11, s[28:31], s49 offen lds
  v_mfma_f32_16x16x32_bf16 v[192:195], a[120:123], a[12:15], v[192:195]
  ds_read_b128 a[4:7], v37 offset:2048
  v_mfma_f32_16x16x32_bf16 v[196:199], a[124:127], a[12:15], v[196:199]
  ds_read_b64_tr_b16 a[64:65], v43 offset:24576
  v_mfma_f32_16x16x32_bf16 v[200:203], a[128:131], a[12:15], v[200:203]
  ds_read_b64_tr_b16 a[66:67], v43 offset:25600
  v_mfma_f32_16x16x32_bf16 v[204:207], a[132:135], a[12:15], v[204:207]
  ds_read_b64_tr_b16 a[68:69], v44 offset:24576
  buffer_load_dwordx4 v12, s[28:31], s49 offen offset:1024 lds
  v_mfma_f32_16x16x32_bf16 v[208:211], a[136:139], a[12:15], v[208:211]
  ds_read_b64_tr_b16 a[70:71], v44 offset:25600
  v_mfma_f32_16x16x32_bf16 v[212:215], a[140:143], a[12:15], v[212:215]
  ds_read_b64_tr_b16 a[72:73], v45 offset:24576
  v_mfma_f32_16x16x32_bf16 v[216:219], a[144:147], a[12:15], v[216:219]
  s_mov_b32 s53, 2098176
  s_add_u32 m0, s52, 8192
  ds_read_b64_tr_b16 a[74:75], v45 offset:25600
  v_mfma_f32_16x16x32_bf16 v[220:223], a[148:151], a[12:15], v[220:223]
  ds_read_b64_tr_b16 a[76:77], v46 offset:24576
  buffer_load_dwordx4 v11, s[28:31], s53 offen lds
  v_mfma_f32_16x16x32_bf16 v[224:227], a[120:123], a[16:19], v[224:227]
  ds_read_b64_tr_b16 a[78:79], v46 offset:25600
  v_mfma_f32_16x16x32_bf16 v[228:231], a[124:127], a[16:19], v[228:231]
  ds_read_b64_tr_b16 a[80:81], v47 offset:24576
  v_mfma_f32_16x16x32_bf16 v[232:235], a[128:131], a[16:19], v[232:235]
  ds_read_b64_tr_b16 a[82:83], v47 offset:25600
  v_mfma_f32_16x16x32_bf16 v[236:239], a[132:135], a[16:19], v[236:239]
  ds_read_b64_tr_b16 a[84:85], v48 offset:24576
  buffer_load_dwordx4 v12, s[28:31], s53 offen offset:1024 lds
  v_mfma_f32_16x16x32_bf16 v[240:243], a[136:139], a[16:19], v[240:243]
  ds_read_b64_tr_b16 a[86:87], v48 offset:25600
  v_mfma_f32_16x16x32_bf16 v[244:247], a[140:143], a[16:19], v[244:247]
  v_mfma_f32_16x16x32_bf16 v[248:251], a[144:147], a[16:19], v[248:251]
  v_mfma_f32_16x16x32_bf16 v[252:255], a[148:151], a[16:19], v[252:255]
  s_waitcnt vmcnt(13) lgkmcnt(0)
  s_barrier
  v_mfma_f32_16x16x32_bf16 v[112:115], a[24:27], a[8:11], v[112:115]
  s_mov_b32 s49, 2359296
  s_add_u32 m0, s52, 16384
  ds_read_b128 a[20:23], v40
  ds_read_b64_tr_b16 a[88:89], v41 offset:32768
  v_mfma_f32_16x16x32_bf16 v[116:119], a[28:31], a[8:11], v[116:119]
  ds_read_b64_tr_b16 a[90:91], v41 offset:33792
  buffer_load_dwordx4 v11, s[28:31], s49 offen lds
  v_mfma_f32_16x16x32_bf16 v[120:123], a[56:59], a[8:11], v[120:123]
  ds_read_b64_tr_b16 a[92:93], v42 offset:32768
  v_mfma_f32_16x16x32_bf16 v[124:127], a[60:63], a[8:11], v[124:127]
  ds_read_b64_tr_b16 a[94:95], v42 offset:33792
  v_mfma_f32_16x16x32_bf16 v[128:131], a[24:27], a[0:3], v[128:131]
  ds_read_b64_tr_b16 a[120:121], v41 offset:40960
  buffer_load_dwordx4 v12, s[28:31], s49 offen offset:1024 lds
  v_mfma_f32_16x16x32_bf16 v[132:135], a[28:31], a[0:3], v[132:135]
  ds_read_b64_tr_b16 a[122:123], v41 offset:41984
  v_mfma_f32_16x16x32_bf16 v[136:139], a[32:35], a[0:3], v[136:139]
  ds_read_b64_tr_b16 a[124:125], v42 offset:40960
  v_mfma_f32_16x16x32_bf16 v[140:143], a[36:39], a[0:3], v[140:143]
  s_mov_b32 s53, 2360320
  s_add_u32 m0, s52, 24576
  ds_read_b64_tr_b16 a[126:127], v42 offset:41984
  v_mfma_f32_16x16x32_bf16 v[144:147], a[40:43], a[0:3], v[144:147]
  ds_read_b128 a[12:15], v38
  buffer_load_dwordx4 v11, s[28:31], s53 offen lds
  v_mfma_f32_16x16x32_bf16 v[148:151], a[44:47], a[0:3], v[148:151]
  ds_read_b64_tr_b16 a[96:97], v43 offset:32768
  v_mfma_f32_16x16x32_bf16 v[152:155], a[48:51], a[0:3], v[152:155]
  ds_read_b64_tr_b16 a[98:99], v43 offset:33792
  v_mfma_f32_16x16x32_bf16 v[156:159], a[52:55], a[0:3], v[156:159]
  ds_read_b64_tr_b16 a[100:101], v44 offset:32768
  v_mfma_f32_16x16x32_bf16 v[160:163], a[24:27], a[4:7], v[160:163]
  ds_read_b64_tr_b16 a[102:103], v44 offset:33792
  buffer_load_dwordx4 v12, s[28:31], s53 offen offset:1024 lds
  v_mfma_f32_16x16x32_bf16 v[164:167], a[28:31], a[4:7], v[164:167]
  ds_read_b64_tr_b16 a[104:105], v45 offset:32768
  v_mfma_f32_16x16x32_bf16 v[168:171], a[32:35], a[4:7], v[168:171]
  ds_read_b64_tr_b16 a[106:107], v45 offset:33792
  v_mfma_f32_16x16x32_bf16 v[172:175], a[36:39], a[4:7], v[172:175]
  s_mov_b32 s78, 896
  ds_read_b64_tr_b16 a[108:109], v46 offset:32768
  v_mfma_f32_16x16x32_bf16 v[176:179], a[40:43], a[4:7], v[176:179]
  ds_read_b64_tr_b16 a[110:111], v46 offset:33792
  ds_read_b64_tr_b16 a[112:113], v47 offset:32768
  buffer_load_dword v25, v26, s[72:75], s78 offen
  v_mfma_f32_16x16x32_bf16 v[180:183], a[44:47], a[4:7], v[180:183]
  ds_read_b64_tr_b16 a[114:115], v47 offset:33792
  v_mfma_f32_16x16x32_bf16 v[184:187], a[48:51], a[4:7], v[184:187]
  s_mov_b32 s79, 3670016
  ds_read_b64_tr_b16 a[116:117], v48 offset:32768
  s_sleep 4
  v_mfma_f32_16x16x32_bf16 v[188:191], a[52:55], a[4:7], v[188:191]
  ds_read_b64_tr_b16 a[118:119], v48 offset:33792
  buffer_load_dword v25, v24, s[68:71], s79 offen
  v_mfma_f32_16x16x32_bf16 v[192:195], a[56:59], a[0:3], v[192:195]
  ds_read_b128 a[16:19], v38 offset:2048
  v_mfma_f32_16x16x32_bf16 v[196:199], a[60:63], a[0:3], v[196:199]
  ds_read_b64_tr_b16 a[128:129], v43 offset:40960
  v_mfma_f32_16x16x32_bf16 v[200:203], a[64:67], a[0:3], v[200:203]
  s_add_u32 m0, s37, 8192
  ds_read_b64_tr_b16 a[130:131], v43 offset:41984
  v_mfma_f32_16x16x32_bf16 v[204:207], a[68:71], a[0:3], v[204:207]
  ds_read_b64_tr_b16 a[132:133], v44 offset:40960
  buffer_load_dwordx4 v3, s[20:23], s40 offen nt lds
  v_mfma_f32_16x16x32_bf16 v[208:211], a[72:75], a[0:3], v[208:211]
  ds_read_b64_tr_b16 a[134:135], v44 offset:41984
  v_mfma_f32_16x16x32_bf16 v[212:215], a[76:79], a[0:3], v[212:215]
  ds_read_b64_tr_b16 a[136:137], v45 offset:40960
  v_mfma_f32_16x16x32_bf16 v[216:219], a[80:83], a[0:3], v[216:219]
  ds_read_b64_tr_b16 a[138:139], v45 offset:41984
  v_mfma_f32_16x16x32_bf16 v[220:223], a[84:87], a[0:3], v[220:223]
  ds_read_b64_tr_b16 a[140:141], v46 offset:40960
  buffer_load_dwordx4 v4, s[20:23], s40 offen offset:1024 nt lds
  v_mfma_f32_16x16x32_bf16 v[224:227], a[56:59], a[4:7], v[224:227]
  ds_read_b64_tr_b16 a[142:143], v46 offset:41984
  v_mfma_f32_16x16x32_bf16 v[228:231], a[60:63], a[4:7], v[228:231]
  ds_read_b64_tr_b16 a[144:145], v47 offset:40960
  v_mfma_f32_16x16x32_bf16 v[232:235], a[64:67], a[4:7], v[232:235]
  s_add_u32 m0, s37, 16384
  ds_read_b64_tr_b16 a[146:147], v47 offset:41984
  v_mfma_f32_16x16x32_bf16 v[236:239], a[68:71], a[4:7], v[236:239]
  ds_read_b64_tr_b16 a[148:149], v48 offset:40960
  buffer_load_dwordx4 v1, s[16:19], s41 offen nt lds
  v_mfma_f32_16x16x32_bf16 v[240:243], a[72:75], a[4:7], v[240:243]
  ds_read_b64_tr_b16 a[150:151], v48 offset:41984
  v_mfma_f32_16x16x32_bf16 v[244:247], a[76:79], a[4:7], v[244:247]
  v_mfma_f32_16x16x32_bf16 v[248:251], a[80:83], a[4:7], v[248:251]
  v_mfma_f32_16x16x32_bf16 v[252:255], a[84:87], a[4:7], v[252:255]
  s_waitcnt vmcnt(9) lgkmcnt(0)
  s_barrier
  v_mfma_f32_16x16x32_bf16 v[112:115], a[88:91], a[20:23], v[112:115]
  s_mov_b32 s54, 640
  s_add_u32 m0, s55, 133120
  ds_read_b128 a[8:11], v15
  ds_read_b64_tr_b16 a[24:25], v41
  v_mfma_f32_16x16x32_bf16 v[116:119], a[92:95], a[20:23], v[116:119]
  ds_read_b64_tr_b16 a[26:27], v41 offset:1024
  buffer_load_dwordx4 v32, s[24:27], s54 offen lds
  v_mfma_f32_16x16x32_bf16 v[120:123], a[120:123], a[20:23], v[120:123]
  ds_read_b64_tr_b16 a[28:29], v42
  v_mfma_f32_16x16x32_bf16 v[124:127], a[124:127], a[20:23], v[124:127]
  ds_read_b64_tr_b16 a[30:31], v42 offset:1024
  v_mfma_f32_16x16x32_bf16 v[128:131], a[88:91], a[12:15], v[128:131]
  ds_read_b64_tr_b16 a[56:57], v41 offset:8192
  buffer_load_dwordx4 v33, s[24:27], s54 offen offset:1024 lds
  v_mfma_f32_16x16x32_bf16 v[132:135], a[92:95], a[12:15], v[132:135]
  ds_read_b64_tr_b16 a[58:59], v41 offset:9216
  v_mfma_f32_16x16x32_bf16 v[136:139], a[96:99], a[12:15], v[136:139]
  ds_read_b64_tr_b16 a[60:61], v42 offset:8192
  v_mfma_f32_16x16x32_bf16 v[140:143], a[100:103], a[12:15], v[140:143]
  ds_read_b64_tr_b16 a[62:63], v42 offset:9216
  v_mfma_f32_16x16x32_bf16 v[144:147], a[104:107], a[12:15], v[144:147]
  ds_read_b128 a[0:3], v13
  buffer_load_dwordx4 v34, s[24:27], s54 offen offset:2048 lds
  v_mfma_f32_16x16x32_bf16 v[148:151], a[108:111], a[12:15], v[148:151]
  ds_read_b64_tr_b16 a[32:33], v43
  v_mfma_f32_16x16x32_bf16 v[152:155], a[112:115], a[12:15], v[152:155]
  ds_read_b64_tr_b16 a[34:35], v43 offset:1024
  v_mfma_f32_16x16x32_bf16 v[156:159], a[116:119], a[12:15], v[156:159]
  ds_read_b64_tr_b16 a[36:37], v44
  v_mfma_f32_16x16x32_bf16 v[160:163], a[88:91], a[16:19], v[160:163]
  ds_read_b64_tr_b16 a[38:39], v44 offset:1024
  buffer_load_dwordx4 v35, s[24:27], s54 offen offset:3072 lds
  v_mfma_f32_16x16x32_bf16 v[164:167], a[92:95], a[16:19], v[164:167]
  ds_read_b64_tr_b16 a[40:41], v45
  v_mfma_f32_16x16x32_bf16 v[168:171], a[96:99], a[16:19], v[168:171]
  ds_read_b64_tr_b16 a[42:43], v45 offset:1024
  v_mfma_f32_16x16x32_bf16 v[172:175], a[100:103], a[16:19], v[172:175]
  s_add_u32 m0, s55, 137216
  ds_read_b64_tr_b16 a[44:45], v46
  v_mfma_f32_16x16x32_bf16 v[176:179], a[104:107], a[16:19], v[176:179]
  ds_read_b64_tr_b16 a[46:47], v46 offset:1024
  ds_read_b64_tr_b16 a[48:49], v47
  buffer_load_dwordx4 v36, s[24:27], s54 offen lds
  v_mfma_f32_16x16x32_bf16 v[180:183], a[108:111], a[16:19], v[180:183]
  ds_read_b64_tr_b16 a[50:51], v47 offset:1024
  v_mfma_f32_16x16x32_bf16 v[184:187], a[112:115], a[16:19], v[184:187]
  s_mov_b32 s49, 2621440
  s_add_u32 m0, s52, 32768
  ds_read_b64_tr_b16 a[52:53], v48
  s_sleep 4
  v_mfma_f32_16x16x32_bf16 v[188:191], a[116:119], a[16:19], v[188:191]
  ds_read_b64_tr_b16 a[54:55], v48 offset:1024
  buffer_load_dwordx4 v11, s[28:31], s49 offen lds
  v_mfma_f32_16x16x32_bf16 v[192:195], a[120:123], a[12:15], v[192:195]
  ds_read_b128 a[4:7], v13 offset:2048
  v_mfma_f32_16x16x32_bf16 v[196:199], a[124:127], a[12:15], v[196:199]
  ds_read_b64_tr_b16 a[64:65], v43 offset:8192
  v_mfma_f32_16x16x32_bf16 v[200:203], a[128:131], a[12:15], v[200:203]
  ds_read_b64_tr_b16 a[66:67], v43 offset:9216
  v_mfma_f32_16x16x32_bf16 v[204:207], a[132:135], a[12:15], v[204:207]
  ds_read_b64_tr_b16 a[68:69], v44 offset:8192
  buffer_load_dwordx4 v12, s[28:31], s49 offen offset:1024 lds
  v_mfma_f32_16x16x32_bf16 v[208:211], a[136:139], a[12:15], v[208:211]
  ds_read_b64_tr_b16 a[70:71], v44 offset:9216
  v_mfma_f32_16x16x32_bf16 v[212:215], a[140:143], a[12:15], v[212:215]
  ds_read_b64_tr_b16 a[72:73], v45 offset:8192
  v_mfma_f32_16x16x32_bf16 v[216:219], a[144:147], a[12:15], v[216:219]
  s_mov_b32 s53, 2622464
  s_add_u32 m0, s52, 40960
  ds_read_b64_tr_b16 a[74:75], v45 offset:9216
  v_mfma_f32_16x16x32_bf16 v[220:223], a[148:151], a[12:15], v[220:223]
  ds_read_b64_tr_b16 a[76:77], v46 offset:8192
  buffer_load_dwordx4 v11, s[28:31], s53 offen lds
  v_mfma_f32_16x16x32_bf16 v[224:227], a[120:123], a[16:19], v[224:227]
  ds_read_b64_tr_b16 a[78:79], v46 offset:9216
  v_mfma_f32_16x16x32_bf16 v[228:231], a[124:127], a[16:19], v[228:231]
  ds_read_b64_tr_b16 a[80:81], v47 offset:8192
  v_mfma_f32_16x16x32_bf16 v[232:235], a[128:131], a[16:19], v[232:235]
  ds_read_b64_tr_b16 a[82:83], v47 offset:9216
  v_mfma_f32_16x16x32_bf16 v[236:239], a[132:135], a[16:19], v[236:239]
  ds_read_b64_tr_b16 a[84:85], v48 offset:8192
  buffer_load_dwordx4 v12, s[28:31], s53 offen offset:1024 lds
  v_mfma_f32_16x16x32_bf16 v[240:243], a[136:139], a[16:19], v[240:243]
  ds_read_b64_tr_b16 a[86:87], v48 offset:9216
  v_mfma_f32_16x16x32_bf16 v[244:247], a[140:143], a[16:19], v[244:247]
  v_mfma_f32_16x16x32_bf16 v[248:251], a[144:147], a[16:19], v[248:251]
  v_mfma_f32_16x16x32_bf16 v[252:255], a[148:151], a[16:19], v[252:255]
  s_waitcnt vmcnt(14) lgkmcnt(0)
  s_barrier
  v_mfma_f32_16x16x32_bf16 v[112:115], a[24:27], a[8:11], v[112:115]
  s_mov_b32 s49, 2883584
  s_mov_b32 m0, s52
  ds_read_b128 a[20:23], v16
  ds_read_b64_tr_b16 a[88:89], v41 offset:16384
  v_mfma_f32_16x16x32_bf16 v[116:119], a[28:31], a[8:11], v[116:119]
  ds_read_b64_tr_b16 a[90:91], v41 offset:17408
  buffer_load_dwordx4 v11, s[28:31], s49 offen lds
  v_mfma_f32_16x16x32_bf16 v[120:123], a[56:59], a[8:11], v[120:123]
  ds_read_b64_tr_b16 a[92:93], v42 offset:16384
  v_mfma_f32_16x16x32_bf16 v[124:127], a[60:63], a[8:11], v[124:127]
  ds_read_b64_tr_b16 a[94:95], v42 offset:17408
  v_mfma_f32_16x16x32_bf16 v[128:131], a[24:27], a[0:3], v[128:131]
  ds_read_b64_tr_b16 a[120:121], v41 offset:24576
  buffer_load_dwordx4 v12, s[28:31], s49 offen offset:1024 lds
  v_mfma_f32_16x16x32_bf16 v[132:135], a[28:31], a[0:3], v[132:135]
  ds_read_b64_tr_b16 a[122:123], v41 offset:25600
  v_mfma_f32_16x16x32_bf16 v[136:139], a[32:35], a[0:3], v[136:139]
  ds_read_b64_tr_b16 a[124:125], v42 offset:24576
  v_mfma_f32_16x16x32_bf16 v[140:143], a[36:39], a[0:3], v[140:143]
  s_mov_b32 s53, 2884608
  s_add_u32 m0, s52, 8192
  ds_read_b64_tr_b16 a[126:127], v42 offset:25600
  v_mfma_f32_16x16x32_bf16 v[144:147], a[40:43], a[0:3], v[144:147]
  ds_read_b128 a[12:15], v14
  buffer_load_dwordx4 v11, s[28:31], s53 offen lds
  v_mfma_f32_16x16x32_bf16 v[148:151], a[44:47], a[0:3], v[148:151]
  ds_read_b64_tr_b16 a[96:97], v43 offset:16384
  v_mfma_f32_16x16x32_bf16 v[152:155], a[48:51], a[0:3], v[152:155]
  ds_read_b64_tr_b16 a[98:99], v43 offset:17408
  v_mfma_f32_16x16x32_bf16 v[156:159], a[52:55], a[0:3], v[156:159]
  ds_read_b64_tr_b16 a[100:101], v44 offset:16384
  v_mfma_f32_16x16x32_bf16 v[160:163], a[24:27], a[4:7], v[160:163]
  ds_read_b64_tr_b16 a[102:103], v44 offset:17408
  buffer_load_dwordx4 v12, s[28:31], s53 offen offset:1024 lds
  v_mfma_f32_16x16x32_bf16 v[164:167], a[28:31], a[4:7], v[164:167]
  ds_read_b64_tr_b16 a[104:105], v45 offset:16384
  v_mfma_f32_16x16x32_bf16 v[168:171], a[32:35], a[4:7], v[168:171]
  ds_read_b64_tr_b16 a[106:107], v45 offset:17408
  v_mfma_f32_16x16x32_bf16 v[172:175], a[36:39], a[4:7], v[172:175]
  s_mov_b32 s78, 1024
  ds_read_b64_tr_b16 a[108:109], v46 offset:16384
  v_mfma_f32_16x16x32_bf16 v[176:179], a[40:43], a[4:7], v[176:179]
  ds_read_b64_tr_b16 a[110:111], v46 offset:17408
  ds_read_b64_tr_b16 a[112:113], v47 offset:16384
  buffer_load_dword v25, v26, s[72:75], s78 offen
  v_mfma_f32_16x16x32_bf16 v[180:183], a[44:47], a[4:7], v[180:183]
  ds_read_b64_tr_b16 a[114:115], v47 offset:17408
  v_mfma_f32_16x16x32_bf16 v[184:187], a[48:51], a[4:7], v[184:187]
  s_mov_b32 s79, 4194304
  ds_read_b64_tr_b16 a[116:117], v48 offset:16384
  v_mfma_f32_16x16x32_bf16 v[188:191], a[52:55], a[4:7], v[188:191]
  ds_read_b64_tr_b16 a[118:119], v48 offset:17408
  buffer_load_dword v25, v24, s[68:71], s79 offen
  v_mfma_f32_16x16x32_bf16 v[192:195], a[56:59], a[0:3], v[192:195]
  ds_read_b128 a[16:19], v14 offset:2048
  v_mfma_f32_16x16x32_bf16 v[196:199], a[60:63], a[0:3], v[196:199]
  ds_read_b64_tr_b16 a[128:129], v43 offset:24576
  v_mfma_f32_16x16x32_bf16 v[200:203], a[64:67], a[0:3], v[200:203]
  s_add_u32 m0, s37, 16384
  ds_read_b64_tr_b16 a[130:131], v43 offset:25600
  v_mfma_f32_16x16x32_bf16 v[204:207], a[68:71], a[0:3], v[204:207]
  ds_read_b64_tr_b16 a[132:133], v44 offset:24576
  buffer_load_dwordx4 v2, s[16:19], s41 offen offset:1024 nt lds
  v_mfma_f32_16x16x32_bf16 v[208:211], a[72:75], a[0:3], v[208:211]
  ds_read_b64_tr_b16 a[134:135], v44 offset:25600
  v_mfma_f32_16x16x32_bf16 v[212:215], a[76:79], a[0:3], v[212:215]
  ds_read_b64_tr_b16 a[136:137], v45 offset:24576
  v_mfma_f32_16x16x32_bf16 v[216:219], a[80:83], a[0:3], v[216:219]
  s_add_u32 m0, s37, 24576
  ds_read_b64_tr_b16 a[138:139], v45 offset:25600
  v_mfma_f32_16x16x32_bf16 v[220:223], a[84:87], a[0:3], v[220:223]
  ds_read_b64_tr_b16 a[140:141], v46 offset:24576
  buffer_load_dwordx4 v3, s[20:23], s41 offen nt lds
  v_mfma_f32_16x16x32_bf16 v[224:227], a[56:59], a[4:7], v[224:227]
  ds_read_b64_tr_b16 a[142:143], v46 offset:25600
  v_mfma_f32_16x16x32_bf16 v[228:231], a[60:63], a[4:7], v[228:231]
  ds_read_b64_tr_b16 a[144:145], v47 offset:24576
  v_mfma_f32_16x16x32_bf16 v[232:235], a[64:67], a[4:7], v[232:235]
  ds_read_b64_tr_b16 a[146:147], v47 offset:25600
  v_mfma_f32_16x16x32_bf16 v[236:239], a[68:71], a[4:7], v[236:239]
  ds_read_b64_tr_b16 a[148:149], v48 offset:24576
  buffer_load_dwordx4 v4, s[20:23], s41 offen offset:1024 nt lds
  v_mfma_f32_16x16x32_bf16 v[240:243], a[72:75], a[4:7], v[240:243]
  ds_read_b64_tr_b16 a[150:151], v48 offset:25600
  v_mfma_f32_16x16x32_bf16 v[244:247], a[76:79], a[4:7], v[244:247]
  v_mfma_f32_16x16x32_bf16 v[248:251], a[80:83], a[4:7], v[248:251]
  v_mfma_f32_16x16x32_bf16 v[252:255], a[84:87], a[4:7], v[252:255]
  s_waitcnt vmcnt(9) lgkmcnt(0)
  s_barrier
  v_mfma_f32_16x16x32_bf16 v[112:115], a[88:91], a[20:23], v[112:115]
  s_mov_b32 s54, 768
  s_add_u32 m0, s55, 65536
  ds_read_b128 a[8:11], v39
  ds_read_b64_tr_b16 a[24:25], v41 offset:32768
  v_mfma_f32_16x16x32_bf16 v[116:119], a[92:95], a[20:23], v[116:119]
  ds_read_b64_tr_b16 a[26:27], v41 offset:33792
  buffer_load_dwordx4 v32, s[24:27], s54 offen lds
  v_mfma_f32_16x16x32_bf16 v[120:123], a[120:123], a[20:23], v[120:123]
  ds_read_b64_tr_b16 a[28:29], v42 offset:32768
  v_mfma_f32_16x16x32_bf16 v[124:127], a[124:127], a[20:23], v[124:127]
  ds_read_b64_tr_b16 a[30:31], v42 offset:33792
  v_mfma_f32_16x16x32_bf16 v[128:131], a[88:91], a[12:15], v[128:131]
  ds_read_b64_tr_b16 a[56:57], v41 offset:40960
  buffer_load_dwordx4 v33, s[24:27], s54 offen offset:1024 lds
  v_mfma_f32_16x16x32_bf16 v[132:135], a[92:95], a[12:15], v[132:135]
  ds_read_b64_tr_b16 a[58:59], v41 offset:41984
  v_mfma_f32_16x16x32_bf16 v[136:139], a[96:99], a[12:15], v[136:139]
  ds_read_b64_tr_b16 a[60:61], v42 offset:40960
  v_mfma_f32_16x16x32_bf16 v[140:143], a[100:103], a[12:15], v[140:143]
  ds_read_b64_tr_b16 a[62:63], v42 offset:41984
  v_mfma_f32_16x16x32_bf16 v[144:147], a[104:107], a[12:15], v[144:147]
  ds_read_b128 a[0:3], v37
  buffer_load_dwordx4 v34, s[24:27], s54 offen offset:2048 lds
  v_mfma_f32_16x16x32_bf16 v[148:151], a[108:111], a[12:15], v[148:151]
  ds_read_b64_tr_b16 a[32:33], v43 offset:32768
  v_mfma_f32_16x16x32_bf16 v[152:155], a[112:115], a[12:15], v[152:155]
  ds_read_b64_tr_b16 a[34:35], v43 offset:33792
  v_mfma_f32_16x16x32_bf16 v[156:159], a[116:119], a[12:15], v[156:159]
  ds_read_b64_tr_b16 a[36:37], v44 offset:32768
  v_mfma_f32_16x16x32_bf16 v[160:163], a[88:91], a[16:19], v[160:163]
  ds_read_b64_tr_b16 a[38:39], v44 offset:33792
  buffer_load_dwordx4 v35, s[24:27], s54 offen offset:3072 lds
  v_mfma_f32_16x16x32_bf16 v[164:167], a[92:95], a[16:19], v[164:167]
  ds_read_b64_tr_b16 a[40:41], v45 offset:32768
  v_mfma_f32_16x16x32_bf16 v[168:171], a[96:99], a[16:19], v[168:171]
  ds_read_b64_tr_b16 a[42:43], v45 offset:33792
  v_mfma_f32_16x16x32_bf16 v[172:175], a[100:103], a[16:19], v[172:175]
  s_add_u32 m0, s55, 69632
  ds_read_b64_tr_b16 a[44:45], v46 offset:32768
  v_mfma_f32_16x16x32_bf16 v[176:179], a[104:107], a[16:19], v[176:179]
  ds_read_b64_tr_b16 a[46:47], v46 offset:33792
  ds_read_b64_tr_b16 a[48:49], v47 offset:32768
  buffer_load_dwordx4 v36, s[24:27], s54 offen lds
  v_mfma_f32_16x16x32_bf16 v[180:183], a[108:111], a[16:19], v[180:183]
  ds_read_b64_tr_b16 a[50:51], v47 offset:33792
  v_mfma_f32_16x16x32_bf16 v[184:187], a[112:115], a[16:19], v[184:187]
  s_mov_b32 s49, 3145728
  s_add_u32 m0, s52, 16384
  ds_read_b64_tr_b16 a[52:53], v48 offset:32768
  v_mfma_f32_16x16x32_bf16 v[188:191], a[116:119], a[16:19], v[188:191]
  ds_read_b64_tr_b16 a[54:55], v48 offset:33792
  buffer_load_dwordx4 v11, s[28:31], s49 offen lds
  v_mfma_f32_16x16x32_bf16 v[192:195], a[120:123], a[12:15], v[192:195]
  ds_read_b128 a[4:7], v37 offset:2048
  v_mfma_f32_16x16x32_bf16 v[196:199], a[124:127], a[12:15], v[196:199]
  ds_read_b64_tr_b16 a[64:65], v43 offset:40960
  v_mfma_f32_16x16x32_bf16 v[200:203], a[128:131], a[12:15], v[200:203]
  ds_read_b64_tr_b16 a[66:67], v43 offset:41984
  v_mfma_f32_16x16x32_bf16 v[204:207], a[132:135], a[12:15], v[204:207]
  ds_read_b64_tr_b16 a[68:69], v44 offset:40960
  buffer_load_dwordx4 v12, s[28:31], s49 offen offset:1024 lds
  v_mfma_f32_16x16x32_bf16 v[208:211], a[136:139], a[12:15], v[208:211]
  ds_read_b64_tr_b16 a[70:71], v44 offset:41984
  v_mfma_f32_16x16x32_bf16 v[212:215], a[140:143], a[12:15], v[212:215]
  ds_read_b64_tr_b16 a[72:73], v45 offset:40960
  v_mfma_f32_16x16x32_bf16 v[216:219], a[144:147], a[12:15], v[216:219]
  s_mov_b32 s53, 3146752
  s_add_u32 m0, s52, 24576
  ds_read_b64_tr_b16 a[74:75], v45 offset:41984
  v_mfma_f32_16x16x32_bf16 v[220:223], a[148:151], a[12:15], v[220:223]
  ds_read_b64_tr_b16 a[76:77], v46 offset:40960
  buffer_load_dwordx4 v11, s[28:31], s53 offen lds
  v_mfma_f32_16x16x32_bf16 v[224:227], a[120:123], a[16:19], v[224:227]
  ds_read_b64_tr_b16 a[78:79], v46 offset:41984
  v_mfma_f32_16x16x32_bf16 v[228:231], a[124:127], a[16:19], v[228:231]
  ds_read_b64_tr_b16 a[80:81], v47 offset:40960
  v_mfma_f32_16x16x32_bf16 v[232:235], a[128:131], a[16:19], v[232:235]
  ds_read_b64_tr_b16 a[82:83], v47 offset:41984
  v_mfma_f32_16x16x32_bf16 v[236:239], a[132:135], a[16:19], v[236:239]
  ds_read_b64_tr_b16 a[84:85], v48 offset:40960
  buffer_load_dwordx4 v12, s[28:31], s53 offen offset:1024 lds
  v_mfma_f32_16x16x32_bf16 v[240:243], a[136:139], a[16:19], v[240:243]
  ds_read_b64_tr_b16 a[86:87], v48 offset:41984
  v_mfma_f32_16x16x32_bf16 v[244:247], a[140:143], a[16:19], v[244:247]
  v_mfma_f32_16x16x32_bf16 v[248:251], a[144:147], a[16:19], v[248:251]
  v_mfma_f32_16x16x32_bf16 v[252:255], a[148:151], a[16:19], v[252:255]
  s_waitcnt vmcnt(14) lgkmcnt(0)
  s_barrier
  v_mfma_f32_16x16x32_bf16 v[112:115], a[24:27], a[8:11], v[112:115]
  s_mov_b32 s49, 3407872
  s_add_u32 m0, s52, 32768
  ds_read_b128 a[20:23], v40
  ds_read_b64_tr_b16 a[88:89], v41
  v_mfma_f32_16x16x32_bf16 v[116:119], a[28:31], a[8:11], v[116:119]
  ds_read_b64_tr_b16 a[90:91], v41 offset:1024
  buffer_load_dwordx4 v11, s[28:31], s49 offen lds
  v_mfma_f32_16x16x32_bf16 v[120:123], a[56:59], a[8:11], v[120:123]
  ds_read_b64_tr_b16 a[92:93], v42
  v_mfma_f32_16x16x32_bf16 v[124:127], a[60:63], a[8:11], v[124:127]
  ds_read_b64_tr_b16 a[94:95], v42 offset:1024
  v_mfma_f32_16x16x32_bf16 v[128:131], a[24:27], a[0:3], v[128:131]
  ds_read_b64_tr_b16 a[120:121], v41 offset:8192
  buffer_load_dwordx4 v12, s[28:31], s49 offen offset:1024 lds
  v_mfma_f32_16x16x32_bf16 v[132:135], a[28:31], a[0:3], v[132:135]
  ds_read_b64_tr_b16 a[122:123], v41 offset:9216
  v_mfma_f32_16x16x32_bf16 v[136:139], a[32:35], a[0:3], v[136:139]
  ds_read_b64_tr_b16 a[124:125], v42 offset:8192
  v_mfma_f32_16x16x32_bf16 v[140:143], a[36:39], a[0:3], v[140:143]
  s_mov_b32 s53, 3408896
  s_add_u32 m0, s52, 40960
  ds_read_b64_tr_b16 a[126:127], v42 offset:9216
  v_mfma_f32_16x16x32_bf16 v[144:147], a[40:43], a[0:3], v[144:147]
  ds_read_b128 a[12:15], v38
  buffer_load_dwordx4 v11, s[28:31], s53 offen lds
  v_mfma_f32_16x16x32_bf16 v[148:151], a[44:47], a[0:3], v[148:151]
  ds_read_b64_tr_b16 a[96:97], v43
  v_mfma_f32_16x16x32_bf16 v[152:155], a[48:51], a[0:3], v[152:155]
  ds_read_b64_tr_b16 a[98:99], v43 offset:1024
  v_mfma_f32_16x16x32_bf16 v[156:159], a[52:55], a[0:3], v[156:159]
  ds_read_b64_tr_b16 a[100:101], v44
  v_mfma_f32_16x16x32_bf16 v[160:163], a[24:27], a[4:7], v[160:163]
  ds_read_b64_tr_b16 a[102:103], v44 offset:1024
  buffer_load_dwordx4 v12, s[28:31], s53 offen offset:1024 lds
  v_mfma_f32_16x16x32_bf16 v[164:167], a[28:31], a[4:7], v[164:167]
  ds_read_b64_tr_b16 a[104:105], v45
  v_mfma_f32_16x16x32_bf16 v[168:171], a[32:35], a[4:7], v[168:171]
  ds_read_b64_tr_b16 a[106:107], v45 offset:1024
  v_mfma_f32_16x16x32_bf16 v[172:175], a[36:39], a[4:7], v[172:175]
  s_mov_b32 s78, 1152
  ds_read_b64_tr_b16 a[108:109], v46
  v_mfma_f32_16x16x32_bf16 v[176:179], a[40:43], a[4:7], v[176:179]
  ds_read_b64_tr_b16 a[110:111], v46 offset:1024
  ds_read_b64_tr_b16 a[112:113], v47
  buffer_load_dword v25, v26, s[72:75], s78 offen
  v_mfma_f32_16x16x32_bf16 v[180:183], a[44:47], a[4:7], v[180:183]
  ds_read_b64_tr_b16 a[114:115], v47 offset:1024
  v_mfma_f32_16x16x32_bf16 v[184:187], a[48:51], a[4:7], v[184:187]
  s_mov_b32 s79, 4718592
  ds_read_b64_tr_b16 a[116:117], v48
  v_mfma_f32_16x16x32_bf16 v[188:191], a[52:55], a[4:7], v[188:191]
  ds_read_b64_tr_b16 a[118:119], v48 offset:1024
  buffer_load_dword v25, v24, s[68:71], s79 offen
  v_mfma_f32_16x16x32_bf16 v[192:195], a[56:59], a[0:3], v[192:195]
  ds_read_b128 a[16:19], v38 offset:2048
  v_mfma_f32_16x16x32_bf16 v[196:199], a[60:63], a[0:3], v[196:199]
  ds_read_b64_tr_b16 a[128:129], v43 offset:8192
  v_mfma_f32_16x16x32_bf16 v[200:203], a[64:67], a[0:3], v[200:203]
  s_add_u32 m0, s37, 32768
  ds_read_b64_tr_b16 a[130:131], v43 offset:9216
  v_mfma_f32_16x16x32_bf16 v[204:207], a[68:71], a[0:3], v[204:207]
  ds_read_b64_tr_b16 a[132:133], v44 offset:8192
  buffer_load_dwordx4 v1, s[16:19], s42 offen nt lds
  v_mfma_f32_16x16x32_bf16 v[208:211], a[72:75], a[0:3], v[208:211]
  ds_read_b64_tr_b16 a[134:135], v44 offset:9216
  v_mfma_f32_16x16x32_bf16 v[212:215], a[76:79], a[0:3], v[212:215]
  ds_read_b64_tr_b16 a[136:137], v45 offset:8192
  v_mfma_f32_16x16x32_bf16 v[216:219], a[80:83], a[0:3], v[216:219]
  ds_read_b64_tr_b16 a[138:139], v45 offset:9216
  v_mfma_f32_16x16x32_bf16 v[220:223], a[84:87], a[0:3], v[220:223]
  ds_read_b64_tr_b16 a[140:141], v46 offset:8192
  buffer_load_dwordx4 v2, s[16:19], s42 offen offset:1024 nt lds
  v_mfma_f32_16x16x32_bf16 v[224:227], a[56:59], a[4:7], v[224:227]
  ds_read_b64_tr_b16 a[142:143], v46 offset:9216
  v_mfma_f32_16x16x32_bf16 v[228:231], a[60:63], a[4:7], v[228:231]
  ds_read_b64_tr_b16 a[144:145], v47 offset:8192
  v_mfma_f32_16x16x32_bf16 v[232:235], a[64:67], a[4:7], v[232:235]
  s_add_u32 m0, s37, 40960
  ds_read_b64_tr_b16 a[146:147], v47 offset:9216
  v_mfma_f32_16x16x32_bf16 v[236:239], a[68:71], a[4:7], v[236:239]
  ds_read_b64_tr_b16 a[148:149], v48 offset:8192
  buffer_load_dwordx4 v3, s[20:23], s42 offen nt lds
  v_mfma_f32_16x16x32_bf16 v[240:243], a[72:75], a[4:7], v[240:243]
  ds_read_b64_tr_b16 a[150:151], v48 offset:9216
  v_mfma_f32_16x16x32_bf16 v[244:247], a[76:79], a[4:7], v[244:247]
  v_mfma_f32_16x16x32_bf16 v[248:251], a[80:83], a[4:7], v[248:251]
  v_mfma_f32_16x16x32_bf16 v[252:255], a[84:87], a[4:7], v[252:255]
  s_waitcnt vmcnt(9) lgkmcnt(0)
  s_barrier
  v_mfma_f32_16x16x32_bf16 v[112:115], a[88:91], a[20:23], v[112:115]
  s_mov_b32 s54, 896
  s_add_u32 m0, s55, 133120
  ds_read_b128 a[8:11], v15
  ds_read_b64_tr_b16 a[24:25], v41 offset:16384
  v_mfma_f32_16x16x32_bf16 v[116:119], a[92:95], a[20:23], v[116:119]
  ds_read_b64_tr_b16 a[26:27], v41 offset:17408
  buffer_load_dwordx4 v32, s[24:27], s54 offen lds
  v_mfma_f32_16x16x32_bf16 v[120:123], a[120:123], a[20:23], v[120:123]
  ds_read_b64_tr_b16 a[28:29], v42 offset:16384
  v_mfma_f32_16x16x32_bf16 v[124:127], a[124:127], a[20:23], v[124:127]
  ds_read_b64_tr_b16 a[30:31], v42 offset:17408
  v_mfma_f32_16x16x32_bf16 v[128:131], a[88:91], a[12:15], v[128:131]
  ds_read_b64_tr_b16 a[56:57], v41 offset:24576
  buffer_load_dwordx4 v33, s[24:27], s54 offen offset:1024 lds
  v_mfma_f32_16x16x32_bf16 v[132:135], a[92:95], a[12:15], v[132:135]
  ds_read_b64_tr_b16 a[58:59], v41 offset:25600
  v_mfma_f32_16x16x32_bf16 v[136:139], a[96:99], a[12:15], v[136:139]
  ds_read_b64_tr_b16 a[60:61], v42 offset:24576
  v_mfma_f32_16x16x32_bf16 v[140:143], a[100:103], a[12:15], v[140:143]
  ds_read_b64_tr_b16 a[62:63], v42 offset:25600
  v_mfma_f32_16x16x32_bf16 v[144:147], a[104:107], a[12:15], v[144:147]
  ds_read_b128 a[0:3], v13
  buffer_load_dwordx4 v34, s[24:27], s54 offen offset:2048 lds
  v_mfma_f32_16x16x32_bf16 v[148:151], a[108:111], a[12:15], v[148:151]
  ds_read_b64_tr_b16 a[32:33], v43 offset:16384
  v_mfma_f32_16x16x32_bf16 v[152:155], a[112:115], a[12:15], v[152:155]
  ds_read_b64_tr_b16 a[34:35], v43 offset:17408
  v_mfma_f32_16x16x32_bf16 v[156:159], a[116:119], a[12:15], v[156:159]
  ds_read_b64_tr_b16 a[36:37], v44 offset:16384
  v_mfma_f32_16x16x32_bf16 v[160:163], a[88:91], a[16:19], v[160:163]
  ds_read_b64_tr_b16 a[38:39], v44 offset:17408
  buffer_load_dwordx4 v35, s[24:27], s54 offen offset:3072 lds
  v_mfma_f32_16x16x32_bf16 v[164:167], a[92:95], a[16:19], v[164:167]
  ds_read_b64_tr_b16 a[40:41], v45 offset:16384
  v_mfma_f32_16x16x32_bf16 v[168:171], a[96:99], a[16:19], v[168:171]
  ds_read_b64_tr_b16 a[42:43], v45 offset:17408
  v_mfma_f32_16x16x32_bf16 v[172:175], a[100:103], a[16:19], v[172:175]
  s_add_u32 m0, s55, 137216
  ds_read_b64_tr_b16 a[44:45], v46 offset:16384
  v_mfma_f32_16x16x32_bf16 v[176:179], a[104:107], a[16:19], v[176:179]
  ds_read_b64_tr_b16 a[46:47], v46 offset:17408
  ds_read_b64_tr_b16 a[48:49], v47 offset:16384
  buffer_load_dwordx4 v36, s[24:27], s54 offen lds
  v_mfma_f32_16x16x32_bf16 v[180:183], a[108:111], a[16:19], v[180:183]
  ds_read_b64_tr_b16 a[50:51], v47 offset:17408
  v_mfma_f32_16x16x32_bf16 v[184:187], a[112:115], a[16:19], v[184:187]
  s_mov_b32 s49, 3670016
  s_mov_b32 m0, s52
  ds_read_b64_tr_b16 a[52:53], v48 offset:16384
  v_mfma_f32_16x16x32_bf16 v[188:191], a[116:119], a[16:19], v[188:191]
  ds_read_b64_tr_b16 a[54:55], v48 offset:17408
  buffer_load_dwordx4 v11, s[28:31], s49 offen lds
  v_mfma_f32_16x16x32_bf16 v[192:195], a[120:123], a[12:15], v[192:195]
  ds_read_b128 a[4:7], v13 offset:2048
  v_mfma_f32_16x16x32_bf16 v[196:199], a[124:127], a[12:15], v[196:199]
  ds_read_b64_tr_b16 a[64:65], v43 offset:24576
  v_mfma_f32_16x16x32_bf16 v[200:203], a[128:131], a[12:15], v[200:203]
  ds_read_b64_tr_b16 a[66:67], v43 offset:25600
  v_mfma_f32_16x16x32_bf16 v[204:207], a[132:135], a[12:15], v[204:207]
  ds_read_b64_tr_b16 a[68:69], v44 offset:24576
  buffer_load_dwordx4 v12, s[28:31], s49 offen offset:1024 lds
  v_mfma_f32_16x16x32_bf16 v[208:211], a[136:139], a[12:15], v[208:211]
  ds_read_b64_tr_b16 a[70:71], v44 offset:25600
  v_mfma_f32_16x16x32_bf16 v[212:215], a[140:143], a[12:15], v[212:215]
  ds_read_b64_tr_b16 a[72:73], v45 offset:24576
  v_mfma_f32_16x16x32_bf16 v[216:219], a[144:147], a[12:15], v[216:219]
  s_mov_b32 s53, 3671040
  s_add_u32 m0, s52, 8192
  ds_read_b64_tr_b16 a[74:75], v45 offset:25600
  v_mfma_f32_16x16x32_bf16 v[220:223], a[148:151], a[12:15], v[220:223]
  ds_read_b64_tr_b16 a[76:77], v46 offset:24576
  buffer_load_dwordx4 v11, s[28:31], s53 offen lds
  v_mfma_f32_16x16x32_bf16 v[224:227], a[120:123], a[16:19], v[224:227]
  ds_read_b64_tr_b16 a[78:79], v46 offset:25600
  v_mfma_f32_16x16x32_bf16 v[228:231], a[124:127], a[16:19], v[228:231]
  ds_read_b64_tr_b16 a[80:81], v47 offset:24576
  v_mfma_f32_16x16x32_bf16 v[232:235], a[128:131], a[16:19], v[232:235]
  ds_read_b64_tr_b16 a[82:83], v47 offset:25600
  v_mfma_f32_16x16x32_bf16 v[236:239], a[132:135], a[16:19], v[236:239]
  ds_read_b64_tr_b16 a[84:85], v48 offset:24576
  buffer_load_dwordx4 v12, s[28:31], s53 offen offset:1024 lds
  v_mfma_f32_16x16x32_bf16 v[240:243], a[136:139], a[16:19], v[240:243]
  ds_read_b64_tr_b16 a[86:87], v48 offset:25600
  v_mfma_f32_16x16x32_bf16 v[244:247], a[140:143], a[16:19], v[244:247]
  v_mfma_f32_16x16x32_bf16 v[248:251], a[144:147], a[16:19], v[248:251]
  v_mfma_f32_16x16x32_bf16 v[252:255], a[148:151], a[16:19], v[252:255]
  s_waitcnt vmcnt(14) lgkmcnt(0)
  s_barrier
  v_mfma_f32_16x16x32_bf16 v[112:115], a[24:27], a[8:11], v[112:115]
  s_mov_b32 s49, 3932160
  s_add_u32 m0, s52, 16384
  ds_read_b128 a[20:23], v16
  ds_read_b64_tr_b16 a[88:89], v41 offset:32768
  v_mfma_f32_16x16x32_bf16 v[116:119], a[28:31], a[8:11], v[116:119]
  ds_read_b64_tr_b16 a[90:91], v41 offset:33792
  buffer_load_dwordx4 v11, s[28:31], s49 offen lds
  v_mfma_f32_16x16x32_bf16 v[120:123], a[56:59], a[8:11], v[120:123]
  ds_read_b64_tr_b16 a[92:93], v42 offset:32768
  v_mfma_f32_16x16x32_bf16 v[124:127], a[60:63], a[8:11], v[124:127]
  ds_read_b64_tr_b16 a[94:95], v42 offset:33792
  v_mfma_f32_16x16x32_bf16 v[128:131], a[24:27], a[0:3], v[128:131]
  ds_read_b64_tr_b16 a[120:121], v41 offset:40960
  v_mfma_f32_16x16x32_bf16 v[132:135], a[28:31], a[0:3], v[132:135]
  ds_read_b64_tr_b16 a[122:123], v41 offset:41984
  buffer_load_dwordx4 v12, s[28:31], s49 offen offset:1024 lds
  v_mfma_f32_16x16x32_bf16 v[136:139], a[32:35], a[0:3], v[136:139]
  ds_read_b64_tr_b16 a[124:125], v42 offset:40960
  v_mfma_f32_16x16x32_bf16 v[140:143], a[36:39], a[0:3], v[140:143]
  ds_read_b64_tr_b16 a[126:127], v42 offset:41984
  v_mfma_f32_16x16x32_bf16 v[144:147], a[40:43], a[0:3], v[144:147]
  ds_read_b128 a[12:15], v14
  v_mfma_f32_16x16x32_bf16 v[148:151], a[44:47], a[0:3], v[148:151]
  s_mov_b32 s53, 3933184
  s_add_u32 m0, s52, 24576
  ds_read_b64_tr_b16 a[96:97], v43 offset:32768
  v_mfma_f32_16x16x32_bf16 v[152:155], a[48:51], a[0:3], v[152:155]
  ds_read_b64_tr_b16 a[98:99], v43 offset:33792
  buffer_load_dwordx4 v11, s[28:31], s53 offen lds
  v_mfma_f32_16x16x32_bf16 v[156:159], a[52:55], a[0:3], v[156:159]
  ds_read_b64_tr_b16 a[100:101], v44 offset:32768
  v_mfma_f32_16x16x32_bf16 v[160:163], a[24:27], a[4:7], v[160:163]
  ds_read_b64_tr_b16 a[102:103], v44 offset:33792
  v_mfma_f32_16x16x32_bf16 v[164:167], a[28:31], a[4:7], v[164:167]
  ds_read_b64_tr_b16 a[104:105], v45 offset:32768
  v_mfma_f32_16x16x32_bf16 v[168:171], a[32:35], a[4:7], v[168:171]
  ds_read_b64_tr_b16 a[106:107], v45 offset:33792
  v_mfma_f32_16x16x32_bf16 v[172:175], a[36:39], a[4:7], v[172:175]
  ds_read_b64_tr_b16 a[108:109], v46 offset:32768
  buffer_load_dwordx4 v12, s[28:31], s53 offen offset:1024 lds
  v_mfma_f32_16x16x32_bf16 v[176:179], a[40:43], a[4:7], v[176:179]
  ds_read_b64_tr_b16 a[110:111], v46 offset:33792
  ds_read_b64_tr_b16 a[112:113], v47 offset:32768
  v_mfma_f32_16x16x32_bf16 v[180:183], a[44:47], a[4:7], v[180:183]
  ds_read_b64_tr_b16 a[114:115], v47 offset:33792
  v_mfma_f32_16x16x32_bf16 v[184:187], a[48:51], a[4:7], v[184:187]
  ds_read_b64_tr_b16 a[116:117], v48 offset:32768
  v_mfma_f32_16x16x32_bf16 v[188:191], a[52:55], a[4:7], v[188:191]
  s_add_u32 m0, s37, 40960
  ds_read_b64_tr_b16 a[118:119], v48 offset:33792
  v_mfma_f32_16x16x32_bf16 v[192:195], a[56:59], a[0:3], v[192:195]
  ds_read_b128 a[16:19], v14 offset:2048
  buffer_load_dwordx4 v4, s[20:23], s42 offen offset:1024 nt lds
  v_mfma_f32_16x16x32_bf16 v[196:199], a[60:63], a[0:3], v[196:199]
  ds_read_b64_tr_b16 a[128:129], v43 offset:40960
  v_mfma_f32_16x16x32_bf16 v[200:203], a[64:67], a[0:3], v[200:203]
  ds_read_b64_tr_b16 a[130:131], v43 offset:41984
  v_mfma_f32_16x16x32_bf16 v[204:207], a[68:71], a[0:3], v[204:207]
  ds_read_b64_tr_b16 a[132:133], v44 offset:40960
  v_mfma_f32_16x16x32_bf16 v[208:211], a[72:75], a[0:3], v[208:211]
  s_add_u32 m0, s37, 49152
  ds_read_b64_tr_b16 a[134:135], v44 offset:41984
  v_mfma_f32_16x16x32_bf16 v[212:215], a[76:79], a[0:3], v[212:215]
  ds_read_b64_tr_b16 a[136:137], v45 offset:40960
  buffer_load_dwordx4 v1, s[16:19], s43 offen nt lds
  v_mfma_f32_16x16x32_bf16 v[216:219], a[80:83], a[0:3], v[216:219]
  ds_read_b64_tr_b16 a[138:139], v45 offset:41984
  v_mfma_f32_16x16x32_bf16 v[220:223], a[84:87], a[0:3], v[220:223]
  ds_read_b64_tr_b16 a[140:141], v46 offset:40960
  v_mfma_f32_16x16x32_bf16 v[224:227], a[56:59], a[4:7], v[224:227]
  ds_read_b64_tr_b16 a[142:143], v46 offset:41984
  v_mfma_f32_16x16x32_bf16 v[228:231], a[60:63], a[4:7], v[228:231]
  ds_read_b64_tr_b16 a[144:145], v47 offset:40960
  v_mfma_f32_16x16x32_bf16 v[232:235], a[64:67], a[4:7], v[232:235]
  ds_read_b64_tr_b16 a[146:147], v47 offset:41984
  buffer_load_dwordx4 v2, s[16:19], s43 offen offset:1024 nt lds
  v_mfma_f32_16x16x32_bf16 v[236:239], a[68:71], a[4:7], v[236:239]
  ds_read_b64_tr_b16 a[148:149], v48 offset:40960
  v_mfma_f32_16x16x32_bf16 v[240:243], a[72:75], a[4:7], v[240:243]
  ds_read_b64_tr_b16 a[150:151], v48 offset:41984
  v_mfma_f32_16x16x32_bf16 v[244:247], a[76:79], a[4:7], v[244:247]
  v_mfma_f32_16x16x32_bf16 v[248:251], a[80:83], a[4:7], v[248:251]
  v_mfma_f32_16x16x32_bf16 v[252:255], a[84:87], a[4:7], v[252:255]
  s_waitcnt vmcnt(7) lgkmcnt(0)
  s_barrier
  v_mfma_f32_16x16x32_bf16 v[112:115], a[88:91], a[20:23], v[112:115]
  s_mov_b32 s54, 1024
  s_add_u32 m0, s55, 65536
  ds_read_b128 a[8:11], v39
  ds_read_b64_tr_b16 a[24:25], v41
  v_mfma_f32_16x16x32_bf16 v[116:119], a[92:95], a[20:23], v[116:119]
  ds_read_b64_tr_b16 a[26:27], v41 offset:1024
  buffer_load_dwordx4 v32, s[24:27], s54 offen lds
  v_mfma_f32_16x16x32_bf16 v[120:123], a[120:123], a[20:23], v[120:123]
  ds_read_b64_tr_b16 a[28:29], v42
  v_mfma_f32_16x16x32_bf16 v[124:127], a[124:127], a[20:23], v[124:127]
  ds_read_b64_tr_b16 a[30:31], v42 offset:1024
  v_mfma_f32_16x16x32_bf16 v[128:131], a[88:91], a[12:15], v[128:131]
  ds_read_b64_tr_b16 a[56:57], v41 offset:8192
  buffer_load_dwordx4 v33, s[24:27], s54 offen offset:1024 lds
  v_mfma_f32_16x16x32_bf16 v[132:135], a[92:95], a[12:15], v[132:135]
  ds_read_b64_tr_b16 a[58:59], v41 offset:9216
  v_mfma_f32_16x16x32_bf16 v[136:139], a[96:99], a[12:15], v[136:139]
  ds_read_b64_tr_b16 a[60:61], v42 offset:8192
  v_mfma_f32_16x16x32_bf16 v[140:143], a[100:103], a[12:15], v[140:143]
  ds_read_b64_tr_b16 a[62:63], v42 offset:9216
  v_mfma_f32_16x16x32_bf16 v[144:147], a[104:107], a[12:15], v[144:147]
  ds_read_b128 a[0:3], v37
  buffer_load_dwordx4 v34, s[24:27], s54 offen offset:2048 lds
  v_mfma_f32_16x16x32_bf16 v[148:151], a[108:111], a[12:15], v[148:151]
  ds_read_b64_tr_b16 a[32:33], v43
  v_mfma_f32_16x16x32_bf16 v[152:155], a[112:115], a[12:15], v[152:155]
  ds_read_b64_tr_b16 a[34:35], v43 offset:1024
  v_mfma_f32_16x16x32_bf16 v[156:159], a[116:119], a[12:15], v[156:159]
  ds_read_b64_tr_b16 a[36:37], v44
  v_mfma_f32_16x16x32_bf16 v[160:163], a[88:91], a[16:19], v[160:163]
  ds_read_b64_tr_b16 a[38:39], v44 offset:1024
  buffer_load_dwordx4 v35, s[24:27], s54 offen offset:3072 lds
  v_mfma_f32_16x16x32_bf16 v[164:167], a[92:95], a[16:19], v[164:167]
  ds_read_b64_tr_b16 a[40:41], v45
  v_mfma_f32_16x16x32_bf16 v[168:171], a[96:99], a[16:19], v[168:171]
  ds_read_b64_tr_b16 a[42:43], v45 offset:1024
  v_mfma_f32_16x16x32_bf16 v[172:175], a[100:103], a[16:19], v[172:175]
  s_add_u32 m0, s55, 69632
  ds_read_b64_tr_b16 a[44:45], v46
  v_mfma_f32_16x16x32_bf16 v[176:179], a[104:107], a[16:19], v[176:179]
  ds_read_b64_tr_b16 a[46:47], v46 offset:1024
  ds_read_b64_tr_b16 a[48:49], v47
  buffer_load_dwordx4 v36, s[24:27], s54 offen lds
  v_mfma_f32_16x16x32_bf16 v[180:183], a[108:111], a[16:19], v[180:183]
  ds_read_b64_tr_b16 a[50:51], v47 offset:1024
  v_mfma_f32_16x16x32_bf16 v[184:187], a[112:115], a[16:19], v[184:187]
  s_mov_b32 s49, 4194304
  s_add_u32 m0, s52, 32768
  ds_read_b64_tr_b16 a[52:53], v48
  v_mfma_f32_16x16x32_bf16 v[188:191], a[116:119], a[16:19], v[188:191]
  ds_read_b64_tr_b16 a[54:55], v48 offset:1024
  buffer_load_dwordx4 v11, s[28:31], s49 offen lds
  v_mfma_f32_16x16x32_bf16 v[192:195], a[120:123], a[12:15], v[192:195]
  ds_read_b128 a[4:7], v37 offset:2048
  v_mfma_f32_16x16x32_bf16 v[196:199], a[124:127], a[12:15], v[196:199]
  ds_read_b64_tr_b16 a[64:65], v43 offset:8192
  v_mfma_f32_16x16x32_bf16 v[200:203], a[128:131], a[12:15], v[200:203]
  ds_read_b64_tr_b16 a[66:67], v43 offset:9216
  v_mfma_f32_16x16x32_bf16 v[204:207], a[132:135], a[12:15], v[204:207]
  ds_read_b64_tr_b16 a[68:69], v44 offset:8192
  buffer_load_dwordx4 v12, s[28:31], s49 offen offset:1024 lds
  v_mfma_f32_16x16x32_bf16 v[208:211], a[136:139], a[12:15], v[208:211]
  ds_read_b64_tr_b16 a[70:71], v44 offset:9216
  v_mfma_f32_16x16x32_bf16 v[212:215], a[140:143], a[12:15], v[212:215]
  ds_read_b64_tr_b16 a[72:73], v45 offset:8192
  v_mfma_f32_16x16x32_bf16 v[216:219], a[144:147], a[12:15], v[216:219]
  s_mov_b32 s53, 4195328
  s_add_u32 m0, s52, 40960
  ds_read_b64_tr_b16 a[74:75], v45 offset:9216
  v_mfma_f32_16x16x32_bf16 v[220:223], a[148:151], a[12:15], v[220:223]
  ds_read_b64_tr_b16 a[76:77], v46 offset:8192
  buffer_load_dwordx4 v11, s[28:31], s53 offen lds
  v_mfma_f32_16x16x32_bf16 v[224:227], a[120:123], a[16:19], v[224:227]
  ds_read_b64_tr_b16 a[78:79], v46 offset:9216
  v_mfma_f32_16x16x32_bf16 v[228:231], a[124:127], a[16:19], v[228:231]
  ds_read_b64_tr_b16 a[80:81], v47 offset:8192
  v_mfma_f32_16x16x32_bf16 v[232:235], a[128:131], a[16:19], v[232:235]
  ds_read_b64_tr_b16 a[82:83], v47 offset:9216
  v_mfma_f32_16x16x32_bf16 v[236:239], a[132:135], a[16:19], v[236:239]
  ds_read_b64_tr_b16 a[84:85], v48 offset:8192
  buffer_load_dwordx4 v12, s[28:31], s53 offen offset:1024 lds
  v_mfma_f32_16x16x32_bf16 v[240:243], a[136:139], a[16:19], v[240:243]
  ds_read_b64_tr_b16 a[86:87], v48 offset:9216
  v_mfma_f32_16x16x32_bf16 v[244:247], a[140:143], a[16:19], v[244:247]
  v_mfma_f32_16x16x32_bf16 v[248:251], a[144:147], a[16:19], v[248:251]
  v_mfma_f32_16x16x32_bf16 v[252:255], a[148:151], a[16:19], v[252:255]
  s_waitcnt vmcnt(12) lgkmcnt(0)
  s_barrier
  v_mfma_f32_16x16x32_bf16 v[112:115], a[24:27], a[8:11], v[112:115]
  s_mov_b32 s49, 4456448
  s_mov_b32 m0, s52
  ds_read_b128 a[20:23], v40
  ds_read_b64_tr_b16 a[88:89], v41 offset:16384
  v_mfma_f32_16x16x32_bf16 v[116:119], a[28:31], a[8:11], v[116:119]
  ds_read_b64_tr_b16 a[90:91], v41 offset:17408
  buffer_load_dwordx4 v11, s[28:31], s49 offen lds
  v_mfma_f32_16x16x32_bf16 v[120:123], a[56:59], a[8:11], v[120:123]
  ds_read_b64_tr_b16 a[92:93], v42 offset:16384
  v_mfma_f32_16x16x32_bf16 v[124:127], a[60:63], a[8:11], v[124:127]
  ds_read_b64_tr_b16 a[94:95], v42 offset:17408
  v_mfma_f32_16x16x32_bf16 v[128:131], a[24:27], a[0:3], v[128:131]
  ds_read_b64_tr_b16 a[120:121], v41 offset:24576
  v_mfma_f32_16x16x32_bf16 v[132:135], a[28:31], a[0:3], v[132:135]
  ds_read_b64_tr_b16 a[122:123], v41 offset:25600
  v_mfma_f32_16x16x32_bf16 v[136:139], a[32:35], a[0:3], v[136:139]
  ds_read_b64_tr_b16 a[124:125], v42 offset:24576
  buffer_load_dwordx4 v12, s[28:31], s49 offen offset:1024 lds
  v_mfma_f32_16x16x32_bf16 v[140:143], a[36:39], a[0:3], v[140:143]
  ds_read_b64_tr_b16 a[126:127], v42 offset:25600
  v_mfma_f32_16x16x32_bf16 v[144:147], a[40:43], a[0:3], v[144:147]
  ds_read_b128 a[12:15], v38
  v_mfma_f32_16x16x32_bf16 v[148:151], a[44:47], a[0:3], v[148:151]
  ds_read_b64_tr_b16 a[96:97], v43 offset:16384
  v_mfma_f32_16x16x32_bf16 v[152:155], a[48:51], a[0:3], v[152:155]
  s_mov_b32 s53, 4457472
  s_add_u32 m0, s52, 8192
  ds_read_b64_tr_b16 a[98:99], v43 offset:17408
  v_mfma_f32_16x16x32_bf16 v[156:159], a[52:55], a[0:3], v[156:159]
  ds_read_b64_tr_b16 a[100:101], v44 offset:16384
  buffer_load_dwordx4 v11, s[28:31], s53 offen lds
  v_mfma_f32_16x16x32_bf16 v[160:163], a[24:27], a[4:7], v[160:163]
  ds_read_b64_tr_b16 a[102:103], v44 offset:17408
  v_mfma_f32_16x16x32_bf16 v[164:167], a[28:31], a[4:7], v[164:167]
  ds_read_b64_tr_b16 a[104:105], v45 offset:16384
  v_mfma_f32_16x16x32_bf16 v[168:171], a[32:35], a[4:7], v[168:171]
  ds_read_b64_tr_b16 a[106:107], v45 offset:17408
  v_mfma_f32_16x16x32_bf16 v[172:175], a[36:39], a[4:7], v[172:175]
  ds_read_b64_tr_b16 a[108:109], v46 offset:16384
  v_mfma_f32_16x16x32_bf16 v[176:179], a[40:43], a[4:7], v[176:179]
  ds_read_b64_tr_b16 a[110:111], v46 offset:17408
  ds_read_b64_tr_b16 a[112:113], v47 offset:16384
  buffer_load_dwordx4 v12, s[28:31], s53 offen offset:1024 lds
  v_mfma_f32_16x16x32_bf16 v[180:183], a[44:47], a[4:7], v[180:183]
  ds_read_b64_tr_b16 a[114:115], v47 offset:17408
  v_mfma_f32_16x16x32_bf16 v[184:187], a[48:51], a[4:7], v[184:187]
  ds_read_b64_tr_b16 a[116:117], v48 offset:16384
  v_mfma_f32_16x16x32_bf16 v[188:191], a[52:55], a[4:7], v[188:191]
  ds_read_b64_tr_b16 a[118:119], v48 offset:17408
  v_mfma_f32_16x16x32_bf16 v[192:195], a[56:59], a[0:3], v[192:195]
  s_add_u32 m0, s37, 57344
  ds_read_b128 a[16:19], v38 offset:2048
  v_mfma_f32_16x16x32_bf16 v[196:199], a[60:63], a[0:3], v[196:199]
  ds_read_b64_tr_b16 a[128:129], v43 offset:24576
  buffer_load_dwordx4 v3, s[20:23], s43 offen nt lds
  v_mfma_f32_16x16x32_bf16 v[200:203], a[64:67], a[0:3], v[200:203]
  ds_read_b64_tr_b16 a[130:131], v43 offset:25600
  v_mfma_f32_16x16x32_bf16 v[204:207], a[68:71], a[0:3], v[204:207]
  ds_read_b64_tr_b16 a[132:133], v44 offset:24576
  v_mfma_f32_16x16x32_bf16 v[208:211], a[72:75], a[0:3], v[208:211]
  ds_read_b64_tr_b16 a[134:135], v44 offset:25600
  v_mfma_f32_16x16x32_bf16 v[212:215], a[76:79], a[0:3], v[212:215]
  ds_read_b64_tr_b16 a[136:137], v45 offset:24576
  v_mfma_f32_16x16x32_bf16 v[216:219], a[80:83], a[0:3], v[216:219]
  ds_read_b64_tr_b16 a[138:139], v45 offset:25600
  buffer_load_dwordx4 v4, s[20:23], s43 offen offset:1024 nt lds
  v_mfma_f32_16x16x32_bf16 v[220:223], a[84:87], a[0:3], v[220:223]
  ds_read_b64_tr_b16 a[140:141], v46 offset:24576
  v_mfma_f32_16x16x32_bf16 v[224:227], a[56:59], a[4:7], v[224:227]
  ds_read_b64_tr_b16 a[142:143], v46 offset:25600
  v_mfma_f32_16x16x32_bf16 v[228:231], a[60:63], a[4:7], v[228:231]
  ds_read_b64_tr_b16 a[144:145], v47 offset:24576
  v_mfma_f32_16x16x32_bf16 v[232:235], a[64:67], a[4:7], v[232:235]
  ds_read_b64_tr_b16 a[146:147], v47 offset:25600
  v_mfma_f32_16x16x32_bf16 v[236:239], a[68:71], a[4:7], v[236:239]
  ds_read_b64_tr_b16 a[148:149], v48 offset:24576
  v_bfe_u32 v28, v27, 2, 2
  v_lshrrev_b32_e32 v29, 4, v27
  v_and_b32_e32 v30, 1, v27
  v_lshlrev_b32_e32 v30, 4, v30
  v_mfma_f32_16x16x32_bf16 v[240:243], a[72:75], a[4:7], v[240:243]
  ds_read_b64_tr_b16 a[150:151], v48 offset:25600
  v_mov_b32_e32 v31, 0x3f80
  v_lshlrev_b32_e32 v30, v30, v31
  v_cmp_eq_u32_e32 vcc, v28, v29
  v_mfma_f32_16x16x32_bf16 v[244:247], a[76:79], a[4:7], v[244:247]
  v_and_b32_e32 v28, 2, v27
  v_mov_b32_e32 v20, 0
  v_cndmask_b32_e32 v30, 0, v30, vcc
  v_mfma_f32_16x16x32_bf16 v[248:251], a[80:83], a[4:7], v[248:251]
  v_cmp_eq_u32_e32 vcc, 0, v28
  v_mov_b32_e32 v21, 0
  v_mov_b32_e32 v24, 0
  v_mfma_f32_16x16x32_bf16 v[252:255], a[84:87], a[4:7], v[252:255]
  v_cndmask_b32_e32 v22, 0, v30, vcc
  v_sub_u32_e32 v23, v30, v22
  v_mov_b32_e32 v25, 0
  s_waitcnt vmcnt(6) lgkmcnt(0)
  s_barrier
  v_mfma_f32_16x16x32_bf16 v[112:115], a[88:91], a[20:23], v[112:115]
  s_mov_b32 s54, 1152
  s_add_u32 m0, s55, 133120
  ds_read_b128 a[8:11], v15
  ds_read_b64_tr_b16 a[24:25], v41 offset:32768
  v_mfma_f32_16x16x32_bf16 v[116:119], a[92:95], a[20:23], v[116:119]
  ds_read_b64_tr_b16 a[26:27], v41 offset:33792
  buffer_load_dwordx4 v32, s[24:27], s54 offen lds
  v_mfma_f32_16x16x32_bf16 v[120:123], a[120:123], a[20:23], v[120:123]
  ds_read_b64_tr_b16 a[28:29], v42 offset:32768
  v_mfma_f32_16x16x32_bf16 v[124:127], a[124:127], a[20:23], v[124:127]
  ds_read_b64_tr_b16 a[30:31], v42 offset:33792
  v_mfma_f32_16x16x32_bf16 v[128:131], a[88:91], a[12:15], v[128:131]
  ds_read_b64_tr_b16 a[56:57], v41 offset:40960
  buffer_load_dwordx4 v33, s[24:27], s54 offen offset:1024 lds
  v_mfma_f32_16x16x32_bf16 v[132:135], a[92:95], a[12:15], v[132:135]
  ds_read_b64_tr_b16 a[58:59], v41 offset:41984
  v_mfma_f32_16x16x32_bf16 v[136:139], a[96:99], a[12:15], v[136:139]
  ds_read_b64_tr_b16 a[60:61], v42 offset:40960
  v_mfma_f32_16x16x32_bf16 v[140:143], a[100:103], a[12:15], v[140:143]
  ds_read_b64_tr_b16 a[62:63], v42 offset:41984
  v_mfma_f32_16x16x32_bf16 v[144:147], a[104:107], a[12:15], v[144:147]
  ds_read_b128 a[0:3], v13
  buffer_load_dwordx4 v34, s[24:27], s54 offen offset:2048 lds
  v_mfma_f32_16x16x32_bf16 v[148:151], a[108:111], a[12:15], v[148:151]
  ds_read_b64_tr_b16 a[32:33], v43 offset:32768
  v_mfma_f32_16x16x32_bf16 v[152:155], a[112:115], a[12:15], v[152:155]
  ds_read_b64_tr_b16 a[34:35], v43 offset:33792
  v_mfma_f32_16x16x32_bf16 v[156:159], a[116:119], a[12:15], v[156:159]
  ds_read_b64_tr_b16 a[36:37], v44 offset:32768
  v_mfma_f32_16x16x32_bf16 v[160:163], a[88:91], a[16:19], v[160:163]
  ds_read_b64_tr_b16 a[38:39], v44 offset:33792
  buffer_load_dwordx4 v35, s[24:27], s54 offen offset:3072 lds
  v_mfma_f32_16x16x32_bf16 v[164:167], a[92:95], a[16:19], v[164:167]
  ds_read_b64_tr_b16 a[40:41], v45 offset:32768
  v_mfma_f32_16x16x32_bf16 v[168:171], a[96:99], a[16:19], v[168:171]
  ds_read_b64_tr_b16 a[42:43], v45 offset:33792
  v_mfma_f32_16x16x32_bf16 v[172:175], a[100:103], a[16:19], v[172:175]
  s_add_u32 m0, s55, 137216
  ds_read_b64_tr_b16 a[44:45], v46 offset:32768
  v_mfma_f32_16x16x32_bf16 v[176:179], a[104:107], a[16:19], v[176:179]
  ds_read_b64_tr_b16 a[46:47], v46 offset:33792
  ds_read_b64_tr_b16 a[48:49], v47 offset:32768
  buffer_load_dwordx4 v36, s[24:27], s54 offen lds
  v_mfma_f32_16x16x32_bf16 v[180:183], a[108:111], a[16:19], v[180:183]
  ds_read_b64_tr_b16 a[50:51], v47 offset:33792
  v_mfma_f32_16x16x32_bf16 v[184:187], a[112:115], a[16:19], v[184:187]
  s_mov_b32 s49, 4718592
  s_add_u32 m0, s52, 16384
  ds_read_b64_tr_b16 a[52:53], v48 offset:32768
  v_mfma_f32_16x16x32_bf16 v[188:191], a[116:119], a[16:19], v[188:191]
  ds_read_b64_tr_b16 a[54:55], v48 offset:33792
  buffer_load_dwordx4 v11, s[28:31], s49 offen lds
  v_mfma_f32_16x16x32_bf16 v[192:195], a[120:123], a[12:15], v[192:195]
  ds_read_b128 a[4:7], v13 offset:2048
  v_mfma_f32_16x16x32_bf16 v[196:199], a[124:127], a[12:15], v[196:199]
  ds_read_b64_tr_b16 a[64:65], v43 offset:40960
  v_mfma_f32_16x16x32_bf16 v[200:203], a[128:131], a[12:15], v[200:203]
  ds_read_b64_tr_b16 a[66:67], v43 offset:41984
  v_mfma_f32_16x16x32_bf16 v[204:207], a[132:135], a[12:15], v[204:207]
  ds_read_b64_tr_b16 a[68:69], v44 offset:40960
  buffer_load_dwordx4 v12, s[28:31], s49 offen offset:1024 lds
  v_mfma_f32_16x16x32_bf16 v[208:211], a[136:139], a[12:15], v[208:211]
  ds_read_b64_tr_b16 a[70:71], v44 offset:41984
  v_mfma_f32_16x16x32_bf16 v[212:215], a[140:143], a[12:15], v[212:215]
  ds_read_b64_tr_b16 a[72:73], v45 offset:40960
  v_mfma_f32_16x16x32_bf16 v[216:219], a[144:147], a[12:15], v[216:219]
  s_mov_b32 s53, 4719616
  s_add_u32 m0, s52, 24576
  ds_read_b64_tr_b16 a[74:75], v45 offset:41984
  v_mfma_f32_16x16x32_bf16 v[220:223], a[148:151], a[12:15], v[220:223]
  ds_read_b64_tr_b16 a[76:77], v46 offset:40960
  buffer_load_dwordx4 v11, s[28:31], s53 offen lds
  v_mfma_f32_16x16x32_bf16 v[224:227], a[120:123], a[16:19], v[224:227]
  ds_read_b64_tr_b16 a[78:79], v46 offset:41984
  v_mfma_f32_16x16x32_bf16 v[228:231], a[124:127], a[16:19], v[228:231]
  ds_read_b64_tr_b16 a[80:81], v47 offset:40960
  v_mfma_f32_16x16x32_bf16 v[232:235], a[128:131], a[16:19], v[232:235]
  ds_read_b64_tr_b16 a[82:83], v47 offset:41984
  v_mfma_f32_16x16x32_bf16 v[236:239], a[132:135], a[16:19], v[236:239]
  ds_read_b64_tr_b16 a[84:85], v48 offset:40960
  buffer_load_dwordx4 v12, s[28:31], s53 offen offset:1024 lds
  v_mfma_f32_16x16x32_bf16 v[240:243], a[136:139], a[16:19], v[240:243]
  ds_read_b64_tr_b16 a[86:87], v48 offset:41984
  v_mfma_f32_16x16x32_bf16 v[244:247], a[140:143], a[16:19], v[244:247]
  v_mfma_f32_16x16x32_bf16 v[248:251], a[144:147], a[16:19], v[248:251]
  v_mfma_f32_16x16x32_bf16 v[252:255], a[148:151], a[16:19], v[252:255]
  s_waitcnt vmcnt(11) lgkmcnt(0)
  s_barrier
  v_mfma_f32_16x16x32_bf16 v[112:115], a[24:27], a[8:11], v[112:115]
  s_mov_b32 s49, 4980736
  s_add_u32 m0, s52, 32768
  ds_read_b128 a[20:23], v16
  ds_read_b64_tr_b16 a[88:89], v41
  v_mfma_f32_16x16x32_bf16 v[116:119], a[28:31], a[8:11], v[116:119]
  ds_read_b64_tr_b16 a[90:91], v41 offset:1024
  buffer_load_dwordx4 v11, s[28:31], s49 offen lds
  v_mfma_f32_16x16x32_bf16 v[120:123], a[56:59], a[8:11], v[120:123]
  ds_read_b64_tr_b16 a[92:93], v42
  v_mfma_f32_16x16x32_bf16 v[124:127], a[60:63], a[8:11], v[124:127]
  ds_read_b64_tr_b16 a[94:95], v42 offset:1024
  v_mfma_f32_16x16x32_bf16 v[128:131], a[24:27], a[0:3], v[128:131]
  ds_read_b64_tr_b16 a[120:121], v41 offset:8192
  v_mfma_f32_16x16x32_bf16 v[132:135], a[28:31], a[0:3], v[132:135]
  ds_read_b64_tr_b16 a[122:123], v41 offset:9216
  v_mfma_f32_16x16x32_bf16 v[136:139], a[32:35], a[0:3], v[136:139]
  ds_read_b64_tr_b16 a[124:125], v42 offset:8192
  buffer_load_dwordx4 v12, s[28:31], s49 offen offset:1024 lds
  v_mfma_f32_16x16x32_bf16 v[140:143], a[36:39], a[0:3], v[140:143]
  ds_read_b64_tr_b16 a[126:127], v42 offset:9216
  v_mfma_f32_16x16x32_bf16 v[144:147], a[40:43], a[0:3], v[144:147]
  ds_read_b128 a[12:15], v14
  v_mfma_f32_16x16x32_bf16 v[148:151], a[44:47], a[0:3], v[148:151]
  ds_read_b64_tr_b16 a[96:97], v43
  v_mfma_f32_16x16x32_bf16 v[152:155], a[48:51], a[0:3], v[152:155]
  s_mov_b32 s53, 4981760
  s_add_u32 m0, s52, 40960
  ds_read_b64_tr_b16 a[98:99], v43 offset:1024
  v_mfma_f32_16x16x32_bf16 v[156:159], a[52:55], a[0:3], v[156:159]
  ds_read_b64_tr_b16 a[100:101], v44
  buffer_load_dwordx4 v11, s[28:31], s53 offen lds
  v_mfma_f32_16x16x32_bf16 v[160:163], a[24:27], a[4:7], v[160:163]
  ds_read_b64_tr_b16 a[102:103], v44 offset:1024
  v_mfma_f32_16x16x32_bf16 v[164:167], a[28:31], a[4:7], v[164:167]
  ds_read_b64_tr_b16 a[104:105], v45
  v_mfma_f32_16x16x32_bf16 v[168:171], a[32:35], a[4:7], v[168:171]
  ds_read_b64_tr_b16 a[106:107], v45 offset:1024
  v_mfma_f32_16x16x32_bf16 v[172:175], a[36:39], a[4:7], v[172:175]
  ds_read_b64_tr_b16 a[108:109], v46
  v_mfma_f32_16x16x32_bf16 v[176:179], a[40:43], a[4:7], v[176:179]
  ds_read_b64_tr_b16 a[110:111], v46 offset:1024
  ds_read_b64_tr_b16 a[112:113], v47
  buffer_load_dwordx4 v12, s[28:31], s53 offen offset:1024 lds
  v_mfma_f32_16x16x32_bf16 v[180:183], a[44:47], a[4:7], v[180:183]
  ds_read_b64_tr_b16 a[114:115], v47 offset:1024
  v_mfma_f32_16x16x32_bf16 v[184:187], a[48:51], a[4:7], v[184:187]
  ds_read_b64_tr_b16 a[116:117], v48
  v_mfma_f32_16x16x32_bf16 v[188:191], a[52:55], a[4:7], v[188:191]
  ds_read_b64_tr_b16 a[118:119], v48 offset:1024
  v_mfma_f32_16x16x32_bf16 v[192:195], a[56:59], a[0:3], v[192:195]
  ds_read_b128 a[16:19], v14 offset:2048
  v_mfma_f32_16x16x32_bf16 v[196:199], a[60:63], a[0:3], v[196:199]
  ds_read_b64_tr_b16 a[128:129], v43 offset:8192
  v_mfma_f32_16x16x32_bf16 v[200:203], a[64:67], a[0:3], v[200:203]
  ds_read_b64_tr_b16 a[130:131], v43 offset:9216
  v_mfma_f32_16x16x32_bf16 v[204:207], a[68:71], a[0:3], v[204:207]
  ds_read_b64_tr_b16 a[132:133], v44 offset:8192
  v_mfma_f32_16x16x32_bf16 v[208:211], a[72:75], a[0:3], v[208:211]
  s_add_u32 s39, s39, 1
  ds_read_b64_tr_b16 a[134:135], v44 offset:9216
  v_mfma_f32_16x16x32_bf16 v[212:215], a[76:79], a[0:3], v[212:215]
  s_lshr_b32 s50, s39, 3
  ds_read_b64_tr_b16 a[136:137], v45 offset:8192
  v_mfma_f32_16x16x32_bf16 v[216:219], a[80:83], a[0:3], v[216:219]
  s_lshl_b32 s50, s50, 20
  ds_read_b64_tr_b16 a[138:139], v45 offset:9216
  v_mfma_f32_16x16x32_bf16 v[220:223], a[84:87], a[0:3], v[220:223]
  s_add_u32 s51, s39, s62
  ds_read_b64_tr_b16 a[140:141], v46 offset:8192
  v_mfma_f32_16x16x32_bf16 v[224:227], a[56:59], a[4:7], v[224:227]
  s_and_b32 s51, s51, 7
  ds_read_b64_tr_b16 a[142:143], v46 offset:9216
  v_mfma_f32_16x16x32_bf16 v[228:231], a[60:63], a[4:7], v[228:231]
  s_lshl_b32 s51, s51, 15
  ds_read_b64_tr_b16 a[144:145], v47 offset:8192
  v_mfma_f32_16x16x32_bf16 v[232:235], a[64:67], a[4:7], v[232:235]
  s_add_u32 s40, s50, s51
  ds_read_b64_tr_b16 a[146:147], v47 offset:9216
  v_mfma_f32_16x16x32_bf16 v[236:239], a[68:71], a[4:7], v[236:239]
  s_add_u32 s41, s40, 8192
  ds_read_b64_tr_b16 a[148:149], v48 offset:8192
  v_mfma_f32_16x16x32_bf16 v[240:243], a[72:75], a[4:7], v[240:243]
  s_add_u32 s42, s40, 16384
  ds_read_b64_tr_b16 a[150:151], v48 offset:9216
  v_mfma_f32_16x16x32_bf16 v[244:247], a[76:79], a[4:7], v[244:247]
  s_add_u32 s43, s40, 24576
  v_mfma_f32_16x16x32_bf16 v[248:251], a[80:83], a[4:7], v[248:251]
  s_xor_b32 s37, s37, 0x10000
  v_mfma_f32_16x16x32_bf16 v[252:255], a[84:87], a[4:7], v[252:255]
  s_waitcnt vmcnt(4) lgkmcnt(0)
  s_barrier
  v_mfma_f32_16x16x32_bf16 v[112:115], a[88:91], a[20:23], v[112:115]
  s_mov_b32 m0, s37
  ds_read_b128 a[8:11], v39
  ds_read_b64_tr_b16 a[24:25], v41 offset:16384
  v_mfma_f32_16x16x32_bf16 v[116:119], a[92:95], a[20:23], v[116:119]
  ds_read_b64_tr_b16 a[26:27], v41 offset:17408
  buffer_load_dwordx4 v1, s[16:19], s40 offen nt lds
  v_mfma_f32_16x16x32_bf16 v[120:123], a[120:123], a[20:23], v[120:123]
  ds_read_b64_tr_b16 a[28:29], v42 offset:16384
  v_mfma_f32_16x16x32_bf16 v[124:127], a[124:127], a[20:23], v[124:127]
  ds_read_b64_tr_b16 a[30:31], v42 offset:17408
  v_mfma_f32_16x16x32_bf16 v[128:131], a[88:91], a[12:15], v[128:131]
  ds_read_b64_tr_b16 a[56:57], v41 offset:24576
  v_mfma_f32_16x16x32_bf16 v[132:135], a[92:95], a[12:15], v[132:135]
  ds_read_b64_tr_b16 a[58:59], v41 offset:25600
  buffer_load_dwordx4 v2, s[16:19], s40 offen offset:1024 nt lds
  v_mfma_f32_16x16x32_bf16 v[136:139], a[96:99], a[12:15], v[136:139]
  ds_read_b64_tr_b16 a[60:61], v42 offset:24576
  v_mfma_f32_16x16x32_bf16 v[140:143], a[100:103], a[12:15], v[140:143]
  ds_read_b64_tr_b16 a[62:63], v42 offset:25600
  v_mfma_f32_16x16x32_bf16 v[144:147], a[104:107], a[12:15], v[144:147]
  s_add_u32 m0, s37, 8192
  ds_read_b128 a[0:3], v37
  v_mfma_f32_16x16x32_bf16 v[148:151], a[108:111], a[12:15], v[148:151]
  ds_read_b64_tr_b16 a[32:33], v43 offset:16384
  buffer_load_dwordx4 v3, s[20:23], s40 offen nt lds
  v_mfma_f32_16x16x32_bf16 v[152:155], a[112:115], a[12:15], v[152:155]
  ds_read_b64_tr_b16 a[34:35], v43 offset:17408
  v_mfma_f32_16x16x32_bf16 v[156:159], a[116:119], a[12:15], v[156:159]
  ds_read_b64_tr_b16 a[36:37], v44 offset:16384
  v_mfma_f32_16x16x32_bf16 v[160:163], a[88:91], a[16:19], v[160:163]
  ds_read_b64_tr_b16 a[38:39], v44 offset:17408
  v_mfma_f32_16x16x32_bf16 v[164:167], a[92:95], a[16:19], v[164:167]
  ds_read_b64_tr_b16 a[40:41], v45 offset:16384
  buffer_load_dwordx4 v4, s[20:23], s40 offen offset:1024 nt lds
  v_mfma_f32_16x16x32_bf16 v[168:171], a[96:99], a[16:19], v[168:171]
  ds_read_b64_tr_b16 a[42:43], v45 offset:17408
  v_mfma_f32_16x16x32_bf16 v[172:175], a[100:103], a[16:19], v[172:175]
  ds_read_b64_tr_b16 a[44:45], v46 offset:16384
  v_mfma_f32_16x16x32_bf16 v[176:179], a[104:107], a[16:19], v[176:179]
  ds_read_b64_tr_b16 a[46:47], v46 offset:17408
  ds_read_b64_tr_b16 a[48:49], v47 offset:16384
  v_mfma_f32_16x16x32_bf16 v[180:183], a[108:111], a[16:19], v[180:183]
  s_add_u32 m0, s37, 16384
  ds_read_b64_tr_b16 a[50:51], v47 offset:17408
  v_mfma_f32_16x16x32_bf16 v[184:187], a[112:115], a[16:19], v[184:187]
  ds_read_b64_tr_b16 a[52:53], v48 offset:16384
  buffer_load_dwordx4 v1, s[16:19], s41 offen nt lds
  v_mfma_f32_16x16x32_bf16 v[188:191], a[116:119], a[16:19], v[188:191]
  ds_read_b64_tr_b16 a[54:55], v48 offset:17408
  v_mfma_f32_16x16x32_bf16 v[192:195], a[120:123], a[12:15], v[192:195]
  ds_read_b128 a[4:7], v37 offset:2048
  v_mfma_f32_16x16x32_bf16 v[196:199], a[124:127], a[12:15], v[196:199]
  ds_read_b64_tr_b16 a[64:65], v43 offset:24576
  v_mfma_f32_16x16x32_bf16 v[200:203], a[128:131], a[12:15], v[200:203]
  ds_read_b64_tr_b16 a[66:67], v43 offset:25600
  buffer_load_dwordx4 v2, s[16:19], s41 offen offset:1024 nt lds
  v_mfma_f32_16x16x32_bf16 v[204:207], a[132:135], a[12:15], v[204:207]
  ds_read_b64_tr_b16 a[68:69], v44 offset:24576
  v_mfma_f32_16x16x32_bf16 v[208:211], a[136:139], a[12:15], v[208:211]
  ds_read_b64_tr_b16 a[70:71], v44 offset:25600
  v_mfma_f32_16x16x32_bf16 v[212:215], a[140:143], a[12:15], v[212:215]
  s_add_u32 m0, s37, 24576
  ds_read_b64_tr_b16 a[72:73], v45 offset:24576
  v_mfma_f32_16x16x32_bf16 v[216:219], a[144:147], a[12:15], v[216:219]
  ds_read_b64_tr_b16 a[74:75], v45 offset:25600
  buffer_load_dwordx4 v3, s[20:23], s41 offen nt lds
  v_mfma_f32_16x16x32_bf16 v[220:223], a[148:151], a[12:15], v[220:223]
  ds_read_b64_tr_b16 a[76:77], v46 offset:24576
  v_mfma_f32_16x16x32_bf16 v[224:227], a[120:123], a[16:19], v[224:227]
  ds_read_b64_tr_b16 a[78:79], v46 offset:25600
  v_mfma_f32_16x16x32_bf16 v[228:231], a[124:127], a[16:19], v[228:231]
  ds_read_b64_tr_b16 a[80:81], v47 offset:24576
  v_mfma_f32_16x16x32_bf16 v[232:235], a[128:131], a[16:19], v[232:235]
  ds_read_b64_tr_b16 a[82:83], v47 offset:25600
  buffer_load_dwordx4 v4, s[20:23], s41 offen offset:1024 nt lds
  v_mfma_f32_16x16x32_bf16 v[236:239], a[132:135], a[16:19], v[236:239]
  ds_read_b64_tr_b16 a[84:85], v48 offset:24576
  v_mfma_f32_16x16x32_bf16 v[240:243], a[136:139], a[16:19], v[240:243]
  ds_read_b64_tr_b16 a[86:87], v48 offset:25600
  v_mfma_f32_16x16x32_bf16 v[244:247], a[140:143], a[16:19], v[244:247]
  v_mfma_f32_16x16x32_bf16 v[248:251], a[144:147], a[16:19], v[248:251]
  v_mfma_f32_16x16x32_bf16 v[252:255], a[148:151], a[16:19], v[252:255]
  s_waitcnt vmcnt(8) lgkmcnt(0)
  s_barrier
  v_mfma_f32_16x16x32_bf16 v[112:115], a[24:27], a[8:11], v[112:115]
  s_add_u32 m0, s37, 32768
  ds_read_b128 a[20:23], v40
  ds_read_b64_tr_b16 a[88:89], v41 offset:32768
  v_mfma_f32_16x16x32_bf16 v[116:119], a[28:31], a[8:11], v[116:119]
  ds_read_b64_tr_b16 a[90:91], v41 offset:33792
  buffer_load_dwordx4 v1, s[16:19], s42 offen nt lds
  v_mfma_f32_16x16x32_bf16 v[120:123], a[56:59], a[8:11], v[120:123]
  ds_read_b64_tr_b16 a[92:93], v42 offset:32768
  v_mfma_f32_16x16x32_bf16 v[124:127], a[60:63], a[8:11], v[124:127]
  ds_read_b64_tr_b16 a[94:95], v42 offset:33792
  v_mfma_f32_16x16x32_bf16 v[128:131], a[24:27], a[0:3], v[128:131]
  ds_read_b64_tr_b16 a[120:121], v41 offset:40960
  v_mfma_f32_16x16x32_bf16 v[132:135], a[28:31], a[0:3], v[132:135]
  ds_read_b64_tr_b16 a[122:123], v41 offset:41984
  v_mfma_f32_16x16x32_bf16 v[136:139], a[32:35], a[0:3], v[136:139]
  ds_read_b64_tr_b16 a[124:125], v42 offset:40960
  buffer_load_dwordx4 v2, s[16:19], s42 offen offset:1024 nt lds
  v_mfma_f32_16x16x32_bf16 v[140:143], a[36:39], a[0:3], v[140:143]
  ds_read_b64_tr_b16 a[126:127], v42 offset:41984
  v_mfma_f32_16x16x32_bf16 v[144:147], a[40:43], a[0:3], v[144:147]
  ds_read_b128 a[12:15], v38
  v_mfma_f32_16x16x32_bf16 v[148:151], a[44:47], a[0:3], v[148:151]
  ds_read_b64_tr_b16 a[96:97], v43 offset:32768
  v_mfma_f32_16x16x32_bf16 v[152:155], a[48:51], a[0:3], v[152:155]
  s_add_u32 m0, s37, 40960
  ds_read_b64_tr_b16 a[98:99], v43 offset:33792
  v_mfma_f32_16x16x32_bf16 v[156:159], a[52:55], a[0:3], v[156:159]
  ds_read_b64_tr_b16 a[100:101], v44 offset:32768
  buffer_load_dwordx4 v3, s[20:23], s42 offen nt lds
  v_mfma_f32_16x16x32_bf16 v[160:163], a[24:27], a[4:7], v[160:163]
  ds_read_b64_tr_b16 a[102:103], v44 offset:33792
  v_mfma_f32_16x16x32_bf16 v[164:167], a[28:31], a[4:7], v[164:167]
  ds_read_b64_tr_b16 a[104:105], v45 offset:32768
  v_mfma_f32_16x16x32_bf16 v[168:171], a[32:35], a[4:7], v[168:171]
  ds_read_b64_tr_b16 a[106:107], v45 offset:33792
  v_mfma_f32_16x16x32_bf16 v[172:175], a[36:39], a[4:7], v[172:175]
  ds_read_b64_tr_b16 a[108:109], v46 offset:32768
  v_mfma_f32_16x16x32_bf16 v[176:179], a[40:43], a[4:7], v[176:179]
  ds_read_b64_tr_b16 a[110:111], v46 offset:33792
  ds_read_b64_tr_b16 a[112:113], v47 offset:32768
  buffer_load_dwordx4 v4, s[20:23], s42 offen offset:1024 nt lds
  v_mfma_f32_16x16x32_bf16 v[180:183], a[44:47], a[4:7], v[180:183]
  ds_read_b64_tr_b16 a[114:115], v47 offset:33792
  v_mfma_f32_16x16x32_bf16 v[184:187], a[48:51], a[4:7], v[184:187]
  ds_read_b64_tr_b16 a[116:117], v48 offset:32768
  v_mfma_f32_16x16x32_bf16 v[188:191], a[52:55], a[4:7], v[188:191]
  ds_read_b64_tr_b16 a[118:119], v48 offset:33792
  v_mfma_f32_16x16x32_bf16 v[192:195], a[56:59], a[0:3], v[192:195]
  ds_read_b128 a[16:19], v38 offset:2048
  v_mfma_f32_16x16x32_bf16 v[196:199], a[60:63], a[0:3], v[196:199]
  ds_read_b64_tr_b16 a[128:129], v43 offset:40960
  v_mfma_f32_16x16x32_bf16 v[200:203], a[64:67], a[0:3], v[200:203]
  ds_read_b64_tr_b16 a[130:131], v43 offset:41984
  v_mfma_f32_16x16x32_bf16 v[204:207], a[68:71], a[0:3], v[204:207]
  ds_read_b64_tr_b16 a[132:133], v44 offset:40960
  v_mfma_f32_16x16x32_bf16 v[208:211], a[72:75], a[0:3], v[208:211]
  ds_read_b64_tr_b16 a[134:135], v44 offset:41984
  v_mfma_f32_16x16x32_bf16 v[212:215], a[76:79], a[0:3], v[212:215]
  ds_read_b64_tr_b16 a[136:137], v45 offset:40960
  v_mfma_f32_16x16x32_bf16 v[216:219], a[80:83], a[0:3], v[216:219]
  ds_read_b64_tr_b16 a[138:139], v45 offset:41984
  v_mfma_f32_16x16x32_bf16 v[220:223], a[84:87], a[0:3], v[220:223]
  ds_read_b64_tr_b16 a[140:141], v46 offset:40960
  v_mfma_f32_16x16x32_bf16 v[224:227], a[56:59], a[4:7], v[224:227]
  s_lshl_b32 s50, s36, 10
  ds_read_b64_tr_b16 a[142:143], v46 offset:41984
  v_mfma_f32_16x16x32_bf16 v[228:231], a[60:63], a[4:7], v[228:231]
  s_add_u32 s50, s50, 131072
  ds_read_b64_tr_b16 a[144:145], v47 offset:40960
  v_mfma_f32_16x16x32_bf16 v[232:235], a[64:67], a[4:7], v[232:235]
  s_mov_b32 s63, 0x3e0293ee
  ds_read_b64_tr_b16 a[146:147], v47 offset:41984
  v_mfma_f32_16x16x32_bf16 v[236:239], a[68:71], a[4:7], v[236:239]
  ds_read_b64_tr_b16 a[148:149], v48 offset:40960
  v_add_u32_e32 v28, s50, v0
  v_mfma_f32_16x16x32_bf16 v[240:243], a[72:75], a[4:7], v[240:243]
  ds_read_b64_tr_b16 a[150:151], v48 offset:41984
  v_add_u32_e32 v29, 131072, v0
  v_mfma_f32_16x16x32_bf16 v[244:247], a[76:79], a[4:7], v[244:247]
  v_mfma_f32_16x16x32_bf16 v[248:251], a[80:83], a[4:7], v[248:251]
  v_mfma_f32_16x16x32_bf16 v[252:255], a[84:87], a[4:7], v[252:255]
  s_waitcnt vmcnt(25) lgkmcnt(0)
  s_barrier
  v_mfma_f32_16x16x32_bf16 v[112:115], a[88:91], a[20:23], v[112:115]
  s_add_u32 m0, s37, 49152
  v_mfma_f32_16x16x32_bf16 v[116:119], a[92:95], a[20:23], v[116:119]
  buffer_load_dwordx4 v1, s[16:19], s43 offen nt lds
  v_mfma_f32_16x16x32_bf16 v[120:123], a[120:123], a[20:23], v[120:123]
  v_mfma_f32_16x16x32_bf16 v[124:127], a[124:127], a[20:23], v[124:127]
  v_mfma_f32_16x16x32_bf16 v[128:131], a[88:91], a[12:15], v[128:131]
  buffer_load_dwordx4 v2, s[16:19], s43 offen offset:1024 nt lds
  v_mfma_f32_16x16x32_bf16 v[132:135], a[92:95], a[12:15], v[132:135]
  v_mfma_f32_16x16x32_bf16 v[136:139], a[96:99], a[12:15], v[136:139]
  s_add_u32 m0, s37, 57344
  v_mfma_f32_16x16x32_bf16 v[140:143], a[100:103], a[12:15], v[140:143]
  buffer_load_dwordx4 v3, s[20:23], s43 offen nt lds
  v_mfma_f32_16x16x32_bf16 v[144:147], a[104:107], a[12:15], v[144:147]
  v_mul_f32_e32 v112, s63, v112
  v_mul_f32_e32 v113, s63, v113
  v_mfma_f32_16x16x32_bf16 v[148:151], a[108:111], a[12:15], v[148:151]
  v_mul_f32_e32 v114, s63, v114
  v_mul_f32_e32 v115, s63, v115
  v_mfma_f32_16x16x32_bf16 v[152:155], a[112:115], a[12:15], v[152:155]
  buffer_load_dwordx4 v4, s[20:23], s43 offen offset:1024 nt lds
  v_mul_f32_e32 v116, s63, v116
  v_mul_f32_e32 v117, s63, v117
  v_mfma_f32_16x16x32_bf16 v[156:159], a[116:119], a[12:15], v[156:159]
  v_mul_f32_e32 v118, s63, v118
  v_mul_f32_e32 v119, s63, v119
  v_mfma_f32_16x16x32_bf16 v[160:163], a[88:91], a[16:19], v[160:163]
  v_cvt_pk_bf16_f32 v72, v112, v113
  v_cvt_pk_bf16_f32 v73, v114, v115
  v_mfma_f32_16x16x32_bf16 v[164:167], a[92:95], a[16:19], v[164:167]
  v_cvt_pk_bf16_f32 v74, v116, v117
  v_cvt_pk_bf16_f32 v75, v118, v119
  v_mfma_f32_16x16x32_bf16 v[168:171], a[96:99], a[16:19], v[168:171]
  v_mul_f32_e32 v120, s63, v120
  v_mul_f32_e32 v121, s63, v121
  v_mfma_f32_16x16x32_bf16 v[172:175], a[100:103], a[16:19], v[172:175]
  v_mul_f32_e32 v122, s63, v122
  v_mul_f32_e32 v123, s63, v123
  v_mfma_f32_16x16x32_bf16 v[176:179], a[104:107], a[16:19], v[176:179]
  v_mul_f32_e32 v124, s63, v124
  v_mul_f32_e32 v125, s63, v125
  v_mfma_f32_16x16x32_bf16 v[180:183], a[108:111], a[16:19], v[180:183]
  v_mul_f32_e32 v126, s63, v126
  v_mul_f32_e32 v127, s63, v127
  v_mfma_f32_16x16x32_bf16 v[184:187], a[112:115], a[16:19], v[184:187]
  v_cvt_pk_bf16_f32 v76, v120, v121
  v_cvt_pk_bf16_f32 v77, v122, v123
  v_mfma_f32_16x16x32_bf16 v[188:191], a[116:119], a[16:19], v[188:191]
  v_cvt_pk_bf16_f32 v78, v124, v125
  v_cvt_pk_bf16_f32 v79, v126, v127
  v_mfma_f32_16x16x32_bf16 v[192:195], a[120:123], a[12:15], v[192:195]
  v_permlane16_swap_b32_e32 v72, v74
  v_permlane16_swap_b32_e32 v73, v75
  v_mfma_f32_16x16x32_bf16 v[196:199], a[124:127], a[12:15], v[196:199]
  ds_write_b128 v28, v[72:75]
  v_permlane16_swap_b32_e32 v76, v78
  v_mfma_f32_16x16x32_bf16 v[200:203], a[128:131], a[12:15], v[200:203]
  v_permlane16_swap_b32_e32 v77, v79
  ds_write_b128 v28, v[76:79] offset:4096
  v_mfma_f32_16x16x32_bf16 v[204:207], a[132:135], a[12:15], v[204:207]
  v_mul_f32_e32 v128, s63, v128
  v_mul_f32_e32 v129, s63, v129
  v_mfma_f32_16x16x32_bf16 v[208:211], a[136:139], a[12:15], v[208:211]
  v_mul_f32_e32 v130, s63, v130
  v_mul_f32_e32 v131, s63, v131
  v_mfma_f32_16x16x32_bf16 v[212:215], a[140:143], a[12:15], v[212:215]
  v_mul_f32_e32 v132, s63, v132
  v_mul_f32_e32 v133, s63, v133
  v_mfma_f32_16x16x32_bf16 v[216:219], a[144:147], a[12:15], v[216:219]
  v_mul_f32_e32 v134, s63, v134
  v_mul_f32_e32 v135, s63, v135
  v_mfma_f32_16x16x32_bf16 v[220:223], a[148:151], a[12:15], v[220:223]
  v_cvt_pk_bf16_f32 v32, v128, v129
  v_cvt_pk_bf16_f32 v33, v130, v131
  v_mfma_f32_16x16x32_bf16 v[224:227], a[120:123], a[16:19], v[224:227]
  v_cvt_pk_bf16_f32 v34, v132, v133
  v_cvt_pk_bf16_f32 v35, v134, v135
  v_mfma_f32_16x16x32_bf16 v[228:231], a[124:127], a[16:19], v[228:231]
  v_mul_f32_e32 v136, s63, v136
  v_mul_f32_e32 v137, s63, v137
  v_mfma_f32_16x16x32_bf16 v[232:235], a[128:131], a[16:19], v[232:235]
  v_mul_f32_e32 v138, s63, v138
  v_mul_f32_e32 v139, s63, v139
  v_mfma_f32_16x16x32_bf16 v[236:239], a[132:135], a[16:19], v[236:239]
  v_mul_f32_e32 v140, s63, v140
  v_mul_f32_e32 v141, s63, v141
  v_mfma_f32_16x16x32_bf16 v[240:243], a[136:139], a[16:19], v[240:243]
  v_mul_f32_e32 v142, s63, v142
  v_mul_f32_e32 v143, s63, v143
  v_mfma_f32_16x16x32_bf16 v[244:247], a[140:143], a[16:19], v[244:247]
  v_cvt_pk_bf16_f32 v36, v136, v137
  v_cvt_pk_bf16_f32 v37, v138, v139
  v_mfma_f32_16x16x32_bf16 v[248:251], a[144:147], a[16:19], v[248:251]
  v_cvt_pk_bf16_f32 v38, v140, v141
  v_cvt_pk_bf16_f32 v39, v142, v143
  v_mfma_f32_16x16x32_bf16 v[252:255], a[148:151], a[16:19], v[252:255]
  v_permlane16_swap_b32_e32 v32, v34
  v_permlane16_swap_b32_e32 v33, v35
  v_mul_f32_e32 v144, s63, v144
  v_mul_f32_e32 v145, s63, v145
  v_mul_f32_e32 v146, s63, v146
  v_mul_f32_e32 v147, s63, v147
  v_mul_f32_e32 v148, s63, v148
  v_mul_f32_e32 v149, s63, v149
  v_mul_f32_e32 v150, s63, v150
  v_mul_f32_e32 v151, s63, v151
  v_cvt_pk_bf16_f32 v40, v144, v145
  v_cvt_pk_bf16_f32 v41, v146, v147
  v_cvt_pk_bf16_f32 v42, v148, v149
  v_cvt_pk_bf16_f32 v43, v150, v151
  v_permlane16_swap_b32_e32 v36, v38
  v_permlane16_swap_b32_e32 v37, v39
  v_mul_f32_e32 v152, s63, v152
  v_mul_f32_e32 v153, s63, v153
  v_mul_f32_e32 v154, s63, v154
  v_mul_f32_e32 v155, s63, v155
  v_mul_f32_e32 v156, s63, v156
  v_mul_f32_e32 v157, s63, v157
  v_mul_f32_e32 v158, s63, v158
  v_mul_f32_e32 v159, s63, v159
  v_cvt_pk_bf16_f32 v44, v152, v153
  v_cvt_pk_bf16_f32 v45, v154, v155
  v_cvt_pk_bf16_f32 v46, v156, v157
  v_cvt_pk_bf16_f32 v47, v158, v159
  v_permlane16_swap_b32_e32 v40, v42
  v_permlane16_swap_b32_e32 v41, v43
  s_nop 0
  v_permlane16_swap_b32_e32 v44, v46
  v_permlane16_swap_b32_e32 v45, v47
  v_mul_f32_e32 v160, s63, v160
  v_mul_f32_e32 v161, s63, v161
  v_mul_f32_e32 v162, s63, v162
  v_mul_f32_e32 v163, s63, v163
  v_mul_f32_e32 v164, s63, v164
  v_mul_f32_e32 v165, s63, v165
  v_mul_f32_e32 v166, s63, v166
  v_mul_f32_e32 v167, s63, v167
  v_cvt_pk_bf16_f32 v48, v160, v161
  v_cvt_pk_bf16_f32 v49, v162, v163
  v_cvt_pk_bf16_f32 v50, v164, v165
  v_cvt_pk_bf16_f32 v51, v166, v167
  v_mul_f32_e32 v168, s63, v168
  v_mul_f32_e32 v169, s63, v169
  v_mul_f32_e32 v170, s63, v170
  v_mul_f32_e32 v171, s63, v171
  v_mul_f32_e32 v172, s63, v172
  v_mul_f32_e32 v173, s63, v173
  v_mul_f32_e32 v174, s63, v174
  v_mul_f32_e32 v175, s63, v175
  v_cvt_pk_bf16_f32 v52, v168, v169
  v_cvt_pk_bf16_f32 v53, v170, v171
  v_cvt_pk_bf16_f32 v54, v172, v173
  v_cvt_pk_bf16_f32 v55, v174, v175
  v_permlane16_swap_b32_e32 v48, v50
  v_permlane16_swap_b32_e32 v49, v51
  v_mul_f32_e32 v176, s63, v176
  v_mul_f32_e32 v177, s63, v177
  v_mul_f32_e32 v178, s63, v178
  v_mul_f32_e32 v179, s63, v179
  v_mul_f32_e32 v180, s63, v180
  v_mul_f32_e32 v181, s63, v181
  v_mul_f32_e32 v182, s63, v182
  v_mul_f32_e32 v183, s63, v183
  v_cvt_pk_bf16_f32 v56, v176, v177
  v_cvt_pk_bf16_f32 v57, v178, v179
  v_cvt_pk_bf16_f32 v58, v180, v181
  v_cvt_pk_bf16_f32 v59, v182, v183
  v_permlane16_swap_b32_e32 v52, v54
  v_permlane16_swap_b32_e32 v53, v55
  v_mul_f32_e32 v184, s63, v184
  v_mul_f32_e32 v185, s63, v185
  v_mul_f32_e32 v186, s63, v186
  v_mul_f32_e32 v187, s63, v187
  v_mul_f32_e32 v188, s63, v188
  v_mul_f32_e32 v189, s63, v189
  v_mul_f32_e32 v190, s63, v190
  v_mul_f32_e32 v191, s63, v191
  v_cvt_pk_bf16_f32 v60, v184, v185
  v_cvt_pk_bf16_f32 v61, v186, v187
  v_cvt_pk_bf16_f32 v62, v188, v189
  v_cvt_pk_bf16_f32 v63, v190, v191
  v_permlane16_swap_b32_e32 v56, v58
  v_permlane16_swap_b32_e32 v57, v59
  s_nop 0
  v_permlane16_swap_b32_e32 v60, v62
  v_permlane16_swap_b32_e32 v61, v63
  v_mul_f32_e32 v192, s63, v192
  v_mul_f32_e32 v193, s63, v193
  v_mul_f32_e32 v194, s63, v194
  v_mul_f32_e32 v195, s63, v195
  v_mul_f32_e32 v196, s63, v196
  v_mul_f32_e32 v197, s63, v197
  v_mul_f32_e32 v198, s63, v198
  v_mul_f32_e32 v199, s63, v199
  v_cvt_pk_bf16_f32 v80, v192, v193
  v_cvt_pk_bf16_f32 v81, v194, v195
  v_cvt_pk_bf16_f32 v82, v196, v197
  v_cvt_pk_bf16_f32 v83, v198, v199
  v_mul_f32_e32 v200, s63, v200
  v_mul_f32_e32 v201, s63, v201
  v_mul_f32_e32 v202, s63, v202
  v_mul_f32_e32 v203, s63, v203
  v_mul_f32_e32 v204, s63, v204
  v_mul_f32_e32 v205, s63, v205
  v_mul_f32_e32 v206, s63, v206
  v_mul_f32_e32 v207, s63, v207
  v_cvt_pk_bf16_f32 v84, v200, v201
  v_cvt_pk_bf16_f32 v85, v202, v203
  v_cvt_pk_bf16_f32 v86, v204, v205
  v_cvt_pk_bf16_f32 v87, v206, v207
  v_permlane16_swap_b32_e32 v80, v82
  v_permlane16_swap_b32_e32 v81, v83
  v_mul_f32_e32 v208, s63, v208
  v_mul_f32_e32 v209, s63, v209
  v_mul_f32_e32 v210, s63, v210
  v_mul_f32_e32 v211, s63, v211
  v_mul_f32_e32 v212, s63, v212
  v_mul_f32_e32 v213, s63, v213
  v_mul_f32_e32 v214, s63, v214
  v_mul_f32_e32 v215, s63, v215
  v_cvt_pk_bf16_f32 v88, v208, v209
  v_cvt_pk_bf16_f32 v89, v210, v211
  v_cvt_pk_bf16_f32 v90, v212, v213
  v_cvt_pk_bf16_f32 v91, v214, v215
  v_permlane16_swap_b32_e32 v84, v86
  v_permlane16_swap_b32_e32 v85, v87
  v_mul_f32_e32 v216, s63, v216
  v_mul_f32_e32 v217, s63, v217
  v_mul_f32_e32 v218, s63, v218
  v_mul_f32_e32 v219, s63, v219
  v_mul_f32_e32 v220, s63, v220
  v_mul_f32_e32 v221, s63, v221
  v_mul_f32_e32 v222, s63, v222
  v_mul_f32_e32 v223, s63, v223
  v_cvt_pk_bf16_f32 v92, v216, v217
  v_cvt_pk_bf16_f32 v93, v218, v219
  v_cvt_pk_bf16_f32 v94, v220, v221
  v_cvt_pk_bf16_f32 v95, v222, v223
  v_permlane16_swap_b32_e32 v88, v90
  v_permlane16_swap_b32_e32 v89, v91
  s_nop 0
  v_permlane16_swap_b32_e32 v92, v94
  v_permlane16_swap_b32_e32 v93, v95
  v_mul_f32_e32 v224, s63, v224
  v_mul_f32_e32 v225, s63, v225
  v_mul_f32_e32 v226, s63, v226
  v_mul_f32_e32 v227, s63, v227
  v_mul_f32_e32 v228, s63, v228
  v_mul_f32_e32 v229, s63, v229
  v_mul_f32_e32 v230, s63, v230
  v_mul_f32_e32 v231, s63, v231
  v_cvt_pk_bf16_f32 v96, v224, v225
  v_cvt_pk_bf16_f32 v97, v226, v227
  v_cvt_pk_bf16_f32 v98, v228, v229
  v_cvt_pk_bf16_f32 v99, v230, v231
  v_mul_f32_e32 v232, s63, v232
  v_mul_f32_e32 v233, s63, v233
  v_mul_f32_e32 v234, s63, v234
  v_mul_f32_e32 v235, s63, v235
  v_mul_f32_e32 v236, s63, v236
  v_mul_f32_e32 v237, s63, v237
  v_mul_f32_e32 v238, s63, v238
  v_mul_f32_e32 v239, s63, v239
  v_cvt_pk_bf16_f32 v100, v232, v233
  v_cvt_pk_bf16_f32 v101, v234, v235
  v_cvt_pk_bf16_f32 v102, v236, v237
  v_cvt_pk_bf16_f32 v103, v238, v239
  v_permlane16_swap_b32_e32 v96, v98
  v_permlane16_swap_b32_e32 v97, v99
  v_mul_f32_e32 v240, s63, v240
  v_mul_f32_e32 v241, s63, v241
  v_mul_f32_e32 v242, s63, v242
  v_mul_f32_e32 v243, s63, v243
  v_mul_f32_e32 v244, s63, v244
  v_mul_f32_e32 v245, s63, v245
  v_mul_f32_e32 v246, s63, v246
  v_mul_f32_e32 v247, s63, v247
  v_cvt_pk_bf16_f32 v104, v240, v241
  v_cvt_pk_bf16_f32 v105, v242, v243
  v_cvt_pk_bf16_f32 v106, v244, v245
  v_cvt_pk_bf16_f32 v107, v246, v247
  v_permlane16_swap_b32_e32 v100, v102
  v_permlane16_swap_b32_e32 v101, v103
  v_mul_f32_e32 v248, s63, v248
  v_mul_f32_e32 v249, s63, v249
  v_mul_f32_e32 v250, s63, v250
  v_mul_f32_e32 v251, s63, v251
  v_mul_f32_e32 v252, s63, v252
  v_mul_f32_e32 v253, s63, v253
  v_mul_f32_e32 v254, s63, v254
  v_mul_f32_e32 v255, s63, v255
  v_cvt_pk_bf16_f32 v108, v248, v249
  v_cvt_pk_bf16_f32 v109, v250, v251
  v_cvt_pk_bf16_f32 v110, v252, v253
  v_cvt_pk_bf16_f32 v111, v254, v255
  v_permlane16_swap_b32_e32 v104, v106
  v_permlane16_swap_b32_e32 v105, v107
  s_nop 0
  v_permlane16_swap_b32_e32 v108, v110
  v_permlane16_swap_b32_e32 v109, v111
  s_waitcnt lgkmcnt(0)
  s_barrier
  s_mov_b32 s49, 2
  s_lshl_b32 s38, s96, 1
  v_add_u32_e32 v8, s38, v29
  s_lshl_b32 s38, s97, 1
  v_add_u32_e32 v236, s38, v29
  s_lshl_b32 s38, s98, 1
  v_add_u32_e32 v237, s38, v29
  s_lshl_b32 s38, s99, 1
  v_add_u32_e32 v238, s38, v29
  ds_read_b128 v[64:67], v8
  ds_read_b128 v[68:71], v236
  ds_read_b128 v[72:75], v237
  ds_read_b128 v[76:79], v238
  ds_read_b128 v[112:115], v8 offset:4096
  ds_read_b128 v[116:119], v236 offset:4096
  ds_read_b128 v[120:123], v237 offset:4096
  ds_read_b128 v[124:127], v238 offset:4096
  s_add_u32 s38, s84, s96
  v_add_u32_e32 v8, s38, v5
  s_add_u32 s38, s84, s97
  v_add_u32_e32 v236, s38, v5
  s_add_u32 s38, s84, s98
  v_add_u32_e32 v237, s38, v5
  s_add_u32 s38, s84, s99
  v_add_u32_e32 v238, s38, v5
  ds_read_b128 a[96:99], v8
  ds_read_b128 a[100:103], v236
  ds_read_b128 a[104:107], v237
  ds_read_b128 a[108:111], v238
  ds_read_b128 a[112:115], v8 offset:4096
  ds_read_b128 a[116:119], v236 offset:4096
  ds_read_b128 a[120:123], v237 offset:4096
  ds_read_b128 a[124:127], v238 offset:4096
  s_add_u32 s38, s85, s96
  v_add_u32_e32 v8, s38, v5
  s_add_u32 s38, s85, s97
  v_add_u32_e32 v236, s38, v5
  s_add_u32 s38, s85, s98
  v_add_u32_e32 v237, s38, v5
  s_add_u32 s38, s85, s99
  v_add_u32_e32 v238, s38, v5
  ds_read_b128 a[128:131], v8
  ds_read_b128 a[132:135], v236
  ds_read_b128 a[136:139], v237
  ds_read_b128 a[140:143], v238
  ds_read_b128 a[144:147], v8 offset:4096
  ds_read_b128 a[148:151], v236 offset:4096
  ds_read_b128 a[152:155], v237 offset:4096
  ds_read_b128 a[156:159], v238 offset:4096
  v_add_u32_e32 v9, s84, v6
  v_add_u32_e32 v10, s84, v7
  ds_read_b64_tr_b16 a[160:161], v9
  ds_read_b64_tr_b16 a[162:163], v9 offset:4096
  ds_read_b64_tr_b16 a[164:165], v10
  ds_read_b64_tr_b16 a[166:167], v10 offset:4096
  ds_read_b64_tr_b16 a[168:169], v9 offset:512
  ds_read_b64_tr_b16 a[170:171], v9 offset:4608
  ds_read_b64_tr_b16 a[172:173], v10 offset:512
  ds_read_b64_tr_b16 a[174:175], v10 offset:4608
  ds_read_b64_tr_b16 a[176:177], v9 offset:1024
  ds_read_b64_tr_b16 a[178:179], v9 offset:5120
  ds_read_b64_tr_b16 a[180:181], v10 offset:1024
  ds_read_b64_tr_b16 a[182:183], v10 offset:5120
  ds_read_b64_tr_b16 a[184:185], v9 offset:1536
  ds_read_b64_tr_b16 a[186:187], v9 offset:5632
  ds_read_b64_tr_b16 a[188:189], v10 offset:1536
  ds_read_b64_tr_b16 a[190:191], v10 offset:5632
.Litem%=:
  v_mov_b32_e32 v156, 0
  v_mov_b32_e32 v157, 0
  v_mov_b32_e32 v158, 0
  s_waitcnt lgkmcnt(0)
  v_mfma_f32_16x16x32_bf16 v[128:131], a[96:99], v[32:35], 0
  v_mfma_f32_16x16x32_bf16 v[128:131], a[100:103], v[36:39], v[128:131]
  v_mfma_f32_16x16x32_bf16 v[128:131], a[104:107], v[40:43], v[128:131]
  v_mfma_f32_16x16x32_bf16 v[128:131], a[108:111], v[44:47], v[128:131]
  v_mfma_f32_16x16x32_bf16 v[132:135], a[112:115], v[32:35], 0
  v_mfma_f32_16x16x32_bf16 v[132:135], a[116:119], v[36:39], v[132:135]
  v_mfma_f32_16x16x32_bf16 v[132:135], a[120:123], v[40:43], v[132:135]
  v_mfma_f32_16x16x32_bf16 v[132:135], a[124:127], v[44:47], v[132:135]
  v_mfma_f32_16x16x32_bf16 v[136:139], a[96:99], v[48:51], 0
  v_mfma_f32_16x16x32_bf16 v[136:139], a[100:103], v[52:55], v[136:139]
  v_mfma_f32_16x16x32_bf16 v[136:139], a[104:107], v[56:59], v[136:139]
  v_mfma_f32_16x16x32_bf16 v[136:139], a[108:111], v[60:63], v[136:139]
  v_mfma_f32_16x16x32_bf16 v[140:143], a[112:115], v[48:51], 0
  v_mfma_f32_16x16x32_bf16 v[140:143], a[116:119], v[52:55], v[140:143]
  v_mfma_f32_16x16x32_bf16 v[140:143], a[120:123], v[56:59], v[140:143]
  v_mfma_f32_16x16x32_bf16 v[140:143], a[124:127], v[60:63], v[140:143]
  v_exp_f32_e32 v128, v128
  v_exp_f32_e32 v129, v129
  v_exp_f32_e32 v130, v130
  v_exp_f32_e32 v131, v131
  v_exp_f32_e32 v132, v132
  v_exp_f32_e32 v133, v133
  v_exp_f32_e32 v134, v134
  v_exp_f32_e32 v135, v135
  s_nop 0
  v_cvt_pk_bf16_f32 v152, v128, v129
  v_cvt_pk_bf16_f32 v153, v130, v131
  v_cvt_pk_bf16_f32 v154, v132, v133
  v_cvt_pk_bf16_f32 v155, v134, v135
  v_add_f32_e32 v156, v128, v156
  v_add_f32_e32 v156, v129, v156
  v_add_f32_e32 v156, v130, v156
  v_add_f32_e32 v156, v131, v156
  v_add_f32_e32 v156, v132, v156
  v_add_f32_e32 v156, v133, v156
  v_add_f32_e32 v156, v134, v156
  v_add_f32_e32 v156, v135, v156
  v_mfma_f32_16x16x32_bf16 a[0:3], a[160:163], v[152:155], 0
  s_add_u32 s38, s86, s96
  v_add_u32_e32 v8, s38, v5
  v_exp_f32_e32 v136, v136
  v_mfma_f32_16x16x32_bf16 a[4:7], a[164:167], v[152:155], 0
  s_add_u32 s38, s86, s97
  v_add_f32_e32 v157, v136, v157
  v_mfma_f32_16x16x32_bf16 a[8:11], a[168:171], v[152:155], 0
  v_add_u32_e32 v236, s38, v5
  s_add_u32 s38, s86, s98
  v_exp_f32_e32 v137, v137
  v_mfma_f32_16x16x32_bf16 a[12:15], a[172:175], v[152:155], 0
  v_add_u32_e32 v237, s38, v5
  v_add_f32_e32 v157, v137, v157
  v_mfma_f32_16x16x32_bf16 a[16:19], a[176:179], v[152:155], 0
  s_add_u32 s38, s86, s99
  v_exp_f32_e32 v138, v138
  v_mfma_f32_16x16x32_bf16 a[20:23], a[180:183], v[152:155], 0
  v_add_u32_e32 v238, s38, v5
  v_add_u32_e32 v9, s85, v6
  v_add_f32_e32 v157, v138, v157
  v_mfma_f32_16x16x32_bf16 a[24:27], a[184:187], v[152:155], 0
  v_add_u32_e32 v10, s85, v7
  v_exp_f32_e32 v139, v139
  v_mfma_f32_16x16x32_bf16 a[28:31], a[188:191], v[152:155], 0
  ds_read_b128 a[96:99], v8
  v_add_f32_e32 v157, v139, v157
  s_sleep 1
  v_mfma_f32_16x16x32_bf16 v[144:147], a[128:131], v[32:35], 0
  ds_read_b128 a[100:103], v236
  ds_read_b128 a[104:107], v237
  v_exp_f32_e32 v140, v140
  v_mfma_f32_16x16x32_bf16 v[144:147], a[132:135], v[36:39], v[144:147]
  ds_read_b128 a[108:111], v238
  v_cvt_pk_bf16_f32 v152, v136, v137
  v_cvt_pk_bf16_f32 v153, v138, v139
  v_mfma_f32_16x16x32_bf16 v[144:147], a[136:139], v[40:43], v[144:147]
  ds_read_b64_tr_b16 a[192:193], v9
  ds_read_b64_tr_b16 a[194:195], v9 offset:4096
  v_exp_f32_e32 v141, v141
  v_mfma_f32_16x16x32_bf16 v[144:147], a[140:143], v[44:47], v[144:147]
  ds_read_b64_tr_b16 a[196:197], v10
  v_add_f32_e32 v157, v140, v157
  v_cvt_pk_bf16_f32 v154, v140, v141
  v_mfma_f32_16x16x32_bf16 v[148:151], a[144:147], v[32:35], 0
  ds_read_b64_tr_b16 a[198:199], v10 offset:4096
  v_exp_f32_e32 v142, v142
  v_mfma_f32_16x16x32_bf16 v[148:151], a[148:151], v[36:39], v[148:151]
  ds_read_b64_tr_b16 a[200:201], v9 offset:512
  ds_read_b64_tr_b16 a[202:203], v9 offset:4608
  v_add_f32_e32 v157, v141, v157
  v_add_f32_e32 v157, v142, v157
  v_mfma_f32_16x16x32_bf16 v[148:151], a[152:155], v[40:43], v[148:151]
  ds_read_b64_tr_b16 a[204:205], v10 offset:512
  v_exp_f32_e32 v143, v143
  v_mfma_f32_16x16x32_bf16 v[148:151], a[156:159], v[44:47], v[148:151]
  ds_read_b64_tr_b16 a[206:207], v10 offset:4608
  v_cvt_pk_bf16_f32 v155, v142, v143
  v_add_f32_e32 v157, v143, v157
  s_nop 0
  v_mfma_f32_16x16x32_bf16 a[32:35], a[160:163], v[152:155], 0
  ds_read_b64_tr_b16 a[208:209], v9 offset:1024
  v_exp_f32_e32 v144, v144
  v_mfma_f32_16x16x32_bf16 a[36:39], a[164:167], v[152:155], 0
  ds_read_b64_tr_b16 a[210:211], v9 offset:5120
  v_add_f32_e32 v156, v144, v156
  v_mfma_f32_16x16x32_bf16 a[40:43], a[168:171], v[152:155], 0
  ds_read_b64_tr_b16 a[212:213], v10 offset:1024
  v_exp_f32_e32 v145, v145
  v_mfma_f32_16x16x32_bf16 a[44:47], a[172:175], v[152:155], 0
  ds_read_b64_tr_b16 a[214:215], v10 offset:5120
  v_add_f32_e32 v156, v145, v156
  v_mfma_f32_16x16x32_bf16 a[48:51], a[176:179], v[152:155], 0
  ds_read_b64_tr_b16 a[216:217], v9 offset:1536
  v_exp_f32_e32 v146, v146
  v_mfma_f32_16x16x32_bf16 a[52:55], a[180:183], v[152:155], 0
  ds_read_b64_tr_b16 a[218:219], v9 offset:5632
  v_add_f32_e32 v156, v146, v156
  v_mfma_f32_16x16x32_bf16 a[56:59], a[184:187], v[152:155], 0
  ds_read_b64_tr_b16 a[220:221], v10 offset:1536
  v_exp_f32_e32 v147, v147
  v_mfma_f32_16x16x32_bf16 a[60:63], a[188:191], v[152:155], 0
  ds_read_b64_tr_b16 a[222:223], v10 offset:5632
  v_add_f32_e32 v156, v147, v156
  s_sleep 1
  v_mfma_f32_16x16x32_bf16 v[128:131], a[128:131], v[48:51], 0
  ds_read_b128 a[112:115], v8 offset:4096
  v_exp_f32_e32 v148, v148
  v_mfma_f32_16x16x32_bf16 v[128:131], a[132:135], v[52:55], v[128:131]
  ds_read_b128 a[116:119], v236 offset:4096
  v_cvt_pk_bf16_f32 v152, v144, v145
  v_cvt_pk_bf16_f32 v153, v146, v147
  v_mfma_f32_16x16x32_bf16 v[128:131], a[136:139], v[56:59], v[128:131]
  ds_read_b128 a[120:123], v237 offset:4096
  v_exp_f32_e32 v149, v149
  v_mfma_f32_16x16x32_bf16 v[128:131], a[140:143], v[60:63], v[128:131]
  ds_read_b128 a[124:127], v238 offset:4096
  v_add_f32_e32 v156, v148, v156
  v_cvt_pk_bf16_f32 v154, v148, v149
  v_mfma_f32_16x16x32_bf16 v[132:135], a[144:147], v[48:51], 0
  v_exp_f32_e32 v150, v150
  v_mfma_f32_16x16x32_bf16 v[132:135], a[148:151], v[52:55], v[132:135]
  v_add_f32_e32 v156, v149, v156
  v_add_f32_e32 v156, v150, v156
  v_mfma_f32_16x16x32_bf16 v[132:135], a[152:155], v[56:59], v[132:135]
  v_exp_f32_e32 v151, v151
  v_mfma_f32_16x16x32_bf16 v[132:135], a[156:159], v[60:63], v[132:135]
  s_waitcnt lgkmcnt(1)
  v_cvt_pk_bf16_f32 v155, v150, v151
  v_add_f32_e32 v156, v151, v156
  s_nop 0
  v_mfma_f32_16x16x32_bf16 a[0:3], a[192:195], v[152:155], a[0:3]
  s_add_u32 s38, s87, s96
  v_add_u32_e32 v8, s38, v5
  v_exp_f32_e32 v128, v128
  v_mfma_f32_16x16x32_bf16 a[4:7], a[196:199], v[152:155], a[4:7]
  s_add_u32 s38, s87, s97
  v_add_f32_e32 v157, v128, v157
  v_mfma_f32_16x16x32_bf16 a[8:11], a[200:203], v[152:155], a[8:11]
  v_add_u32_e32 v236, s38, v5
  s_add_u32 s38, s87, s98
  v_exp_f32_e32 v129, v129
  v_mfma_f32_16x16x32_bf16 a[12:15], a[204:207], v[152:155], a[12:15]
  v_add_u32_e32 v237, s38, v5
  v_add_f32_e32 v157, v129, v157
  v_mfma_f32_16x16x32_bf16 a[16:19], a[208:211], v[152:155], a[16:19]
  s_add_u32 s38, s87, s99
  v_exp_f32_e32 v130, v130
  v_mfma_f32_16x16x32_bf16 a[20:23], a[212:215], v[152:155], a[20:23]
  v_add_u32_e32 v238, s38, v5
  v_add_u32_e32 v9, s86, v6
  v_add_f32_e32 v157, v130, v157
  v_mfma_f32_16x16x32_bf16 a[24:27], a[216:219], v[152:155], a[24:27]
  v_add_u32_e32 v10, s86, v7
  v_exp_f32_e32 v131, v131
  v_mfma_f32_16x16x32_bf16 a[28:31], a[220:223], v[152:155], a[28:31]
  ds_read_b128 a[128:131], v8
  v_add_f32_e32 v157, v131, v157
  s_sleep 1
  v_mfma_f32_16x16x32_bf16 v[136:139], a[96:99], v[32:35], 0
  ds_read_b128 a[132:135], v236
  ds_read_b128 a[136:139], v237
  v_exp_f32_e32 v132, v132
  v_mfma_f32_16x16x32_bf16 v[136:139], a[100:103], v[36:39], v[136:139]
  ds_read_b128 a[140:143], v238
  v_cvt_pk_bf16_f32 v152, v128, v129
  v_cvt_pk_bf16_f32 v153, v130, v131
  v_mfma_f32_16x16x32_bf16 v[136:139], a[104:107], v[40:43], v[136:139]
  ds_read_b64_tr_b16 a[160:161], v9
  ds_read_b64_tr_b16 a[162:163], v9 offset:4096
  v_exp_f32_e32 v133, v133
  v_mfma_f32_16x16x32_bf16 v[136:139], a[108:111], v[44:47], v[136:139]
  ds_read_b64_tr_b16 a[164:165], v10
  v_add_f32_e32 v157, v132, v157
  v_cvt_pk_bf16_f32 v154, v132, v133
  v_mfma_f32_16x16x32_bf16 v[140:143], a[112:115], v[32:35], 0
  ds_read_b64_tr_b16 a[166:167], v10 offset:4096
  v_exp_f32_e32 v134, v134
  v_mfma_f32_16x16x32_bf16 v[140:143], a[116:119], v[36:39], v[140:143]
  ds_read_b64_tr_b16 a[168:169], v9 offset:512
  ds_read_b64_tr_b16 a[170:171], v9 offset:4608
  v_add_f32_e32 v157, v133, v157
  v_add_f32_e32 v157, v134, v157
  v_mfma_f32_16x16x32_bf16 v[140:143], a[120:123], v[40:43], v[140:143]
  s_waitcnt lgkmcnt(6)
  ds_read_b64_tr_b16 a[172:173], v10 offset:512
  v_exp_f32_e32 v135, v135
  v_mfma_f32_16x16x32_bf16 v[140:143], a[124:127], v[44:47], v[140:143]
  ds_read_b64_tr_b16 a[174:175], v10 offset:4608
  v_cvt_pk_bf16_f32 v155, v134, v135
  v_add_f32_e32 v157, v135, v157
  s_nop 0
  v_mfma_f32_16x16x32_bf16 a[32:35], a[192:195], v[152:155], a[32:35]
  ds_read_b128 a[144:147], v8 offset:4096
  v_exp_f32_e32 v136, v136
  v_mfma_f32_16x16x32_bf16 a[36:39], a[196:199], v[152:155], a[36:39]
  ds_read_b128 a[148:151], v236 offset:4096
  v_add_f32_e32 v156, v136, v156
  v_mfma_f32_16x16x32_bf16 a[40:43], a[200:203], v[152:155], a[40:43]
  s_add_u32 s39, s39, 1
  ds_read_b128 a[152:155], v237 offset:4096
  v_exp_f32_e32 v137, v137
  v_mfma_f32_16x16x32_bf16 a[44:47], a[204:207], v[152:155], a[44:47]
  s_lshr_b32 s50, s39, 3
  ds_read_b128 a[156:159], v238 offset:4096
  v_add_f32_e32 v156, v137, v156
  v_mfma_f32_16x16x32_bf16 a[48:51], a[208:211], v[152:155], a[48:51]
  s_lshl_b32 s50, s50, 20
  ds_read_b64_tr_b16 a[176:177], v9 offset:1024
  v_exp_f32_e32 v138, v138
  v_mfma_f32_16x16x32_bf16 a[52:55], a[212:215], v[152:155], a[52:55]
  s_add_u32 s51, s39, s62
  ds_read_b64_tr_b16 a[178:179], v9 offset:5120
  v_add_f32_e32 v156, v138, v156
  v_mfma_f32_16x16x32_bf16 a[56:59], a[216:219], v[152:155], a[56:59]
  s_and_b32 s51, s51, 7
  ds_read_b64_tr_b16 a[180:181], v10 offset:1024
  v_exp_f32_e32 v139, v139
  v_mfma_f32_16x16x32_bf16 a[60:63], a[220:223], v[152:155], a[60:63]
  s_lshl_b32 s51, s51, 15
  ds_read_b64_tr_b16 a[182:183], v10 offset:5120
  v_add_f32_e32 v156, v139, v156
  s_sleep 1
  v_mfma_f32_16x16x32_bf16 v[144:147], a[96:99], v[48:51], 0
  s_add_u32 s40, s50, s51
  ds_read_b64_tr_b16 a[184:185], v9 offset:1536
  v_exp_f32_e32 v140, v140
  v_mfma_f32_16x16x32_bf16 v[144:147], a[100:103], v[52:55], v[144:147]
  s_add_u32 s41, s40, 8192
  ds_read_b64_tr_b16 a[186:187], v9 offset:5632
  v_cvt_pk_bf16_f32 v152, v136, v137
  v_cvt_pk_bf16_f32 v153, v138, v139
  v_mfma_f32_16x16x32_bf16 v[144:147], a[104:107], v[56:59], v[144:147]
  s_add_u32 s42, s40, 16384
  ds_read_b64_tr_b16 a[188:189], v10 offset:1536
  v_exp_f32_e32 v141, v141
  v_mfma_f32_16x16x32_bf16 v[144:147], a[108:111], v[60:63], v[144:147]
  s_add_u32 s43, s40, 24576
  ds_read_b64_tr_b16 a[190:191], v10 offset:5632
  v_add_f32_e32 v156, v140, v156
  v_cvt_pk_bf16_f32 v154, v140, v141
  v_mfma_f32_16x16x32_bf16 v[148:151], a[112:115], v[48:51], 0
  s_xor_b32 s37, s37, 0x10000
  v_exp_f32_e32 v142, v142
  v_mfma_f32_16x16x32_bf16 v[148:151], a[116:119], v[52:55], v[148:151]
  v_add_f32_e32 v156, v141, v156
  v_add_f32_e32 v156, v142, v156
  v_mfma_f32_16x16x32_bf16 v[148:151], a[120:123], v[56:59], v[148:151]
  v_exp_f32_e32 v143, v143
  v_mfma_f32_16x16x32_bf16 v[148:151], a[124:127], v[60:63], v[148:151]
  s_waitcnt lgkmcnt(1)
  v_cvt_pk_bf16_f32 v155, v142, v143
  v_add_f32_e32 v156, v143, v156
  s_nop 0
  v_mfma_f32_16x16x32_bf16 a[0:3], a[160:163], v[152:155], a[0:3]
  v_exp_f32_e32 v144, v144
  s_waitcnt lgkmcnt(0)
  s_barrier
  v_mfma_f32_16x16x32_bf16 a[4:7], a[164:167], v[152:155], a[4:7]
  s_mov_b32 m0, s37
  v_add_u32_e32 v9, s87, v6
  v_add_f32_e32 v157, v144, v157
  v_mfma_f32_16x16x32_bf16 a[8:11], a[168:171], v[152:155], a[8:11]
  v_add_u32_e32 v10, s87, v7
  buffer_load_dwordx4 v1, s[16:19], s40 offen nt lds
  v_exp_f32_e32 v145, v145
  v_mfma_f32_16x16x32_bf16 a[12:15], a[172:175], v[152:155], a[12:15]
  ds_read_b64_tr_b16 a[192:193], v9
  v_add_f32_e32 v157, v145, v157
  v_mfma_f32_16x16x32_bf16 a[16:19], a[176:179], v[152:155], a[16:19]
  ds_read_b64_tr_b16 a[194:195], v9 offset:4096
  buffer_load_dwordx4 v2, s[16:19], s40 offen offset:1024 nt lds
  v_exp_f32_e32 v146, v146
  v_mfma_f32_16x16x32_bf16 a[20:23], a[180:183], v[152:155], a[20:23]
  s_add_u32 m0, s37, 16384
  v_add_f32_e32 v157, v146, v157
  v_mfma_f32_16x16x32_bf16 a[24:27], a[184:187], v[152:155], a[24:27]
  ds_read_b64_tr_b16 a[196:197], v10
  buffer_load_dwordx4 v1, s[16:19], s41 offen nt lds
  v_exp_f32_e32 v147, v147
  v_mfma_f32_16x16x32_bf16 a[28:31], a[188:191], v[152:155], a[28:31]
  ds_read_b64_tr_b16 a[198:199], v10 offset:4096
  v_add_f32_e32 v157, v147, v157
  s_sleep 1
  v_mfma_f32_16x16x32_bf16 v[128:131], a[128:131], v[32:35], 0
  ds_read_b64_tr_b16 a[200:201], v9 offset:512
  buffer_load_dwordx4 v2, s[16:19], s41 offen offset:1024 nt lds
  v_exp_f32_e32 v148, v148
  v_mfma_f32_16x16x32_bf16 v[128:131], a[132:135], v[36:39], v[128:131]
  s_add_u32 m0, s37, 32768
  ds_read_b64_tr_b16 a[202:203], v9 offset:4608
  v_cvt_pk_bf16_f32 v152, v144, v145
  v_cvt_pk_bf16_f32 v153, v146, v147
  v_mfma_f32_16x16x32_bf16 v[128:131], a[136:139], v[40:43], v[128:131]
  buffer_load_dwordx4 v1, s[16:19], s42 offen nt lds
  v_exp_f32_e32 v149, v149
  v_mfma_f32_16x16x32_bf16 v[128:131], a[140:143], v[44:47], v[128:131]
  ds_read_b64_tr_b16 a[204:205], v10 offset:512
  v_add_f32_e32 v157, v148, v157
  v_cvt_pk_bf16_f32 v154, v148, v149
  v_mfma_f32_16x16x32_bf16 v[132:135], a[144:147], v[32:35], 0
  ds_read_b64_tr_b16 a[206:207], v10 offset:4608
  buffer_load_dwordx4 v2, s[16:19], s42 offen offset:1024 nt lds
  v_exp_f32_e32 v150, v150
  v_mfma_f32_16x16x32_bf16 v[132:135], a[148:151], v[36:39], v[132:135]
  s_add_u32 m0, s37, 49152
  ds_read_b64_tr_b16 a[208:209], v9 offset:1024
  v_add_f32_e32 v157, v149, v157
  v_add_f32_e32 v157, v150, v157
  v_mfma_f32_16x16x32_bf16 v[132:135], a[152:155], v[40:43], v[132:135]
  ds_read_b64_tr_b16 a[210:211], v9 offset:5120
  buffer_load_dwordx4 v1, s[16:19], s43 offen nt lds
  v_exp_f32_e32 v151, v151
  v_mfma_f32_16x16x32_bf16 v[132:135], a[156:159], v[44:47], v[132:135]
  v_cvt_pk_bf16_f32 v155, v150, v151
  v_add_f32_e32 v157, v151, v157
  s_nop 0
  v_mfma_f32_16x16x32_bf16 a[32:35], a[160:163], v[152:155], a[32:35]
  ds_read_b64_tr_b16 a[212:213], v10 offset:1024
  v_exp_f32_e32 v128, v128
  v_mfma_f32_16x16x32_bf16 a[36:39], a[164:167], v[152:155], a[36:39]
  ds_read_b64_tr_b16 a[214:215], v10 offset:5120
  buffer_load_dwordx4 v2, s[16:19], s43 offen offset:1024 nt lds
  v_add_f32_e32 v156, v128, v156
  v_mfma_f32_16x16x32_bf16 a[40:43], a[168:171], v[152:155], a[40:43]
  s_xor_b32 s84, s84, 0x10000
  ds_read_b64_tr_b16 a[216:217], v9 offset:1536
  v_exp_f32_e32 v129, v129
  v_mfma_f32_16x16x32_bf16 a[44:47], a[172:175], v[152:155], a[44:47]
  v_add_f32_e32 v156, v129, v156
  v_mfma_f32_16x16x32_bf16 a[48:51], a[176:179], v[152:155], a[48:51]
  ds_read_b64_tr_b16 a[218:219], v9 offset:5632
  v_exp_f32_e32 v130, v130
  v_mfma_f32_16x16x32_bf16 a[52:55], a[180:183], v[152:155], a[52:55]
  ds_read_b64_tr_b16 a[220:221], v10 offset:1536
  v_add_f32_e32 v156, v130, v156
  v_mfma_f32_16x16x32_bf16 a[56:59], a[184:187], v[152:155], a[56:59]
  ds_read_b64_tr_b16 a[222:223], v10 offset:5632
  v_exp_f32_e32 v131, v131
  v_mfma_f32_16x16x32_bf16 a[60:63], a[188:191], v[152:155], a[60:63]
  v_add_f32_e32 v156, v131, v156
  s_sleep 1
  v_mfma_f32_16x16x32_bf16 v[136:139], a[128:131], v[48:51], 0
  v_exp_f32_e32 v132, v132
  v_mfma_f32_16x16x32_bf16 v[136:139], a[132:135], v[52:55], v[136:139]
  v_cvt_pk_bf16_f32 v152, v128, v129
  v_cvt_pk_bf16_f32 v153, v130, v131
  v_mfma_f32_16x16x32_bf16 v[136:139], a[136:139], v[56:59], v[136:139]
  v_exp_f32_e32 v133, v133
  v_mfma_f32_16x16x32_bf16 v[136:139], a[140:143], v[60:63], v[136:139]
  v_add_f32_e32 v156, v132, v156
  v_cvt_pk_bf16_f32 v154, v132, v133
  v_mfma_f32_16x16x32_bf16 v[140:143], a[144:147], v[48:51], 0
  v_exp_f32_e32 v134, v134
  v_mfma_f32_16x16x32_bf16 v[140:143], a[148:151], v[52:55], v[140:143]
  v_add_f32_e32 v156, v133, v156
  v_add_f32_e32 v156, v134, v156
  v_mfma_f32_16x16x32_bf16 v[140:143], a[152:155], v[56:59], v[140:143]
  s_waitcnt vmcnt(8) lgkmcnt(0)
  v_exp_f32_e32 v135, v135
  v_mfma_f32_16x16x32_bf16 v[140:143], a[156:159], v[60:63], v[140:143]
  v_cvt_pk_bf16_f32 v155, v134, v135
  v_add_f32_e32 v156, v135, v156
  s_nop 0
  s_barrier
  v_mfma_f32_16x16x32_bf16 a[0:3], a[192:195], v[152:155], a[0:3]
  s_add_u32 m0, s37, 8192
  s_add_u32 s38, s84, s96
  v_add_u32_e32 v8, s38, v5
  v_exp_f32_e32 v136, v136
  v_mfma_f32_16x16x32_bf16 a[4:7], a[196:199], v[152:155], a[4:7]
  s_add_u32 s38, s84, s97
  v_add_u32_e32 v236, s38, v5
  buffer_load_dwordx4 v3, s[20:23], s40 offen nt lds
  v_add_f32_e32 v157, v136, v157
  v_mfma_f32_16x16x32_bf16 a[8:11], a[200:203], v[152:155], a[8:11]
  s_xor_b32 s85, s85, 0x10000
  s_add_u32 s38, s84, s98
  v_exp_f32_e32 v137, v137
  v_mfma_f32_16x16x32_bf16 a[12:15], a[204:207], v[152:155], a[12:15]
  v_add_u32_e32 v237, s38, v5
  s_add_u32 s38, s84, s99
  buffer_load_dwordx4 v4, s[20:23], s40 offen offset:1024 nt lds
  v_add_f32_e32 v157, v137, v157
  v_mfma_f32_16x16x32_bf16 a[16:19], a[208:211], v[152:155], a[16:19]
  s_add_u32 m0, s37, 24576
  v_add_u32_e32 v238, s38, v5
  v_exp_f32_e32 v138, v138
  v_mfma_f32_16x16x32_bf16 a[20:23], a[212:215], v[152:155], a[20:23]
  ds_read_b128 a[96:99], v8
  ds_read_b128 a[100:103], v236
  buffer_load_dwordx4 v3, s[20:23], s41 offen nt lds
  v_add_f32_e32 v157, v138, v157
  v_mfma_f32_16x16x32_bf16 a[24:27], a[216:219], v[152:155], a[24:27]
  ds_read_b128 a[104:107], v237
  ds_read_b128 a[108:111], v238
  v_exp_f32_e32 v139, v139
  v_mfma_f32_16x16x32_bf16 a[28:31], a[220:223], v[152:155], a[28:31]
  ds_read_b128 a[112:115], v8 offset:4096
  buffer_load_dwordx4 v4, s[20:23], s41 offen offset:1024 nt lds
  v_add_f32_e32 v157, v139, v157
  s_sleep 1
  v_mfma_f32_16x16x32_bf16 v[144:147], a[128:131], v[64:67], 0
  s_add_u32 m0, s37, 40960
  ds_read_b128 a[116:119], v236 offset:4096
  ds_read_b128 a[120:123], v237 offset:4096
  v_exp_f32_e32 v140, v140
  v_mfma_f32_16x16x32_bf16 v[144:147], a[132:135], v[68:71], v[144:147]
  ds_read_b128 a[124:127], v238 offset:4096
  buffer_load_dwordx4 v3, s[20:23], s42 offen nt lds
  v_cvt_pk_bf16_f32 v152, v136, v137
  v_cvt_pk_bf16_f32 v153, v138, v139
  v_mfma_f32_16x16x32_bf16 v[144:147], a[136:139], v[72:75], v[144:147]
  v_exp_f32_e32 v141, v141
  v_mfma_f32_16x16x32_bf16 v[144:147], a[140:143], v[76:79], v[144:147]
  buffer_load_dwordx4 v4, s[20:23], s42 offen offset:1024 nt lds
  v_add_f32_e32 v157, v140, v157
  v_cvt_pk_bf16_f32 v154, v140, v141
  v_mfma_f32_16x16x32_bf16 v[148:151], a[144:147], v[64:67], 0
  s_add_u32 m0, s37, 57344
  v_exp_f32_e32 v142, v142
  v_mfma_f32_16x16x32_bf16 v[148:151], a[148:151], v[68:71], v[148:151]
  buffer_load_dwordx4 v3, s[20:23], s43 offen nt lds
  v_add_f32_e32 v157, v141, v157
  v_add_f32_e32 v157, v142, v157
  v_mfma_f32_16x16x32_bf16 v[148:151], a[152:155], v[72:75], v[148:151]
  v_exp_f32_e32 v143, v143
  v_mfma_f32_16x16x32_bf16 v[148:151], a[156:159], v[76:79], v[148:151]
  buffer_load_dwordx4 v4, s[20:23], s43 offen offset:1024 nt lds
  v_cvt_pk_bf16_f32 v155, v142, v143
  v_add_f32_e32 v157, v143, v157
  s_nop 0
  v_mfma_f32_16x16x32_bf16 a[32:35], a[192:195], v[152:155], a[32:35]
  s_add_u32 s38, s85, s96
  v_add_u32_e32 v8, s38, v5
  v_exp_f32_e32 v144, v144
  v_mfma_f32_16x16x32_bf16 a[36:39], a[196:199], v[152:155], a[36:39]
  s_add_u32 s38, s85, s97
  v_add_f32_e32 v158, v144, v158
  v_mfma_f32_16x16x32_bf16 a[40:43], a[200:203], v[152:155], a[40:43]
  s_xor_b32 s87, s87, 0x10000
  v_add_u32_e32 v236, s38, v5
  s_add_u32 s38, s85, s98
  v_exp_f32_e32 v145, v145
  v_mfma_f32_16x16x32_bf16 a[44:47], a[204:207], v[152:155], a[44:47]
  v_add_u32_e32 v237, s38, v5
  v_add_f32_e32 v158, v145, v158
  v_mfma_f32_16x16x32_bf16 a[48:51], a[208:211], v[152:155], a[48:51]
  s_add_u32 s38, s85, s99
  v_exp_f32_e32 v146, v146
  v_mfma_f32_16x16x32_bf16 a[52:55], a[212:215], v[152:155], a[52:55]
  v_add_u32_e32 v238, s38, v5
  v_add_u32_e32 v9, s84, v6
  v_add_f32_e32 v158, v146, v158
  v_mfma_f32_16x16x32_bf16 a[56:59], a[216:219], v[152:155], a[56:59]
  v_add_u32_e32 v10, s84, v7
  v_exp_f32_e32 v147, v147
  v_mfma_f32_16x16x32_bf16 a[60:63], a[220:223], v[152:155], a[60:63]
  s_waitcnt lgkmcnt(0)
  ds_read_b64_tr_b16 a[160:161], v9
  v_add_f32_e32 v158, v147, v158
  s_sleep 1
  v_mfma_f32_16x16x32_bf16 v[128:131], a[96:99], v[32:35], 0
  ds_read_b64_tr_b16 a[162:163], v9 offset:4096
  ds_read_b64_tr_b16 a[164:165], v10
  v_exp_f32_e32 v148, v148
  v_mfma_f32_16x16x32_bf16 v[128:131], a[100:103], v[36:39], v[128:131]
  ds_read_b64_tr_b16 a[166:167], v10 offset:4096
  v_cvt_pk_bf16_f32 v152, v144, v145
  v_cvt_pk_bf16_f32 v153, v146, v147
  v_mfma_f32_16x16x32_bf16 v[128:131], a[104:107], v[40:43], v[128:131]
  ds_read_b64_tr_b16 a[168:169], v9 offset:512
  ds_read_b64_tr_b16 a[170:171], v9 offset:4608
  v_exp_f32_e32 v149, v149
  v_mfma_f32_16x16x32_bf16 v[128:131], a[108:111], v[44:47], v[128:131]
  ds_read_b64_tr_b16 a[172:173], v10 offset:512
  v_add_f32_e32 v158, v148, v158
  v_cvt_pk_bf16_f32 v154, v148, v149
  v_mfma_f32_16x16x32_bf16 v[132:135], a[112:115], v[32:35], 0
  ds_read_b64_tr_b16 a[174:175], v10 offset:4608
  v_exp_f32_e32 v150, v150
  v_mfma_f32_16x16x32_bf16 v[132:135], a[116:119], v[36:39], v[132:135]
  ds_read_b64_tr_b16 a[176:177], v9 offset:1024
  ds_read_b64_tr_b16 a[178:179], v9 offset:5120
  v_add_f32_e32 v158, v149, v158
  v_add_f32_e32 v158, v150, v158
  v_mfma_f32_16x16x32_bf16 v[132:135], a[120:123], v[40:43], v[132:135]
  ds_read_b128 a[128:131], v8
  v_exp_f32_e32 v151, v151
  v_mfma_f32_16x16x32_bf16 v[132:135], a[124:127], v[44:47], v[132:135]
  ds_read_b128 a[132:135], v236
  v_cvt_pk_bf16_f32 v155, v150, v151
  v_add_f32_e32 v158, v151, v158
  s_nop 0
  v_mfma_f32_16x16x32_bf16 a[64:67], a[192:195], v[152:155], 0
  ds_read_b64_tr_b16 a[180:181], v10 offset:1024
  v_exp_f32_e32 v128, v128
  v_mfma_f32_16x16x32_bf16 a[68:71], a[196:199], v[152:155], 0
  ds_read_b64_tr_b16 a[182:183], v10 offset:5120
  v_add_f32_e32 v156, v128, v156
  v_mfma_f32_16x16x32_bf16 a[72:75], a[200:203], v[152:155], 0
  s_xor_b32 s86, s86, 0x10000
  ds_read_b64_tr_b16 a[184:185], v9 offset:1536
  v_exp_f32_e32 v129, v129
  v_mfma_f32_16x16x32_bf16 a[76:79], a[204:207], v[152:155], 0
  ds_read_b64_tr_b16 a[186:187], v9 offset:5632
  v_add_f32_e32 v156, v129, v156
  v_mfma_f32_16x16x32_bf16 a[80:83], a[208:211], v[152:155], 0
  ds_read_b64_tr_b16 a[188:189], v10 offset:1536
  v_exp_f32_e32 v130, v130
  v_mfma_f32_16x16x32_bf16 a[84:87], a[212:215], v[152:155], 0
  ds_read_b64_tr_b16 a[190:191], v10 offset:5632
  v_add_f32_e32 v156, v130, v156
  v_mfma_f32_16x16x32_bf16 a[88:91], a[216:219], v[152:155], 0
  ds_read_b128 a[136:139], v237
  v_exp_f32_e32 v131, v131
  v_mfma_f32_16x16x32_bf16 a[92:95], a[220:223], v[152:155], 0
  ds_read_b128 a[140:143], v238
  v_add_f32_e32 v156, v131, v156
  s_sleep 1
  v_mfma_f32_16x16x32_bf16 v[136:139], a[96:99], v[48:51], 0
  ds_read_b128 a[144:147], v8 offset:4096
  v_exp_f32_e32 v132, v132
  v_mfma_f32_16x16x32_bf16 v[136:139], a[100:103], v[52:55], v[136:139]
  ds_read_b128 a[148:151], v236 offset:4096
  v_cvt_pk_bf16_f32 v152, v128, v129
  v_cvt_pk_bf16_f32 v153, v130, v131
  v_mfma_f32_16x16x32_bf16 v[136:139], a[104:107], v[56:59], v[136:139]
  ds_read_b128 a[152:155], v237 offset:4096
  v_exp_f32_e32 v133, v133
  v_mfma_f32_16x16x32_bf16 v[136:139], a[108:111], v[60:63], v[136:139]
  ds_read_b128 a[156:159], v238 offset:4096
  v_add_f32_e32 v156, v132, v156
  v_cvt_pk_bf16_f32 v154, v132, v133
  v_mfma_f32_16x16x32_bf16 v[140:143], a[112:115], v[48:51], 0
  v_exp_f32_e32 v134, v134
  v_mfma_f32_16x16x32_bf16 v[140:143], a[116:119], v[52:55], v[140:143]
  v_add_f32_e32 v156, v133, v156
  v_add_f32_e32 v156, v134, v156
  v_mfma_f32_16x16x32_bf16 v[140:143], a[120:123], v[56:59], v[140:143]
  v_exp_f32_e32 v135, v135
  v_mfma_f32_16x16x32_bf16 v[140:143], a[124:127], v[60:63], v[140:143]
  s_waitcnt lgkmcnt(1)
  v_cvt_pk_bf16_f32 v155, v134, v135
  v_add_f32_e32 v156, v135, v156
  s_nop 0
  s_mov_b32 s44, 6
.Lbody%=:
  s_cmp_eq_u32 s45, 0
  s_cbranch_scc1 .Lbody2%=
  s_cmp_eq_u32 s44, 6
  s_cbranch_scc1 .Lsig0%=
  s_cmp_eq_u32 s44, 1
  s_cbranch_scc0 .Lbody2%=
  s_mov_b64 exec, 0xff
  global_load_dwordx2 v[246:247], v26, s[92:93] offset:16 sc1
  s_mov_b64 exec, -1
  s_mov_b32 s49, 0
  s_mov_b64 s[68:69], s[80:81]
  s_mov_b32 s70, 5242880
  s_mov_b32 s71, 0x20000
  s_and_b32 s76, %[gid], 3
  s_lshr_b32 s77, %[gid], 3
  s_lshl_b32 s76, s76, 5
  s_add_u32 s76, s76, s77
  s_lshl_b32 s76, s76, 2
  s_add_u32 s76, s76, s36
  s_mul_i32 s76, s76, 10240
  s_add_u32 s77, s76, 2048
  v_lshlrev_b32_e32 v248, 3, v0
  buffer_load_dword v249, v248, s[68:71], s76 offen
  buffer_load_dword v249, v248, s[68:71], s77 offen
.Lbody2%=:
  v_mfma_f32_16x16x32_bf16 a[0:3], a[160:163], v[152:155], a[0:3]
  s_add_u32 s38, s86, s96
  v_add_u32_e32 v8, s38, v5
  v_exp_f32_e32 v136, v136
  v_mfma_f32_16x16x32_bf16 a[4:7], a[164:167], v[152:155], a[4:7]
  s_add_u32 s38, s86, s97
  v_add_f32_e32 v157, v136, v157
  v_mfma_f32_16x16x32_bf16 a[8:11], a[168:171], v[152:155], a[8:11]
  v_add_u32_e32 v236, s38, v5
  s_add_u32 s38, s86, s98
  v_exp_f32_e32 v137, v137
  v_mfma_f32_16x16x32_bf16 a[12:15], a[172:175], v[152:155], a[12:15]
  v_add_u32_e32 v237, s38, v5
  v_add_f32_e32 v157, v137, v157
  v_mfma_f32_16x16x32_bf16 a[16:19], a[176:179], v[152:155], a[16:19]
  s_add_u32 s38, s86, s99
  v_exp_f32_e32 v138, v138
  v_mfma_f32_16x16x32_bf16 a[20:23], a[180:183], v[152:155], a[20:23]
  v_add_u32_e32 v238, s38, v5
  v_add_u32_e32 v9, s85, v6
  v_add_f32_e32 v157, v138, v157
  v_mfma_f32_16x16x32_bf16 a[24:27], a[184:187], v[152:155], a[24:27]
  v_add_u32_e32 v10, s85, v7
  v_exp_f32_e32 v139, v139
  v_mfma_f32_16x16x32_bf16 a[28:31], a[188:191], v[152:155], a[28:31]
  ds_read_b128 a[96:99], v8
  v_add_f32_e32 v157, v139, v157
  s_sleep 1
  v_mfma_f32_16x16x32_bf16 v[144:147], a[128:131], v[32:35], 0
  ds_read_b128 a[100:103], v236
  ds_read_b128 a[104:107], v237
  v_exp_f32_e32 v140, v140
  v_mfma_f32_16x16x32_bf16 v[144:147], a[132:135], v[36:39], v[144:147]
  ds_read_b128 a[108:111], v238
  v_cvt_pk_bf16_f32 v152, v136, v137
  v_cvt_pk_bf16_f32 v153, v138, v139
  v_mfma_f32_16x16x32_bf16 v[144:147], a[136:139], v[40:43], v[144:147]
  ds_read_b64_tr_b16 a[192:193], v9
  ds_read_b64_tr_b16 a[194:195], v9 offset:4096
  v_exp_f32_e32 v141, v141
  v_mfma_f32_16x16x32_bf16 v[144:147], a[140:143], v[44:47], v[144:147]
  ds_read_b64_tr_b16 a[196:197], v10
  v_add_f32_e32 v157, v140, v157
  v_cvt_pk_bf16_f32 v154, v140, v141
  v_mfma_f32_16x16x32_bf16 v[148:151], a[144:147], v[32:35], 0
  ds_read_b64_tr_b16 a[198:199], v10 offset:4096
  v_exp_f32_e32 v142, v142
  v_mfma_f32_16x16x32_bf16 v[148:151], a[148:151], v[36:39], v[148:151]
  ds_read_b64_tr_b16 a[200:201], v9 offset:512
  ds_read_b64_tr_b16 a[202:203], v9 offset:4608
  v_add_f32_e32 v157, v141, v157
  v_add_f32_e32 v157, v142, v157
  v_mfma_f32_16x16x32_bf16 v[148:151], a[152:155], v[40:43], v[148:151]
  s_waitcnt lgkmcnt(6)
  ds_read_b64_tr_b16 a[204:205], v10 offset:512
  v_exp_f32_e32 v143, v143
  v_mfma_f32_16x16x32_bf16 v[148:151], a[156:159], v[44:47], v[148:151]
  ds_read_b64_tr_b16 a[206:207], v10 offset:4608
  v_cvt_pk_bf16_f32 v155, v142, v143
  v_add_f32_e32 v157, v143, v157
  s_nop 0
  v_mfma_f32_16x16x32_bf16 a[32:35], a[160:163], v[152:155], a[32:35]
  ds_read_b64_tr_b16 a[208:209], v9 offset:1024
  v_exp_f32_e32 v144, v144
  v_mfma_f32_16x16x32_bf16 a[36:39], a[164:167], v[152:155], a[36:39]
  ds_read_b64_tr_b16 a[210:211], v9 offset:5120
  v_add_f32_e32 v156, v144, v156
  v_mfma_f32_16x16x32_bf16 a[40:43], a[168:171], v[152:155], a[40:43]
  ds_read_b64_tr_b16 a[212:213], v10 offset:1024
  v_exp_f32_e32 v145, v145
  v_mfma_f32_16x16x32_bf16 a[44:47], a[172:175], v[152:155], a[44:47]
  ds_read_b64_tr_b16 a[214:215], v10 offset:5120
  v_add_f32_e32 v156, v145, v156
  v_mfma_f32_16x16x32_bf16 a[48:51], a[176:179], v[152:155], a[48:51]
  ds_read_b64_tr_b16 a[216:217], v9 offset:1536
  v_exp_f32_e32 v146, v146
  v_mfma_f32_16x16x32_bf16 a[52:55], a[180:183], v[152:155], a[52:55]
  ds_read_b64_tr_b16 a[218:219], v9 offset:5632
  v_add_f32_e32 v156, v146, v156
  v_mfma_f32_16x16x32_bf16 a[56:59], a[184:187], v[152:155], a[56:59]
  ds_read_b64_tr_b16 a[220:221], v10 offset:1536
  v_exp_f32_e32 v147, v147
  v_mfma_f32_16x16x32_bf16 a[60:63], a[188:191], v[152:155], a[60:63]
  ds_read_b64_tr_b16 a[222:223], v10 offset:5632
  v_add_f32_e32 v156, v147, v156
  s_sleep 1
  v_mfma_f32_16x16x32_bf16 v[128:131], a[128:131], v[48:51], 0
  ds_read_b128 a[112:115], v8 offset:4096
  v_exp_f32_e32 v148, v148
  v_mfma_f32_16x16x32_bf16 v[128:131], a[132:135], v[52:55], v[128:131]
  ds_read_b128 a[116:119], v236 offset:4096
  v_cvt_pk_bf16_f32 v152, v144, v145
  v_cvt_pk_bf16_f32 v153, v146, v147
  v_mfma_f32_16x16x32_bf16 v[128:131], a[136:139], v[56:59], v[128:131]
  ds_read_b128 a[120:123], v237 offset:4096
  v_exp_f32_e32 v149, v149
  v_mfma_f32_16x16x32_bf16 v[128:131], a[140:143], v[60:63], v[128:131]
  ds_read_b128 a[124:127], v238 offset:4096
  v_add_f32_e32 v156, v148, v156
  v_cvt_pk_bf16_f32 v154, v148, v149
  v_mfma_f32_16x16x32_bf16 v[132:135], a[144:147], v[48:51], 0
  v_exp_f32_e32 v150, v150
  v_mfma_f32_16x16x32_bf16 v[132:135], a[148:151], v[52:55], v[132:135]
  v_add_f32_e32 v156, v149, v156
  v_add_f32_e32 v156, v150, v156
  v_mfma_f32_16x16x32_bf16 v[132:135], a[152:155], v[56:59], v[132:135]
  v_exp_f32_e32 v151, v151
  v_mfma_f32_16x16x32_bf16 v[132:135], a[156:159], v[60:63], v[132:135]
  s_waitcnt lgkmcnt(1)
  v_cvt_pk_bf16_f32 v155, v150, v151
  v_add_f32_e32 v156, v151, v156
  s_nop 0
  v_mfma_f32_16x16x32_bf16 a[0:3], a[192:195], v[152:155], a[0:3]
  s_add_u32 s38, s87, s96
  v_add_u32_e32 v8, s38, v5
  v_exp_f32_e32 v128, v128
  v_mfma_f32_16x16x32_bf16 a[4:7], a[196:199], v[152:155], a[4:7]
  s_add_u32 s38, s87, s97
  v_add_f32_e32 v157, v128, v157
  v_mfma_f32_16x16x32_bf16 a[8:11], a[200:203], v[152:155], a[8:11]
  v_add_u32_e32 v236, s38, v5
  s_add_u32 s38, s87, s98
  v_exp_f32_e32 v129, v129
  v_mfma_f32_16x16x32_bf16 a[12:15], a[204:207], v[152:155], a[12:15]
  v_add_u32_e32 v237, s38, v5
  v_add_f32_e32 v157, v129, v157
  v_mfma_f32_16x16x32_bf16 a[16:19], a[208:211], v[152:155], a[16:19]
  s_add_u32 s38, s87, s99
  v_exp_f32_e32 v130, v130
  v_mfma_f32_16x16x32_bf16 a[20:23], a[212:215], v[152:155], a[20:23]
  v_add_u32_e32 v238, s38, v5
  v_add_u32_e32 v9, s86, v6
  v_add_f32_e32 v157, v130, v157
  v_mfma_f32_16x16x32_bf16 a[24:27], a[216:219], v[152:155], a[24:27]
  v_add_u32_e32 v10, s86, v7
  v_exp_f32_e32 v131, v131
  v_mfma_f32_16x16x32_bf16 a[28:31], a[220:223], v[152:155], a[28:31]
  ds_read_b128 a[128:131], v8
  v_add_f32_e32 v157, v131, v157
  s_sleep 1
  v_mfma_f32_16x16x32_bf16 v[136:139], a[96:99], v[32:35], 0
  ds_read_b128 a[132:135], v236
  ds_read_b128 a[136:139], v237
  v_exp_f32_e32 v132, v132
  v_mfma_f32_16x16x32_bf16 v[136:139], a[100:103], v[36:39], v[136:139]
  ds_read_b128 a[140:143], v238
  v_cvt_pk_bf16_f32 v152, v128, v129
  v_cvt_pk_bf16_f32 v153, v130, v131
  v_mfma_f32_16x16x32_bf16 v[136:139], a[104:107], v[40:43], v[136:139]
  ds_read_b64_tr_b16 a[160:161], v9
  ds_read_b64_tr_b16 a[162:163], v9 offset:4096
  v_exp_f32_e32 v133, v133
  v_mfma_f32_16x16x32_bf16 v[136:139], a[108:111], v[44:47], v[136:139]
  ds_read_b64_tr_b16 a[164:165], v10
  v_add_f32_e32 v157, v132, v157
  v_cvt_pk_bf16_f32 v154, v132, v133
  v_mfma_f32_16x16x32_bf16 v[140:143], a[112:115], v[32:35], 0
  ds_read_b64_tr_b16 a[166:167], v10 offset:4096
  v_exp_f32_e32 v134, v134
  v_mfma_f32_16x16x32_bf16 v[140:143], a[116:119], v[36:39], v[140:143]
  ds_read_b64_tr_b16 a[168:169], v9 offset:512
  ds_read_b64_tr_b16 a[170:171], v9 offset:4608
  v_add_f32_e32 v157, v133, v157
  v_add_f32_e32 v157, v134, v157
  v_mfma_f32_16x16x32_bf16 v[140:143], a[120:123], v[40:43], v[140:143]
  s_waitcnt lgkmcnt(6)
  ds_read_b64_tr_b16 a[172:173], v10 offset:512
  v_exp_f32_e32 v135, v135
  v_mfma_f32_16x16x32_bf16 v[140:143], a[124:127], v[44:47], v[140:143]
  ds_read_b64_tr_b16 a[174:175], v10 offset:4608
  v_cvt_pk_bf16_f32 v155, v134, v135
  v_add_f32_e32 v157, v135, v157
  s_nop 0
  v_mfma_f32_16x16x32_bf16 a[32:35], a[192:195], v[152:155], a[32:35]
  ds_read_b128 a[144:147], v8 offset:4096
  v_exp_f32_e32 v136, v136
  v_mfma_f32_16x16x32_bf16 a[36:39], a[196:199], v[152:155], a[36:39]
  ds_read_b128 a[148:151], v236 offset:4096
  v_add_f32_e32 v156, v136, v156
  v_mfma_f32_16x16x32_bf16 a[40:43], a[200:203], v[152:155], a[40:43]
  s_add_u32 s39, s39, 1
  ds_read_b128 a[152:155], v237 offset:4096
  v_exp_f32_e32 v137, v137
  v_mfma_f32_16x16x32_bf16 a[44:47], a[204:207], v[152:155], a[44:47]
  s_lshr_b32 s50, s39, 3
  ds_read_b128 a[156:159], v238 offset:4096
  v_add_f32_e32 v156, v137, v156
  v_mfma_f32_16x16x32_bf16 a[48:51], a[208:211], v[152:155], a[48:51]
  s_lshl_b32 s50, s50, 20
  ds_read_b64_tr_b16 a[176:177], v9 offset:1024
  v_exp_f32_e32 v138, v138
  v_mfma_f32_16x16x32_bf16 a[52:55], a[212:215], v[152:155], a[52:55]
  s_add_u32 s51, s39, s62
  ds_read_b64_tr_b16 a[178:179], v9 offset:5120
  v_add_f32_e32 v156, v138, v156
  v_mfma_f32_16x16x32_bf16 a[56:59], a[216:219], v[152:155], a[56:59]
  s_and_b32 s51, s51, 7
  ds_read_b64_tr_b16 a[180:181], v10 offset:1024
  v_exp_f32_e32 v139, v139
  v_mfma_f32_16x16x32_bf16 a[60:63], a[220:223], v[152:155], a[60:63]
  s_lshl_b32 s51, s51, 15
  ds_read_b64_tr_b16 a[182:183], v10 offset:5120
  v_add_f32_e32 v156, v139, v156
  s_sleep 1
  v_mfma_f32_16x16x32_bf16 v[144:147], a[96:99], v[48:51], 0
  s_add_u32 s40, s50, s51
  ds_read_b64_tr_b16 a[184:185], v9 offset:1536
  v_exp_f32_e32 v140, v140
  v_mfma_f32_16x16x32_bf16 v[144:147], a[100:103], v[52:55], v[144:147]
  s_add_u32 s41, s40, 8192
  ds_read_b64_tr_b16 a[186:187], v9 offset:5632
  v_cvt_pk_bf16_f32 v152, v136, v137
  v_cvt_pk_bf16_f32 v153, v138, v139
  v_mfma_f32_16x16x32_bf16 v[144:147], a[104:107], v[56:59], v[144:147]
  s_add_u32 s42, s40, 16384
  ds_read_b64_tr_b16 a[188:189], v10 offset:1536
  v_exp_f32_e32 v141, v141
  v_mfma_f32_16x16x32_bf16 v[144:147], a[108:111], v[60:63], v[144:147]
  s_add_u32 s43, s40, 24576
  ds_read_b64_tr_b16 a[190:191], v10 offset:5632
  v_add_f32_e32 v156, v140, v156
  v_cvt_pk_bf16_f32 v154, v140, v141
  v_mfma_f32_16x16x32_bf16 v[148:151], a[112:115], v[48:51], 0
  s_xor_b32 s37, s37, 0x10000
  v_exp_f32_e32 v142, v142
  v_mfma_f32_16x16x32_bf16 v[148:151], a[116:119], v[52:55], v[148:151]
  v_add_f32_e32 v156, v141, v156
  v_add_f32_e32 v156, v142, v156
  v_mfma_f32_16x16x32_bf16 v[148:151], a[120:123], v[56:59], v[148:151]
  v_exp_f32_e32 v143, v143
  v_mfma_f32_16x16x32_bf16 v[148:151], a[124:127], v[60:63], v[148:151]
  s_waitcnt lgkmcnt(1)
  v_cvt_pk_bf16_f32 v155, v142, v143
  v_add_f32_e32 v156, v143, v156
  s_nop 0
  v_mfma_f32_16x16x32_bf16 a[0:3], a[160:163], v[152:155], a[0:3]
  v_exp_f32_e32 v144, v144
  s_waitcnt lgkmcnt(0)
  s_barrier
  v_mfma_f32_16x16x32_bf16 a[4:7], a[164:167], v[152:155], a[4:7]
  s_mov_b32 m0, s37
  v_add_u32_e32 v9, s87, v6
  v_add_f32_e32 v157, v144, v157
  v_mfma_f32_16x16x32_bf16 a[8:11], a[168:171], v[152:155], a[8:11]
  v_add_u32_e32 v10, s87, v7
  buffer_load_dwordx4 v1, s[16:19], s40 offen nt lds
  v_exp_f32_e32 v145, v145
  v_mfma_f32_16x16x32_bf16 a[12:15], a[172:175], v[152:155], a[12:15]
  ds_read_b64_tr_b16 a[192:193], v9
  v_add_f32_e32 v157, v145, v157
  v_mfma_f32_16x16x32_bf16 a[16:19], a[176:179], v[152:155], a[16:19]
  ds_read_b64_tr_b16 a[194:195], v9 offset:4096
  buffer_load_dwordx4 v2, s[16:19], s40 offen offset:1024 nt lds
  v_exp_f32_e32 v146, v146
  v_mfma_f32_16x16x32_bf16 a[20:23], a[180:183], v[152:155], a[20:23]
  s_add_u32 m0, s37, 16384
  v_add_f32_e32 v157, v146, v157
  v_mfma_f32_16x16x32_bf16 a[24:27], a[184:187], v[152:155], a[24:27]
  ds_read_b64_tr_b16 a[196:197], v10
  buffer_load_dwordx4 v1, s[16:19], s41 offen nt lds
  v_exp_f32_e32 v147, v147
  v_mfma_f32_16x16x32_bf16 a[28:31], a[188:191], v[152:155], a[28:31]
  ds_read_b64_tr_b16 a[198:199], v10 offset:4096
  v_add_f32_e32 v157, v147, v157
  s_sleep 1
  v_mfma_f32_16x16x32_bf16 v[128:131], a[128:131], v[32:35], 0
  ds_read_b64_tr_b16 a[200:201], v9 offset:512
  buffer_load_dwordx4 v2, s[16:19], s41 offen offset:1024 nt lds
  v_exp_f32_e32 v148, v148
  v_mfma_f32_16x16x32_bf16 v[128:131], a[132:135], v[36:39], v[128:131]
  s_add_u32 m0, s37, 32768
  ds_read_b64_tr_b16 a[202:203], v9 offset:4608
  v_cvt_pk_bf16_f32 v152, v144, v145
  v_cvt_pk_bf16_f32 v153, v146, v147
  v_mfma_f32_16x16x32_bf16 v[128:131], a[136:139], v[40:43], v[128:131]
  buffer_load_dwordx4 v1, s[16:19], s42 offen nt lds
  v_exp_f32_e32 v149, v149
  v_mfma_f32_16x16x32_bf16 v[128:131], a[140:143], v[44:47], v[128:131]
  ds_read_b64_tr_b16 a[204:205], v10 offset:512
  v_add_f32_e32 v157, v148, v157
  v_cvt_pk_bf16_f32 v154, v148, v149
  v_mfma_f32_16x16x32_bf16 v[132:135], a[144:147], v[32:35], 0
  ds_read_b64_tr_b16 a[206:207], v10 offset:4608
  buffer_load_dwordx4 v2, s[16:19], s42 offen offset:1024 nt lds
  v_exp_f32_e32 v150, v150
  v_mfma_f32_16x16x32_bf16 v[132:135], a[148:151], v[36:39], v[132:135]
  s_add_u32 m0, s37, 49152
  ds_read_b64_tr_b16 a[208:209], v9 offset:1024
  v_add_f32_e32 v157, v149, v157
  v_add_f32_e32 v157, v150, v157
  v_mfma_f32_16x16x32_bf16 v[132:135], a[152:155], v[40:43], v[132:135]
  ds_read_b64_tr_b16 a[210:211], v9 offset:5120
  buffer_load_dwordx4 v1, s[16:19], s43 offen nt lds
  v_exp_f32_e32 v151, v151
  v_mfma_f32_16x16x32_bf16 v[132:135], a[156:159], v[44:47], v[132:135]
  v_cvt_pk_bf16_f32 v155, v150, v151
  v_add_f32_e32 v157, v151, v157
  s_nop 0
  v_mfma_f32_16x16x32_bf16 a[32:35], a[160:163], v[152:155], a[32:35]
  ds_read_b64_tr_b16 a[212:213], v10 offset:1024
  v_exp_f32_e32 v128, v128
  v_mfma_f32_16x16x32_bf16 a[36:39], a[164:167], v[152:155], a[36:39]
  ds_read_b64_tr_b16 a[214:215], v10 offset:5120
  buffer_load_dwordx4 v2, s[16:19], s43 offen offset:1024 nt lds
  v_add_f32_e32 v156, v128, v156
  v_mfma_f32_16x16x32_bf16 a[40:43], a[168:171], v[152:155], a[40:43]
  s_xor_b32 s84, s84, 0x10000
  ds_read_b64_tr_b16 a[216:217], v9 offset:1536
  v_exp_f32_e32 v129, v129
  v_mfma_f32_16x16x32_bf16 a[44:47], a[172:175], v[152:155], a[44:47]
  v_add_f32_e32 v156, v129, v156
  v_mfma_f32_16x16x32_bf16 a[48:51], a[176:179], v[152:155], a[48:51]
  ds_read_b64_tr_b16 a[218:219], v9 offset:5632
  v_exp_f32_e32 v130, v130
  v_mfma_f32_16x16x32_bf16 a[52:55], a[180:183], v[152:155], a[52:55]
  ds_read_b64_tr_b16 a[220:221], v10 offset:1536
  v_add_f32_e32 v156, v130, v156
  v_mfma_f32_16x16x32_bf16 a[56:59], a[184:187], v[152:155], a[56:59]
  ds_read_b64_tr_b16 a[222:223], v10 offset:5632
  v_exp_f32_e32 v131, v131
  v_mfma_f32_16x16x32_bf16 a[60:63], a[188:191], v[152:155], a[60:63]
  v_add_f32_e32 v156, v131, v156
  s_sleep 1
  v_mfma_f32_16x16x32_bf16 v[136:139], a[128:131], v[48:51], 0
  v_exp_f32_e32 v132, v132
  v_mfma_f32_16x16x32_bf16 v[136:139], a[132:135], v[52:55], v[136:139]
  v_cvt_pk_bf16_f32 v152, v128, v129
  v_cvt_pk_bf16_f32 v153, v130, v131
  v_mfma_f32_16x16x32_bf16 v[136:139], a[136:139], v[56:59], v[136:139]
  v_exp_f32_e32 v133, v133
  v_mfma_f32_16x16x32_bf16 v[136:139], a[140:143], v[60:63], v[136:139]
  v_add_f32_e32 v156, v132, v156
  v_cvt_pk_bf16_f32 v154, v132, v133
  v_mfma_f32_16x16x32_bf16 v[140:143], a[144:147], v[48:51], 0
  v_exp_f32_e32 v134, v134
  v_mfma_f32_16x16x32_bf16 v[140:143], a[148:151], v[52:55], v[140:143]
  v_add_f32_e32 v156, v133, v156
  v_add_f32_e32 v156, v134, v156
  v_mfma_f32_16x16x32_bf16 v[140:143], a[152:155], v[56:59], v[140:143]
  s_waitcnt vmcnt(8) lgkmcnt(0)
  v_exp_f32_e32 v135, v135
  v_mfma_f32_16x16x32_bf16 v[140:143], a[156:159], v[60:63], v[140:143]
  v_cvt_pk_bf16_f32 v155, v134, v135
  v_add_f32_e32 v156, v135, v156
  s_nop 0
  s_barrier
  v_mfma_f32_16x16x32_bf16 a[0:3], a[192:195], v[152:155], a[0:3]
  s_add_u32 m0, s37, 8192
  s_add_u32 s38, s84, s96
  v_add_u32_e32 v8, s38, v5
  v_exp_f32_e32 v136, v136
  v_mfma_f32_16x16x32_bf16 a[4:7], a[196:199], v[152:155], a[4:7]
  s_add_u32 s38, s84, s97
  v_add_u32_e32 v236, s38, v5
  buffer_load_dwordx4 v3, s[20:23], s40 offen nt lds
  v_add_f32_e32 v157, v136, v157
  v_mfma_f32_16x16x32_bf16 a[8:11], a[200:203], v[152:155], a[8:11]
  s_xor_b32 s85, s85, 0x10000
  s_add_u32 s38, s84, s98
  v_exp_f32_e32 v137, v137
  v_mfma_f32_16x16x32_bf16 a[12:15], a[204:207], v[152:155], a[12:15]
  v_add_u32_e32 v237, s38, v5
  s_add_u32 s38, s84, s99
  buffer_load_dwordx4 v4, s[20:23], s40 offen offset:1024 nt lds
  v_add_f32_e32 v157, v137, v157
  v_mfma_f32_16x16x32_bf16 a[16:19], a[208:211], v[152:155], a[16:19]
  s_add_u32 m0, s37, 24576
  v_add_u32_e32 v238, s38, v5
  v_exp_f32_e32 v138, v138
  v_mfma_f32_16x16x32_bf16 a[20:23], a[212:215], v[152:155], a[20:23]
  ds_read_b128 a[96:99], v8
  ds_read_b128 a[100:103], v236
  buffer_load_dwordx4 v3, s[20:23], s41 offen nt lds
  v_add_f32_e32 v157, v138, v157
  v_mfma_f32_16x16x32_bf16 a[24:27], a[216:219], v[152:155], a[24:27]
  ds_read_b128 a[104:107], v237
  ds_read_b128 a[108:111], v238
  v_exp_f32_e32 v139, v139
  v_mfma_f32_16x16x32_bf16 a[28:31], a[220:223], v[152:155], a[28:31]
  ds_read_b128 a[112:115], v8 offset:4096
  buffer_load_dwordx4 v4, s[20:23], s41 offen offset:1024 nt lds
  v_add_f32_e32 v157, v139, v157
  s_sleep 1
  v_mfma_f32_16x16x32_bf16 v[144:147], a[128:131], v[64:67], 0
  s_add_u32 m0, s37, 40960
  ds_read_b128 a[116:119], v236 offset:4096
  ds_read_b128 a[120:123], v237 offset:4096
  v_exp_f32_e32 v140, v140
  v_mfma_f32_16x16x32_bf16 v[144:147], a[132:135], v[68:71], v[144:147]
  ds_read_b128 a[124:127], v238 offset:4096
  buffer_load_dwordx4 v3, s[20:23], s42 offen nt lds
  v_cvt_pk_bf16_f32 v152, v136, v137
  v_cvt_pk_bf16_f32 v153, v138, v139
  v_mfma_f32_16x16x32_bf16 v[144:147], a[136:139], v[72:75], v[144:147]
  v_exp_f32_e32 v141, v141
  v_mfma_f32_16x16x32_bf16 v[144:147], a[140:143], v[76:79], v[144:147]
  buffer_load_dwordx4 v4, s[20:23], s42 offen offset:1024 nt lds
  v_add_f32_e32 v157, v140, v157
  v_cvt_pk_bf16_f32 v154, v140, v141
  v_mfma_f32_16x16x32_bf16 v[148:151], a[144:147], v[64:67], 0
  s_add_u32 m0, s37, 57344
  v_exp_f32_e32 v142, v142
  v_mfma_f32_16x16x32_bf16 v[148:151], a[148:151], v[68:71], v[148:151]
  buffer_load_dwordx4 v3, s[20:23], s43 offen nt lds
  v_add_f32_e32 v157, v141, v157
  v_add_f32_e32 v157, v142, v157
  v_mfma_f32_16x16x32_bf16 v[148:151], a[152:155], v[72:75], v[148:151]
  v_exp_f32_e32 v143, v143
  v_mfma_f32_16x16x32_bf16 v[148:151], a[156:159], v[76:79], v[148:151]
  buffer_load_dwordx4 v4, s[20:23], s43 offen offset:1024 nt lds
  v_cvt_pk_bf16_f32 v155, v142, v143
  v_add_f32_e32 v157, v143, v157
  s_nop 0
  v_mfma_f32_16x16x32_bf16 a[32:35], a[192:195], v[152:155], a[32:35]
  s_add_u32 s38, s85, s96
  v_add_u32_e32 v8, s38, v5
  v_exp_f32_e32 v144, v144
  v_mfma_f32_16x16x32_bf16 a[36:39], a[196:199], v[152:155], a[36:39]
  s_add_u32 s38, s85, s97
  v_add_f32_e32 v158, v144, v158
  v_mfma_f32_16x16x32_bf16 a[40:43], a[200:203], v[152:155], a[40:43]
  s_xor_b32 s87, s87, 0x10000
  v_add_u32_e32 v236, s38, v5
  s_add_u32 s38, s85, s98
  v_exp_f32_e32 v145, v145
  v_mfma_f32_16x16x32_bf16 a[44:47], a[204:207], v[152:155], a[44:47]
  v_add_u32_e32 v237, s38, v5
  v_add_f32_e32 v158, v145, v158
  v_mfma_f32_16x16x32_bf16 a[48:51], a[208:211], v[152:155], a[48:51]
  s_add_u32 s38, s85, s99
  v_exp_f32_e32 v146, v146
  v_mfma_f32_16x16x32_bf16 a[52:55], a[212:215], v[152:155], a[52:55]
  v_add_u32_e32 v238, s38, v5
  v_add_u32_e32 v9, s84, v6
  v_add_f32_e32 v158, v146, v158
  v_mfma_f32_16x16x32_bf16 a[56:59], a[216:219], v[152:155], a[56:59]
  v_add_u32_e32 v10, s84, v7
  v_exp_f32_e32 v147, v147
  v_mfma_f32_16x16x32_bf16 a[60:63], a[220:223], v[152:155], a[60:63]
  s_waitcnt lgkmcnt(0)
  ds_read_b64_tr_b16 a[160:161], v9
  v_add_f32_e32 v158, v147, v158
  s_sleep 1
  v_mfma_f32_16x16x32_bf16 v[128:131], a[96:99], v[32:35], 0
  ds_read_b64_tr_b16 a[162:163], v9 offset:4096
  ds_read_b64_tr_b16 a[164:165], v10
  v_exp_f32_e32 v148, v148
  v_mfma_f32_16x16x32_bf16 v[128:131], a[100:103], v[36:39], v[128:131]
  ds_read_b64_tr_b16 a[166:167], v10 offset:4096
  v_cvt_pk_bf16_f32 v152, v144, v145
  v_cvt_pk_bf16_f32 v153, v146, v147
  v_mfma_f32_16x16x32_bf16 v[128:131], a[104:107], v[40:43], v[128:131]
  ds_read_b64_tr_b16 a[168:169], v9 offset:512
  ds_read_b64_tr_b16 a[170:171], v9 offset:4608
  v_exp_f32_e32 v149, v149
  v_mfma_f32_16x16x32_bf16 v[128:131], a[108:111], v[44:47], v[128:131]
  ds_read_b64_tr_b16 a[172:173], v10 offset:512
  v_add_f32_e32 v158, v148, v158
  v_cvt_pk_bf16_f32 v154, v148, v149
  v_mfma_f32_16x16x32_bf16 v[132:135], a[112:115], v[32:35], 0
  ds_read_b64_tr_b16 a[174:175], v10 offset:4608
  v_exp_f32_e32 v150, v150
  v_mfma_f32_16x16x32_bf16 v[132:135], a[116:119], v[36:39], v[132:135]
  ds_read_b64_tr_b16 a[176:177], v9 offset:1024
  ds_read_b64_tr_b16 a[178:179], v9 offset:5120
  v_add_f32_e32 v158, v149, v158
  v_add_f32_e32 v158, v150, v158
  v_mfma_f32_16x16x32_bf16 v[132:135], a[120:123], v[40:43], v[132:135]
  ds_read_b128 a[128:131], v8
  v_exp_f32_e32 v151, v151
  v_mfma_f32_16x16x32_bf16 v[132:135], a[124:127], v[44:47], v[132:135]
  ds_read_b128 a[132:135], v236
  v_cvt_pk_bf16_f32 v155, v150, v151
  v_add_f32_e32 v158, v151, v158
  s_nop 0
  v_mfma_f32_16x16x32_bf16 a[64:67], a[192:195], v[152:155], a[64:67]
  ds_read_b64_tr_b16 a[180:181], v10 offset:1024
  v_exp_f32_e32 v128, v128
  v_mfma_f32_16x16x32_bf16 a[68:71], a[196:199], v[152:155], a[68:71]
  ds_read_b64_tr_b16 a[182:183], v10 offset:5120
  v_add_f32_e32 v156, v128, v156
  v_mfma_f32_16x16x32_bf16 a[72:75], a[200:203], v[152:155], a[72:75]
  s_xor_b32 s86, s86, 0x10000
  ds_read_b64_tr_b16 a[184:185], v9 offset:1536
  v_exp_f32_e32 v129, v129
  v_mfma_f32_16x16x32_bf16 a[76:79], a[204:207], v[152:155], a[76:79]
  ds_read_b64_tr_b16 a[186:187], v9 offset:5632
  v_add_f32_e32 v156, v129, v156
  v_mfma_f32_16x16x32_bf16 a[80:83], a[208:211], v[152:155], a[80:83]
  ds_read_b64_tr_b16 a[188:189], v10 offset:1536
  v_exp_f32_e32 v130, v130
  v_mfma_f32_16x16x32_bf16 a[84:87], a[212:215], v[152:155], a[84:87]
  ds_read_b64_tr_b16 a[190:191], v10 offset:5632
  v_add_f32_e32 v156, v130, v156
  v_mfma_f32_16x16x32_bf16 a[88:91], a[216:219], v[152:155], a[88:91]
  ds_read_b128 a[136:139], v237
  v_exp_f32_e32 v131, v131
  v_mfma_f32_16x16x32_bf16 a[92:95], a[220:223], v[152:155], a[92:95]
  ds_read_b128 a[140:143], v238
  v_add_f32_e32 v156, v131, v156
  s_sleep 1
  v_mfma_f32_16x16x32_bf16 v[136:139], a[96:99], v[48:51], 0
  ds_read_b128 a[144:147], v8 offset:4096
  v_exp_f32_e32 v132, v132
  v_mfma_f32_16x16x32_bf16 v[136:139], a[100:103], v[52:55], v[136:139]
  ds_read_b128 a[148:151], v236 offset:4096
  v_cvt_pk_bf16_f32 v152, v128, v129
  v_cvt_pk_bf16_f32 v153, v130, v131
  v_mfma_f32_16x16x32_bf16 v[136:139], a[104:107], v[56:59], v[136:139]
  ds_read_b128 a[152:155], v237 offset:4096
  v_exp_f32_e32 v133, v133
  v_mfma_f32_16x16x32_bf16 v[136:139], a[108:111], v[60:63], v[136:139]
  ds_read_b128 a[156:159], v238 offset:4096
  v_add_f32_e32 v156, v132, v156
  v_cvt_pk_bf16_f32 v154, v132, v133
  v_mfma_f32_16x16x32_bf16 v[140:143], a[112:115], v[48:51], 0
  v_exp_f32_e32 v134, v134
  v_mfma_f32_16x16x32_bf16 v[140:143], a[116:119], v[52:55], v[140:143]
  v_add_f32_e32 v156, v133, v156
  v_add_f32_e32 v156, v134, v156
  v_mfma_f32_16x16x32_bf16 v[140:143], a[120:123], v[56:59], v[140:143]
  v_exp_f32_e32 v135, v135
  v_mfma_f32_16x16x32_bf16 v[140:143], a[124:127], v[60:63], v[140:143]
  s_waitcnt lgkmcnt(1)
  v_cvt_pk_bf16_f32 v155, v134, v135
  v_add_f32_e32 v156, v135, v156
  s_nop 0
  s_sub_u32 s44, s44, 1
  s_cmp_lg_u32 s44, 0
  s_cbranch_scc1 .Lbody%=
  s_cmp_eq_u32 s45, 0
  s_cbranch_scc1 .Llast%=
  s_cmp_lg_u32 s79, 0
  s_cbranch_scc1 .Llast%=
  s_cmp_lt_u32 %[gid], 240
  s_cbranch_scc1 .Lfinal%=
.Llast%=:
  v_mfma_f32_16x16x32_bf16 a[0:3], a[160:163], v[152:155], a[0:3]
  s_add_u32 s38, s86, s96
  v_add_u32_e32 v8, s38, v5
  v_exp_f32_e32 v136, v136
  v_mfma_f32_16x16x32_bf16 a[4:7], a[164:167], v[152:155], a[4:7]
  s_add_u32 s38, s86, s97
  v_add_f32_e32 v157, v136, v157
  v_mfma_f32_16x16x32_bf16 a[8:11], a[168:171], v[152:155], a[8:11]
  v_add_u32_e32 v236, s38, v5
  s_add_u32 s38, s86, s98
  v_exp_f32_e32 v137, v137
  v_mfma_f32_16x16x32_bf16 a[12:15], a[172:175], v[152:155], a[12:15]
  v_add_u32_e32 v237, s38, v5
  v_add_f32_e32 v157, v137, v157
  v_mfma_f32_16x16x32_bf16 a[16:19], a[176:179], v[152:155], a[16:19]
  s_add_u32 s38, s86, s99
  v_exp_f32_e32 v138, v138
  v_mfma_f32_16x16x32_bf16 a[20:23], a[180:183], v[152:155], a[20:23]
  v_add_u32_e32 v238, s38, v5
  v_add_u32_e32 v9, s85, v6
  v_add_f32_e32 v157, v138, v157
  v_mfma_f32_16x16x32_bf16 a[24:27], a[184:187], v[152:155], a[24:27]
  v_add_u32_e32 v10, s85, v7
  v_exp_f32_e32 v139, v139
  v_mfma_f32_16x16x32_bf16 a[28:31], a[188:191], v[152:155], a[28:31]
  ds_read_b128 a[96:99], v8
  v_add_f32_e32 v157, v139, v157
  s_sleep 1
  v_mfma_f32_16x16x32_bf16 v[144:147], a[128:131], v[32:35], 0
  ds_read_b128 a[100:103], v236
  ds_read_b128 a[104:107], v237
  v_exp_f32_e32 v140, v140
  v_mfma_f32_16x16x32_bf16 v[144:147], a[132:135], v[36:39], v[144:147]
  ds_read_b128 a[108:111], v238
  v_cvt_pk_bf16_f32 v152, v136, v137
  v_cvt_pk_bf16_f32 v153, v138, v139
  v_mfma_f32_16x16x32_bf16 v[144:147], a[136:139], v[40:43], v[144:147]
  ds_read_b64_tr_b16 a[192:193], v9
  ds_read_b64_tr_b16 a[194:195], v9 offset:4096
  v_exp_f32_e32 v141, v141
  v_mfma_f32_16x16x32_bf16 v[144:147], a[140:143], v[44:47], v[144:147]
  ds_read_b64_tr_b16 a[196:197], v10
  v_add_f32_e32 v157, v140, v157
  v_cvt_pk_bf16_f32 v154, v140, v141
  v_mfma_f32_16x16x32_bf16 v[148:151], a[144:147], v[32:35], 0
  ds_read_b64_tr_b16 a[198:199], v10 offset:4096
  v_exp_f32_e32 v142, v142
  v_mfma_f32_16x16x32_bf16 v[148:151], a[148:151], v[36:39], v[148:151]
  ds_read_b64_tr_b16 a[200:201], v9 offset:512
  ds_read_b64_tr_b16 a[202:203], v9 offset:4608
  v_add_f32_e32 v157, v141, v157
  v_add_f32_e32 v157, v142, v157
  v_mfma_f32_16x16x32_bf16 v[148:151], a[152:155], v[40:43], v[148:151]
  s_waitcnt lgkmcnt(6)
  ds_read_b64_tr_b16 a[204:205], v10 offset:512
  v_exp_f32_e32 v143, v143
  v_mfma_f32_16x16x32_bf16 v[148:151], a[156:159], v[44:47], v[148:151]
  ds_read_b64_tr_b16 a[206:207], v10 offset:4608
  v_cvt_pk_bf16_f32 v155, v142, v143
  v_add_f32_e32 v157, v143, v157
  s_nop 0
  v_mfma_f32_16x16x32_bf16 a[32:35], a[160:163], v[152:155], a[32:35]
  ds_read_b64_tr_b16 a[208:209], v9 offset:1024
  v_exp_f32_e32 v144, v144
  v_mfma_f32_16x16x32_bf16 a[36:39], a[164:167], v[152:155], a[36:39]
  ds_read_b64_tr_b16 a[210:211], v9 offset:5120
  v_add_f32_e32 v156, v144, v156
  v_mfma_f32_16x16x32_bf16 a[40:43], a[168:171], v[152:155], a[40:43]
  ds_read_b64_tr_b16 a[212:213], v10 offset:1024
  v_exp_f32_e32 v145, v145
  v_mfma_f32_16x16x32_bf16 a[44:47], a[172:175], v[152:155], a[44:47]
  ds_read_b64_tr_b16 a[214:215], v10 offset:5120
  v_add_f32_e32 v156, v145, v156
  v_mfma_f32_16x16x32_bf16 a[48:51], a[176:179], v[152:155], a[48:51]
  ds_read_b64_tr_b16 a[216:217], v9 offset:1536
  v_exp_f32_e32 v146, v146
  v_mfma_f32_16x16x32_bf16 a[52:55], a[180:183], v[152:155], a[52:55]
  ds_read_b64_tr_b16 a[218:219], v9 offset:5632
  v_add_f32_e32 v156, v146, v156
  v_mfma_f32_16x16x32_bf16 a[56:59], a[184:187], v[152:155], a[56:59]
  ds_read_b64_tr_b16 a[220:221], v10 offset:1536
  v_exp_f32_e32 v147, v147
  v_mfma_f32_16x16x32_bf16 a[60:63], a[188:191], v[152:155], a[60:63]
  ds_read_b64_tr_b16 a[222:223], v10 offset:5632
  v_add_f32_e32 v156, v147, v156
  s_sleep 1
  v_mfma_f32_16x16x32_bf16 v[128:131], a[128:131], v[48:51], 0
  ds_read_b128 a[112:115], v8 offset:4096
  v_exp_f32_e32 v148, v148
  v_mfma_f32_16x16x32_bf16 v[128:131], a[132:135], v[52:55], v[128:131]
  ds_read_b128 a[116:119], v236 offset:4096
  v_cvt_pk_bf16_f32 v152, v144, v145
  v_cvt_pk_bf16_f32 v153, v146, v147
  v_mfma_f32_16x16x32_bf16 v[128:131], a[136:139], v[56:59], v[128:131]
  ds_read_b128 a[120:123], v237 offset:4096
  v_exp_f32_e32 v149, v149
  v_mfma_f32_16x16x32_bf16 v[128:131], a[140:143], v[60:63], v[128:131]
  ds_read_b128 a[124:127], v238 offset:4096
  v_add_f32_e32 v156, v148, v156
  v_cvt_pk_bf16_f32 v154, v148, v149
  v_mfma_f32_16x16x32_bf16 v[132:135], a[144:147], v[48:51], 0
  v_exp_f32_e32 v150, v150
  v_mfma_f32_16x16x32_bf16 v[132:135], a[148:151], v[52:55], v[132:135]
  v_add_f32_e32 v156, v149, v156
  v_add_f32_e32 v156, v150, v156
  v_mfma_f32_16x16x32_bf16 v[132:135], a[152:155], v[56:59], v[132:135]
  v_exp_f32_e32 v151, v151
  v_mfma_f32_16x16x32_bf16 v[132:135], a[156:159], v[60:63], v[132:135]
  s_waitcnt lgkmcnt(1)
  v_cvt_pk_bf16_f32 v155, v150, v151
  v_add_f32_e32 v156, v151, v156
  s_nop 0
  v_mfma_f32_16x16x32_bf16 a[0:3], a[192:195], v[152:155], a[0:3]
  s_add_u32 s38, s87, s96
  v_add_u32_e32 v8, s38, v5
  v_exp_f32_e32 v128, v128
  v_mfma_f32_16x16x32_bf16 a[4:7], a[196:199], v[152:155], a[4:7]
  s_add_u32 s38, s87, s97
  v_add_f32_e32 v157, v128, v157
  v_mfma_f32_16x16x32_bf16 a[8:11], a[200:203], v[152:155], a[8:11]
  v_add_u32_e32 v236, s38, v5
  s_add_u32 s38, s87, s98
  v_exp_f32_e32 v129, v129
  v_mfma_f32_16x16x32_bf16 a[12:15], a[204:207], v[152:155], a[12:15]
  v_add_u32_e32 v237, s38, v5
  v_add_f32_e32 v157, v129, v157
  v_mfma_f32_16x16x32_bf16 a[16:19], a[208:211], v[152:155], a[16:19]
  s_add_u32 s38, s87, s99
  v_exp_f32_e32 v130, v130
  v_mfma_f32_16x16x32_bf16 a[20:23], a[212:215], v[152:155], a[20:23]
  v_add_u32_e32 v238, s38, v5
  v_add_u32_e32 v9, s86, v6
  v_add_f32_e32 v157, v130, v157
  v_mfma_f32_16x16x32_bf16 a[24:27], a[216:219], v[152:155], a[24:27]
  v_add_u32_e32 v10, s86, v7
  v_exp_f32_e32 v131, v131
  v_mfma_f32_16x16x32_bf16 a[28:31], a[220:223], v[152:155], a[28:31]
  ds_read_b128 a[128:131], v8
  v_add_f32_e32 v157, v131, v157
  s_sleep 1
  v_mfma_f32_16x16x32_bf16 v[136:139], a[96:99], v[32:35], 0
  ds_read_b128 a[132:135], v236
  ds_read_b128 a[136:139], v237
  v_exp_f32_e32 v132, v132
  v_mfma_f32_16x16x32_bf16 v[136:139], a[100:103], v[36:39], v[136:139]
  ds_read_b128 a[140:143], v238
  v_cvt_pk_bf16_f32 v152, v128, v129
  v_cvt_pk_bf16_f32 v153, v130, v131
  v_mfma_f32_16x16x32_bf16 v[136:139], a[104:107], v[40:43], v[136:139]
  ds_read_b64_tr_b16 a[160:161], v9
  ds_read_b64_tr_b16 a[162:163], v9 offset:4096
  v_exp_f32_e32 v133, v133
  v_mfma_f32_16x16x32_bf16 v[136:139], a[108:111], v[44:47], v[136:139]
  ds_read_b64_tr_b16 a[164:165], v10
  v_add_f32_e32 v157, v132, v157
  v_cvt_pk_bf16_f32 v154, v132, v133
  v_mfma_f32_16x16x32_bf16 v[140:143], a[112:115], v[32:35], 0
  ds_read_b64_tr_b16 a[166:167], v10 offset:4096
  v_exp_f32_e32 v134, v134
  v_mfma_f32_16x16x32_bf16 v[140:143], a[116:119], v[36:39], v[140:143]
  ds_read_b64_tr_b16 a[168:169], v9 offset:512
  ds_read_b64_tr_b16 a[170:171], v9 offset:4608
  v_add_f32_e32 v157, v133, v157
  v_add_f32_e32 v157, v134, v157
  v_mfma_f32_16x16x32_bf16 v[140:143], a[120:123], v[40:43], v[140:143]
  s_waitcnt lgkmcnt(6)
  ds_read_b64_tr_b16 a[172:173], v10 offset:512
  v_exp_f32_e32 v135, v135
  v_mfma_f32_16x16x32_bf16 v[140:143], a[124:127], v[44:47], v[140:143]
  ds_read_b64_tr_b16 a[174:175], v10 offset:4608
  v_cvt_pk_bf16_f32 v155, v134, v135
  v_add_f32_e32 v157, v135, v157
  s_nop 0
  v_mfma_f32_16x16x32_bf16 a[32:35], a[192:195], v[152:155], a[32:35]
  ds_read_b128 a[144:147], v8 offset:4096
  v_exp_f32_e32 v136, v136
  v_mfma_f32_16x16x32_bf16 a[36:39], a[196:199], v[152:155], a[36:39]
  ds_read_b128 a[148:151], v236 offset:4096
  v_add_f32_e32 v156, v136, v156
  v_mfma_f32_16x16x32_bf16 a[40:43], a[200:203], v[152:155], a[40:43]
  s_add_u32 s39, s39, 1
  ds_read_b128 a[152:155], v237 offset:4096
  v_exp_f32_e32 v137, v137
  v_mfma_f32_16x16x32_bf16 a[44:47], a[204:207], v[152:155], a[44:47]
  s_lshr_b32 s50, s39, 3
  ds_read_b128 a[156:159], v238 offset:4096
  v_add_f32_e32 v156, v137, v156
  v_mfma_f32_16x16x32_bf16 a[48:51], a[208:211], v[152:155], a[48:51]
  s_lshl_b32 s50, s50, 20
  ds_read_b64_tr_b16 a[176:177], v9 offset:1024
  v_exp_f32_e32 v138, v138
  v_mfma_f32_16x16x32_bf16 a[52:55], a[212:215], v[152:155], a[52:55]
  s_add_u32 s51, s39, s62
  ds_read_b64_tr_b16 a[178:179], v9 offset:5120
  v_add_f32_e32 v156, v138, v156
  v_mfma_f32_16x16x32_bf16 a[56:59], a[216:219], v[152:155], a[56:59]
  s_and_b32 s51, s51, 7
  ds_read_b64_tr_b16 a[180:181], v10 offset:1024
  v_exp_f32_e32 v139, v139
  v_mfma_f32_16x16x32_bf16 a[60:63], a[220:223], v[152:155], a[60:63]
  s_lshl_b32 s51, s51, 15
  ds_read_b64_tr_b16 a[182:183], v10 offset:5120
  v_add_f32_e32 v156, v139, v156
  s_sleep 1
  v_mfma_f32_16x16x32_bf16 v[144:147], a[96:99], v[48:51], 0
  s_add_u32 s40, s50, s51
  ds_read_b64_tr_b16 a[184:185], v9 offset:1536
  v_exp_f32_e32 v140, v140
  v_mfma_f32_16x16x32_bf16 v[144:147], a[100:103], v[52:55], v[144:147]
  s_add_u32 s41, s40, 8192
  ds_read_b64_tr_b16 a[186:187], v9 offset:5632
  v_cvt_pk_bf16_f32 v152, v136, v137
  v_cvt_pk_bf16_f32 v153, v138, v139
  v_mfma_f32_16x16x32_bf16 v[144:147], a[104:107], v[56:59], v[144:147]
  s_add_u32 s42, s40, 16384
  ds_read_b64_tr_b16 a[188:189], v10 offset:1536
  v_exp_f32_e32 v141, v141
  v_mfma_f32_16x16x32_bf16 v[144:147], a[108:111], v[60:63], v[144:147]
  s_add_u32 s43, s40, 24576
  ds_read_b64_tr_b16 a[190:191], v10 offset:5632
  v_add_f32_e32 v156, v140, v156
  v_cvt_pk_bf16_f32 v154, v140, v141
  v_mfma_f32_16x16x32_bf16 v[148:151], a[112:115], v[48:51], 0
  s_xor_b32 s37, s37, 0x10000
  v_exp_f32_e32 v142, v142
  v_mfma_f32_16x16x32_bf16 v[148:151], a[116:119], v[52:55], v[148:151]
  v_add_f32_e32 v156, v141, v156
  v_add_f32_e32 v156, v142, v156
  v_mfma_f32_16x16x32_bf16 v[148:151], a[120:123], v[56:59], v[148:151]
  v_exp_f32_e32 v143, v143
  v_mfma_f32_16x16x32_bf16 v[148:151], a[124:127], v[60:63], v[148:151]
  s_waitcnt lgkmcnt(1)
  v_cvt_pk_bf16_f32 v155, v142, v143
  v_add_f32_e32 v156, v143, v156
  s_nop 0
  v_mfma_f32_16x16x32_bf16 a[0:3], a[160:163], v[152:155], a[0:3]
  v_exp_f32_e32 v144, v144
  s_waitcnt lgkmcnt(0)
  s_barrier
  v_mfma_f32_16x16x32_bf16 a[4:7], a[164:167], v[152:155], a[4:7]
  s_mov_b32 m0, s37
  v_add_u32_e32 v9, s87, v6
  v_add_f32_e32 v157, v144, v157
  v_mfma_f32_16x16x32_bf16 a[8:11], a[168:171], v[152:155], a[8:11]
  v_add_u32_e32 v10, s87, v7
  buffer_load_dwordx4 v1, s[16:19], s40 offen nt lds
  v_exp_f32_e32 v145, v145
  v_mfma_f32_16x16x32_bf16 a[12:15], a[172:175], v[152:155], a[12:15]
  ds_read_b64_tr_b16 a[192:193], v9
  v_add_f32_e32 v157, v145, v157
  v_mfma_f32_16x16x32_bf16 a[16:19], a[176:179], v[152:155], a[16:19]
  ds_read_b64_tr_b16 a[194:195], v9 offset:4096
  buffer_load_dwordx4 v2, s[16:19], s40 offen offset:1024 nt lds
  v_exp_f32_e32 v146, v146
  v_mfma_f32_16x16x32_bf16 a[20:23], a[180:183], v[152:155], a[20:23]
  s_add_u32 m0, s37, 16384
  v_add_f32_e32 v157, v146, v157
  v_mfma_f32_16x16x32_bf16 a[24:27], a[184:187], v[152:155], a[24:27]
  ds_read_b64_tr_b16 a[196:197], v10
  buffer_load_dwordx4 v1, s[16:19], s41 offen nt lds
  v_exp_f32_e32 v147, v147
  v_mfma_f32_16x16x32_bf16 a[28:31], a[188:191], v[152:155], a[28:31]
  ds_read_b64_tr_b16 a[198:199], v10 offset:4096
  v_add_f32_e32 v157, v147, v157
  s_sleep 1
  v_mfma_f32_16x16x32_bf16 v[128:131], a[128:131], v[32:35], 0
  ds_read_b64_tr_b16 a[200:201], v9 offset:512
  buffer_load_dwordx4 v2, s[16:19], s41 offen offset:1024 nt lds
  v_exp_f32_e32 v148, v148
  v_mfma_f32_16x16x32_bf16 v[128:131], a[132:135], v[36:39], v[128:131]
  s_add_u32 m0, s37, 32768
  ds_read_b64_tr_b16 a[202:203], v9 offset:4608
  v_cvt_pk_bf16_f32 v152, v144, v145
  v_cvt_pk_bf16_f32 v153, v146, v147
  v_mfma_f32_16x16x32_bf16 v[128:131], a[136:139], v[40:43], v[128:131]
  buffer_load_dwordx4 v1, s[16:19], s42 offen nt lds
  v_exp_f32_e32 v149, v149
  v_mfma_f32_16x16x32_bf16 v[128:131], a[140:143], v[44:47], v[128:131]
  ds_read_b64_tr_b16 a[204:205], v10 offset:512
  v_add_f32_e32 v157, v148, v157
  v_cvt_pk_bf16_f32 v154, v148, v149
  v_mfma_f32_16x16x32_bf16 v[132:135], a[144:147], v[32:35], 0
  ds_read_b64_tr_b16 a[206:207], v10 offset:4608
  buffer_load_dwordx4 v2, s[16:19], s42 offen offset:1024 nt lds
  v_exp_f32_e32 v150, v150
  v_mfma_f32_16x16x32_bf16 v[132:135], a[148:151], v[36:39], v[132:135]
  s_add_u32 m0, s37, 49152
  ds_read_b64_tr_b16 a[208:209], v9 offset:1024
  v_add_f32_e32 v157, v149, v157
  v_add_f32_e32 v157, v150, v157
  v_mfma_f32_16x16x32_bf16 v[132:135], a[152:155], v[40:43], v[132:135]
  ds_read_b64_tr_b16 a[210:211], v9 offset:5120
  buffer_load_dwordx4 v1, s[16:19], s43 offen nt lds
  v_exp_f32_e32 v151, v151
  v_mfma_f32_16x16x32_bf16 v[132:135], a[156:159], v[44:47], v[132:135]
  v_cvt_pk_bf16_f32 v155, v150, v151
  v_add_f32_e32 v157, v151, v157
  s_nop 0
  v_mfma_f32_16x16x32_bf16 a[32:35], a[160:163], v[152:155], a[32:35]
  ds_read_b64_tr_b16 a[212:213], v10 offset:1024
  v_exp_f32_e32 v128, v128
  v_mfma_f32_16x16x32_bf16 a[36:39], a[164:167], v[152:155], a[36:39]
  ds_read_b64_tr_b16 a[214:215], v10 offset:5120
  buffer_load_dwordx4 v2, s[16:19], s43 offen offset:1024 nt lds
  v_add_f32_e32 v156, v128, v156
  v_mfma_f32_16x16x32_bf16 a[40:43], a[168:171], v[152:155], a[40:43]
  s_xor_b32 s84, s84, 0x10000
  ds_read_b64_tr_b16 a[216:217], v9 offset:1536
  v_exp_f32_e32 v129, v129
  v_mfma_f32_16x16x32_bf16 a[44:47], a[172:175], v[152:155], a[44:47]
  v_add_f32_e32 v156, v129, v156
  v_mfma_f32_16x16x32_bf16 a[48:51], a[176:179], v[152:155], a[48:51]
  ds_read_b64_tr_b16 a[218:219], v9 offset:5632
  v_exp_f32_e32 v130, v130
  v_mfma_f32_16x16x32_bf16 a[52:55], a[180:183], v[152:155], a[52:55]
  ds_read_b64_tr_b16 a[220:221], v10 offset:1536
  v_add_f32_e32 v156, v130, v156
  v_mfma_f32_16x16x32_bf16 a[56:59], a[184:187], v[152:155], a[56:59]
  ds_read_b64_tr_b16 a[222:223], v10 offset:5632
  v_exp_f32_e32 v131, v131
  v_mfma_f32_16x16x32_bf16 a[60:63], a[188:191], v[152:155], a[60:63]
  v_add_f32_e32 v156, v131, v156
  s_sleep 1
  v_mfma_f32_16x16x32_bf16 v[136:139], a[128:131], v[48:51], 0
  v_exp_f32_e32 v132, v132
  v_mfma_f32_16x16x32_bf16 v[136:139], a[132:135], v[52:55], v[136:139]
  v_cvt_pk_bf16_f32 v152, v128, v129
  v_cvt_pk_bf16_f32 v153, v130, v131
  v_mfma_f32_16x16x32_bf16 v[136:139], a[136:139], v[56:59], v[136:139]
  v_exp_f32_e32 v133, v133
  v_mfma_f32_16x16x32_bf16 v[136:139], a[140:143], v[60:63], v[136:139]
  v_add_f32_e32 v156, v132, v156
  v_cvt_pk_bf16_f32 v154, v132, v133
  v_mfma_f32_16x16x32_bf16 v[140:143], a[144:147], v[48:51], 0
  v_exp_f32_e32 v134, v134
  v_mfma_f32_16x16x32_bf16 v[140:143], a[148:151], v[52:55], v[140:143]
  v_add_f32_e32 v156, v133, v156
  v_add_f32_e32 v156, v134, v156
  v_mfma_f32_16x16x32_bf16 v[140:143], a[152:155], v[56:59], v[140:143]
  s_waitcnt vmcnt(8) lgkmcnt(0)
  v_exp_f32_e32 v135, v135
  v_mfma_f32_16x16x32_bf16 v[140:143], a[156:159], v[60:63], v[140:143]
  v_cvt_pk_bf16_f32 v155, v134, v135
  v_add_f32_e32 v156, v135, v156
  s_nop 0
  s_barrier
  v_mfma_f32_16x16x32_bf16 a[0:3], a[192:195], v[152:155], a[0:3]
  s_add_u32 m0, s37, 8192
  s_add_u32 s38, s84, s96
  v_add_u32_e32 v8, s38, v5
  v_exp_f32_e32 v136, v136
  v_mfma_f32_16x16x32_bf16 a[4:7], a[196:199], v[152:155], a[4:7]
  s_add_u32 s38, s84, s97
  v_add_u32_e32 v236, s38, v5
  buffer_load_dwordx4 v3, s[20:23], s40 offen nt lds
  v_add_f32_e32 v157, v136, v157
  v_mfma_f32_16x16x32_bf16 a[8:11], a[200:203], v[152:155], a[8:11]
  s_xor_b32 s85, s85, 0x10000
  s_add_u32 s38, s84, s98
  v_exp_f32_e32 v137, v137
  v_mfma_f32_16x16x32_bf16 a[12:15], a[204:207], v[152:155], a[12:15]
  v_add_u32_e32 v237, s38, v5
  s_add_u32 s38, s84, s99
  buffer_load_dwordx4 v4, s[20:23], s40 offen offset:1024 nt lds
  v_add_f32_e32 v157, v137, v157
  v_mfma_f32_16x16x32_bf16 a[16:19], a[208:211], v[152:155], a[16:19]
  s_add_u32 m0, s37, 24576
  v_add_u32_e32 v238, s38, v5
  v_exp_f32_e32 v138, v138
  v_mfma_f32_16x16x32_bf16 a[20:23], a[212:215], v[152:155], a[20:23]
  ds_read_b128 a[96:99], v8
  ds_read_b128 a[100:103], v236
  buffer_load_dwordx4 v3, s[20:23], s41 offen nt lds
  v_add_f32_e32 v157, v138, v157
  v_mfma_f32_16x16x32_bf16 a[24:27], a[216:219], v[152:155], a[24:27]
  ds_read_b128 a[104:107], v237
  ds_read_b128 a[108:111], v238
  v_exp_f32_e32 v139, v139
  v_mfma_f32_16x16x32_bf16 a[28:31], a[220:223], v[152:155], a[28:31]
  ds_read_b128 a[112:115], v8 offset:4096
  buffer_load_dwordx4 v4, s[20:23], s41 offen offset:1024 nt lds
  v_add_f32_e32 v157, v139, v157
  s_sleep 1
  v_mfma_f32_16x16x32_bf16 v[144:147], a[128:131], v[64:67], 0
  s_add_u32 m0, s37, 40960
  ds_read_b128 a[116:119], v236 offset:4096
  ds_read_b128 a[120:123], v237 offset:4096
  v_exp_f32_e32 v140, v140
  v_mfma_f32_16x16x32_bf16 v[144:147], a[132:135], v[68:71], v[144:147]
  ds_read_b128 a[124:127], v238 offset:4096
  buffer_load_dwordx4 v3, s[20:23], s42 offen nt lds
  v_cvt_pk_bf16_f32 v152, v136, v137
  v_cvt_pk_bf16_f32 v153, v138, v139
  v_mfma_f32_16x16x32_bf16 v[144:147], a[136:139], v[72:75], v[144:147]
  v_exp_f32_e32 v141, v141
  v_mfma_f32_16x16x32_bf16 v[144:147], a[140:143], v[76:79], v[144:147]
  buffer_load_dwordx4 v4, s[20:23], s42 offen offset:1024 nt lds
  v_add_f32_e32 v157, v140, v157
  v_cvt_pk_bf16_f32 v154, v140, v141
  v_mfma_f32_16x16x32_bf16 v[148:151], a[144:147], v[64:67], 0
  s_add_u32 m0, s37, 57344
  v_exp_f32_e32 v142, v142
  v_mfma_f32_16x16x32_bf16 v[148:151], a[148:151], v[68:71], v[148:151]
  buffer_load_dwordx4 v3, s[20:23], s43 offen nt lds
  v_add_f32_e32 v157, v141, v157
  v_add_f32_e32 v157, v142, v157
  v_mfma_f32_16x16x32_bf16 v[148:151], a[152:155], v[72:75], v[148:151]
  v_exp_f32_e32 v143, v143
  v_mfma_f32_16x16x32_bf16 v[148:151], a[156:159], v[76:79], v[148:151]
  buffer_load_dwordx4 v4, s[20:23], s43 offen offset:1024 nt lds
  v_cvt_pk_bf16_f32 v155, v142, v143
  v_add_f32_e32 v157, v143, v157
  s_nop 0
  v_mfma_f32_16x16x32_bf16 a[32:35], a[192:195], v[152:155], a[32:35]
  s_add_u32 s38, s85, s96
  v_add_u32_e32 v8, s38, v5
  s_add_u32 s38, s85, s97
  v_exp_f32_e32 v144, v144
  v_mfma_f32_16x16x32_bf16 a[36:39], a[196:199], v[152:155], a[36:39]
  v_add_u32_e32 v236, s38, v5
  s_add_u32 s38, s85, s98
  v_add_u32_e32 v237, s38, v5
  v_exp_f32_e32 v145, v145
  v_mfma_f32_16x16x32_bf16 a[40:43], a[200:203], v[152:155], a[40:43]
  s_xor_b32 s87, s87, 0x10000
  s_add_u32 s38, s85, s99
  v_add_u32_e32 v238, s38, v5
  v_add_u32_e32 v9, s84, v6
  v_exp_f32_e32 v146, v146
  v_mfma_f32_16x16x32_bf16 a[44:47], a[204:207], v[152:155], a[44:47]
  v_add_u32_e32 v10, s84, v7
  ds_read_b64_tr_b16 a[160:161], v9
  v_exp_f32_e32 v147, v147
  v_mfma_f32_16x16x32_bf16 a[48:51], a[208:211], v[152:155], a[48:51]
  ds_read_b64_tr_b16 a[162:163], v9 offset:4096
  ds_read_b64_tr_b16 a[164:165], v10
  ds_read_b64_tr_b16 a[166:167], v10 offset:4096
  v_exp_f32_e32 v148, v148
  v_mfma_f32_16x16x32_bf16 a[52:55], a[212:215], v[152:155], a[52:55]
  ds_read_b64_tr_b16 a[168:169], v9 offset:512
  ds_read_b64_tr_b16 a[170:171], v9 offset:4608
  ds_read_b64_tr_b16 a[172:173], v10 offset:512
  v_exp_f32_e32 v149, v149
  v_mfma_f32_16x16x32_bf16 a[56:59], a[216:219], v[152:155], a[56:59]
  ds_read_b64_tr_b16 a[174:175], v10 offset:4608
  ds_read_b64_tr_b16 a[176:177], v9 offset:1024
  ds_read_b64_tr_b16 a[178:179], v9 offset:5120
  v_exp_f32_e32 v150, v150
  v_mfma_f32_16x16x32_bf16 a[60:63], a[220:223], v[152:155], a[60:63]
  ds_read_b128 a[128:131], v8
  ds_read_b128 a[132:135], v236
  v_exp_f32_e32 v151, v151
  v_add_f32_e32 v158, v144, v158
  v_cvt_pk_bf16_f32 v152, v144, v145
  v_cvt_pk_bf16_f32 v153, v146, v147
  v_cvt_pk_bf16_f32 v154, v148, v149
  v_cvt_pk_bf16_f32 v155, v150, v151
  v_add_f32_e32 v158, v145, v158
  v_add_f32_e32 v158, v146, v158
  v_mfma_f32_16x16x32_bf16 a[64:67], a[192:195], v[152:155], a[64:67]
  ds_read_b64_tr_b16 a[180:181], v10 offset:1024
  ds_read_b64_tr_b16 a[182:183], v10 offset:5120
  ds_read_b64_tr_b16 a[184:185], v9 offset:1536
  v_add_f32_e32 v158, v147, v158
  v_mfma_f32_16x16x32_bf16 a[68:71], a[196:199], v[152:155], a[68:71]
  ds_read_b64_tr_b16 a[186:187], v9 offset:5632
  ds_read_b64_tr_b16 a[188:189], v10 offset:1536
  v_add_f32_e32 v158, v148, v158
  v_mfma_f32_16x16x32_bf16 a[72:75], a[200:203], v[152:155], a[72:75]
  s_xor_b32 s86, s86, 0x10000
  ds_read_b64_tr_b16 a[190:191], v10 offset:5632
  ds_read_b128 a[136:139], v237
  ds_read_b128 a[140:143], v238
  v_add_f32_e32 v158, v149, v158
  v_mfma_f32_16x16x32_bf16 a[76:79], a[204:207], v[152:155], a[76:79]
  ds_read_b128 a[144:147], v8 offset:4096
  ds_read_b128 a[148:151], v236 offset:4096
  v_add_f32_e32 v158, v150, v158
  v_mfma_f32_16x16x32_bf16 a[80:83], a[208:211], v[152:155], a[80:83]
  ds_read_b128 a[152:155], v237 offset:4096
  ds_read_b128 a[156:159], v238 offset:4096
  v_add_f32_e32 v158, v151, v158
  v_mfma_f32_16x16x32_bf16 a[84:87], a[212:215], v[152:155], a[84:87]
  v_mfma_f32_16x16x32_bf16 a[88:91], a[216:219], v[152:155], a[88:91]
  v_mfma_f32_16x16x32_bf16 a[92:95], a[220:223], v[152:155], a[92:95]
  s_nop 7
  s_lshl_b32 s50, s36, 12
  s_add_u32 s50, s50, 131072
  s_lshl_b32 s51, s36, 10
  s_add_u32 s51, s51, 131072
  v_add_u32_e32 v240, s50, v0
  v_add_u32_e32 v241, s51, v0
  v_and_b32_e32 v242, 0xf0, v0
  s_lshl_b32 s50, s36, 2
  v_add_u32_e32 v242, 147456, v242
  v_add_u32_e32 v243, s50, v242
  v_mov_b32_e32 v160, v156
  v_mov_b32_e32 v161, v157
  v_mov_b32_e32 v162, v158
  s_nop 0
  v_permlane16_swap_b32_e32 v160, v156
  v_permlane16_swap_b32_e32 v161, v157
  v_permlane16_swap_b32_e32 v162, v158
  v_add_f32_e32 v156, v160, v156
  v_add_f32_e32 v157, v161, v157
  v_add_f32_e32 v158, v162, v158
  v_mov_b32_e32 v160, v156
  v_mov_b32_e32 v161, v157
  v_mov_b32_e32 v162, v158
  s_nop 0
  v_permlane32_swap_b32_e32 v160, v156
  v_permlane32_swap_b32_e32 v161, v157
  v_permlane32_swap_b32_e32 v162, v158
  v_add_f32_e32 v156, v160, v156
  v_add_f32_e32 v157, v161, v157
  v_add_f32_e32 v158, v162, v158
  ds_write_b32 v243, v158
  v_accvgpr_read_b32 v160, a64
  v_accvgpr_read_b32 v161, a65
  v_accvgpr_read_b32 v162, a66
  v_accvgpr_read_b32 v163, a67
  v_accvgpr_read_b32 v164, a68
  v_accvgpr_read_b32 v165, a69
  v_accvgpr_read_b32 v166, a70
  v_accvgpr_read_b32 v167, a71
  v_cvt_pk_bf16_f32 v168, v160, v161
  v_cvt_pk_bf16_f32 v169, v162, v163
  v_cvt_pk_bf16_f32 v170, v164, v165
  v_cvt_pk_bf16_f32 v171, v166, v167
  ds_write_b128 v240, v[168:171]
  v_accvgpr_read_b32 v172, a72
  v_accvgpr_read_b32 v173, a73
  v_accvgpr_read_b32 v174, a74
  v_accvgpr_read_b32 v175, a75
  v_accvgpr_read_b32 v176, a76
  v_accvgpr_read_b32 v177, a77
  v_accvgpr_read_b32 v178, a78
  v_accvgpr_read_b32 v179, a79
  v_cvt_pk_bf16_f32 v180, v172, v173
  v_cvt_pk_bf16_f32 v181, v174, v175
  v_cvt_pk_bf16_f32 v182, v176, v177
  v_cvt_pk_bf16_f32 v183, v178, v179
  ds_write_b128 v240, v[180:183] offset:1024
  v_accvgpr_read_b32 v160, a80
  v_accvgpr_read_b32 v161, a81
  v_accvgpr_read_b32 v162, a82
  v_accvgpr_read_b32 v163, a83
  v_accvgpr_read_b32 v164, a84
  v_accvgpr_read_b32 v165, a85
  v_accvgpr_read_b32 v166, a86
  v_accvgpr_read_b32 v167, a87
  v_cvt_pk_bf16_f32 v168, v160, v161
  v_cvt_pk_bf16_f32 v169, v162, v163
  v_cvt_pk_bf16_f32 v170, v164, v165
  v_cvt_pk_bf16_f32 v171, v166, v167
  ds_write_b128 v240, v[168:171] offset:2048
  v_accvgpr_read_b32 v172, a88
  v_accvgpr_read_b32 v173, a89
  v_accvgpr_read_b32 v174, a90
  v_accvgpr_read_b32 v175, a91
  v_accvgpr_read_b32 v176, a92
  v_accvgpr_read_b32 v177, a93
  v_accvgpr_read_b32 v178, a94
  v_accvgpr_read_b32 v179, a95
  v_cvt_pk_bf16_f32 v180, v172, v173
  v_cvt_pk_bf16_f32 v181, v174, v175
  v_cvt_pk_bf16_f32 v182, v176, v177
  v_cvt_pk_bf16_f32 v183, v178, v179
  ds_write_b128 v240, v[180:183] offset:3072
  v_min_f32_e32 v244, v156, v157
  v_max_f32_e32 v245, v156, v157
  v_rcp_f32_e32 v156, v156
  v_rcp_f32_e32 v157, v157
  v_mov_b32_e32 v28, 0
  v_mov_b32_e32 v29, 0
  v_mov_b32_e32 v30, 0
  v_mov_b32_e32 v31, 0
  s_lshl_b32 s50, s36, 18
  s_add_u32 s50, s50, s48
  s_add_u32 s52, s50, 131072
  s_lshl_b32 s51, s36, 10
  s_add_u32 s51, s51, s48
  s_add_u32 s51, s51, 1048576
  s_waitcnt lgkmcnt(0)
  s_barrier
  ds_read_b128 v[208:211], v241
  ds_read_b128 v[212:215], v241 offset:4096
  ds_read_b128 v[216:219], v241 offset:8192
  ds_read_b128 v[220:223], v241 offset:12288
  ds_read_b128 v[224:227], v242
  v_accvgpr_read_b32 v160, a0
  v_accvgpr_read_b32 v161, a1
  v_accvgpr_read_b32 v162, a2
  v_accvgpr_read_b32 v163, a3
  v_accvgpr_read_b32 v164, a4
  v_accvgpr_read_b32 v165, a5
  v_accvgpr_read_b32 v166, a6
  v_accvgpr_read_b32 v167, a7
  v_pk_mul_f32 v[160:161], v[160:161], v[156:157] op_sel:[0,0] op_sel_hi:[1,0]
  v_pk_mul_f32 v[162:163], v[162:163], v[156:157] op_sel:[0,0] op_sel_hi:[1,0]
  v_pk_mul_f32 v[164:165], v[164:165], v[156:157] op_sel:[0,0] op_sel_hi:[1,0]
  v_pk_mul_f32 v[166:167], v[166:167], v[156:157] op_sel:[0,0] op_sel_hi:[1,0]
  v_cvt_pk_bf16_f32 v168, v160, v161
  v_cvt_pk_bf16_f32 v169, v162, v163
  v_cvt_pk_bf16_f32 v170, v164, v165
  v_cvt_pk_bf16_f32 v171, v166, v167
  buffer_store_dwordx4 v[168:171], v0, s[64:67], s50 offen sc1
  v_pk_fma_f32 v[28:29], v[160:161], v[30:31], v[28:29]
  v_pk_fma_f32 v[28:29], v[162:163], v[30:31], v[28:29]
  v_pk_fma_f32 v[28:29], v[164:165], v[30:31], v[28:29]
  v_pk_fma_f32 v[28:29], v[166:167], v[30:31], v[28:29]
  s_waitcnt lgkmcnt(0)
  v_mfma_f32_16x16x32_bf16 v[228:231], v[22:25], v[208:211], 0
  v_mfma_f32_16x16x32_bf16 v[232:235], v[20:23], v[208:211], 0
  v_mfma_f32_16x16x32_bf16 v[228:231], v[22:25], v[212:215], v[228:231]
  v_mfma_f32_16x16x32_bf16 v[232:235], v[20:23], v[212:215], v[232:235]
  v_mfma_f32_16x16x32_bf16 v[228:231], v[22:25], v[216:219], v[228:231]
  v_mfma_f32_16x16x32_bf16 v[232:235], v[20:23], v[216:219], v[232:235]
  v_mfma_f32_16x16x32_bf16 v[228:231], v[22:25], v[220:223], v[228:231]
  v_mfma_f32_16x16x32_bf16 v[232:235], v[20:23], v[220:223], v[232:235]
  v_pk_add_f32 v[224:225], v[224:225], v[226:227]
  s_nop 0
  v_add_f32_e32 v158, v224, v225
  v_min_f32_e32 v244, v244, v158
  v_max_f32_e32 v245, v245, v158
  v_rcp_f32_e32 v158, v158
  v_cmp_nle_f32_e32 vcc, 0x3a800000, v244
  s_nop 0
  s_mov_b64 s[56:57], vcc
  v_cmp_nge_f32_e32 vcc, 0x5f800000, v245
  s_nop 0
  s_or_b64 s[56:57], s[56:57], vcc
  v_accvgpr_read_b32 v172, a8
  v_accvgpr_read_b32 v173, a9
  v_accvgpr_read_b32 v174, a10
  v_accvgpr_read_b32 v175, a11
  v_accvgpr_read_b32 v176, a12
  v_accvgpr_read_b32 v177, a13
  v_accvgpr_read_b32 v178, a14
  v_accvgpr_read_b32 v179, a15
  v_pk_mul_f32 v[172:173], v[172:173], v[156:157] op_sel:[0,0] op_sel_hi:[1,0]
  v_pk_mul_f32 v[174:175], v[174:175], v[156:157] op_sel:[0,0] op_sel_hi:[1,0]
  v_pk_mul_f32 v[176:177], v[176:177], v[156:157] op_sel:[0,0] op_sel_hi:[1,0]
  v_pk_mul_f32 v[178:179], v[178:179], v[156:157] op_sel:[0,0] op_sel_hi:[1,0]
  v_cvt_pk_bf16_f32 v180, v172, v173
  v_cvt_pk_bf16_f32 v181, v174, v175
  v_cvt_pk_bf16_f32 v182, v176, v177
  v_cvt_pk_bf16_f32 v183, v178, v179
  buffer_store_dwordx4 v[180:183], v0, s[64:67], s50 offen offset:1024 sc1
  v_pk_fma_f32 v[28:29], v[172:173], v[30:31], v[28:29]
  v_pk_fma_f32 v[28:29], v[174:175], v[30:31], v[28:29]
  v_pk_fma_f32 v[28:29], v[176:177], v[30:31], v[28:29]
  v_pk_fma_f32 v[28:29], v[178:179], v[30:31], v[28:29]
  v_pk_mul_f32 v[196:197], v[228:229], v[158:159] op_sel:[0,0] op_sel_hi:[1,0]
  v_pk_mul_f32 v[198:199], v[230:231], v[158:159] op_sel:[0,0] op_sel_hi:[1,0]
  v_pk_mul_f32 v[200:201], v[232:233], v[158:159] op_sel:[0,0] op_sel_hi:[1,0]
  v_pk_mul_f32 v[202:203], v[234:235], v[158:159] op_sel:[0,0] op_sel_hi:[1,0]
  v_cvt_pk_bf16_f32 v204, v196, v197
  v_cvt_pk_bf16_f32 v205, v198, v199
  v_cvt_pk_bf16_f32 v206, v200, v201
  v_cvt_pk_bf16_f32 v207, v202, v203
  buffer_store_dwordx4 v[204:207], v0, s[64:67], s51 offen sc1
  v_pk_fma_f32 v[28:29], v[196:197], v[30:31], v[28:29]
  v_pk_fma_f32 v[28:29], v[198:199], v[30:31], v[28:29]
  v_pk_fma_f32 v[28:29], v[200:201], v[30:31], v[28:29]
  v_pk_fma_f32 v[28:29], v[202:203], v[30:31], v[28:29]
  v_accvgpr_read_b32 v184, a16
  v_accvgpr_read_b32 v185, a17
  v_accvgpr_read_b32 v186, a18
  v_accvgpr_read_b32 v187, a19
  v_accvgpr_read_b32 v188, a20
  v_accvgpr_read_b32 v189, a21
  v_accvgpr_read_b32 v190, a22
  v_accvgpr_read_b32 v191, a23
  v_pk_mul_f32 v[184:185], v[184:185], v[156:157] op_sel:[0,0] op_sel_hi:[1,0]
  v_pk_mul_f32 v[186:187], v[186:187], v[156:157] op_sel:[0,0] op_sel_hi:[1,0]
  v_pk_mul_f32 v[188:189], v[188:189], v[156:157] op_sel:[0,0] op_sel_hi:[1,0]
  v_pk_mul_f32 v[190:191], v[190:191], v[156:157] op_sel:[0,0] op_sel_hi:[1,0]
  v_cvt_pk_bf16_f32 v192, v184, v185
  v_cvt_pk_bf16_f32 v193, v186, v187
  v_cvt_pk_bf16_f32 v194, v188, v189
  v_cvt_pk_bf16_f32 v195, v190, v191
  buffer_store_dwordx4 v[192:195], v0, s[64:67], s50 offen offset:2048 sc1
  v_pk_fma_f32 v[28:29], v[184:185], v[30:31], v[28:29]
  v_pk_fma_f32 v[28:29], v[186:187], v[30:31], v[28:29]
  v_pk_fma_f32 v[28:29], v[188:189], v[30:31], v[28:29]
  v_pk_fma_f32 v[28:29], v[190:191], v[30:31], v[28:29]
  v_accvgpr_read_b32 v160, a24
  v_accvgpr_read_b32 v161, a25
  v_accvgpr_read_b32 v162, a26
  v_accvgpr_read_b32 v163, a27
  v_accvgpr_read_b32 v164, a28
  v_accvgpr_read_b32 v165, a29
  v_accvgpr_read_b32 v166, a30
  v_accvgpr_read_b32 v167, a31
  v_pk_mul_f32 v[160:161], v[160:161], v[156:157] op_sel:[0,0] op_sel_hi:[1,0]
  v_pk_mul_f32 v[162:163], v[162:163], v[156:157] op_sel:[0,0] op_sel_hi:[1,0]
  v_pk_mul_f32 v[164:165], v[164:165], v[156:157] op_sel:[0,0] op_sel_hi:[1,0]
  v_pk_mul_f32 v[166:167], v[166:167], v[156:157] op_sel:[0,0] op_sel_hi:[1,0]
  v_cvt_pk_bf16_f32 v168, v160, v161
  v_cvt_pk_bf16_f32 v169, v162, v163
  v_cvt_pk_bf16_f32 v170, v164, v165
  v_cvt_pk_bf16_f32 v171, v166, v167
  buffer_store_dwordx4 v[168:171], v0, s[64:67], s50 offen offset:3072 sc1
  v_pk_fma_f32 v[28:29], v[160:161], v[30:31], v[28:29]
  v_pk_fma_f32 v[28:29], v[162:163], v[30:31], v[28:29]
  v_pk_fma_f32 v[28:29], v[164:165], v[30:31], v[28:29]
  v_pk_fma_f32 v[28:29], v[166:167], v[30:31], v[28:29]
  v_accvgpr_read_b32 v172, a32
  v_accvgpr_read_b32 v173, a33
  v_accvgpr_read_b32 v174, a34
  v_accvgpr_read_b32 v175, a35
  v_accvgpr_read_b32 v176, a36
  v_accvgpr_read_b32 v177, a37
  v_accvgpr_read_b32 v178, a38
  v_accvgpr_read_b32 v179, a39
  v_pk_mul_f32 v[172:173], v[172:173], v[156:157] op_sel:[0,1] op_sel_hi:[1,1]
  v_pk_mul_f32 v[174:175], v[174:175], v[156:157] op_sel:[0,1] op_sel_hi:[1,1]
  v_pk_mul_f32 v[176:177], v[176:177], v[156:157] op_sel:[0,1] op_sel_hi:[1,1]
  v_pk_mul_f32 v[178:179], v[178:179], v[156:157] op_sel:[0,1] op_sel_hi:[1,1]
  v_cvt_pk_bf16_f32 v180, v172, v173
  v_cvt_pk_bf16_f32 v181, v174, v175
  v_cvt_pk_bf16_f32 v182, v176, v177
  v_cvt_pk_bf16_f32 v183, v178, v179
  buffer_store_dwordx4 v[180:183], v0, s[64:67], s52 offen sc1
  v_pk_fma_f32 v[28:29], v[172:173], v[30:31], v[28:29]
  v_pk_fma_f32 v[28:29], v[174:175], v[30:31], v[28:29]
  v_pk_fma_f32 v[28:29], v[176:177], v[30:31], v[28:29]
  v_pk_fma_f32 v[28:29], v[178:179], v[30:31], v[28:29]
  v_accvgpr_read_b32 v184, a40
  v_accvgpr_read_b32 v185, a41
  v_accvgpr_read_b32 v186, a42
  v_accvgpr_read_b32 v187, a43
  v_accvgpr_read_b32 v188, a44
  v_accvgpr_read_b32 v189, a45
  v_accvgpr_read_b32 v190, a46
  v_accvgpr_read_b32 v191, a47
  v_pk_mul_f32 v[184:185], v[184:185], v[156:157] op_sel:[0,1] op_sel_hi:[1,1]
  v_pk_mul_f32 v[186:187], v[186:187], v[156:157] op_sel:[0,1] op_sel_hi:[1,1]
  v_pk_mul_f32 v[188:189], v[188:189], v[156:157] op_sel:[0,1] op_sel_hi:[1,1]
  v_pk_mul_f32 v[190:191], v[190:191], v[156:157] op_sel:[0,1] op_sel_hi:[1,1]
  v_cvt_pk_bf16_f32 v192, v184, v185
  v_cvt_pk_bf16_f32 v193, v186, v187
  v_cvt_pk_bf16_f32 v194, v188, v189
  v_cvt_pk_bf16_f32 v195, v190, v191
  buffer_store_dwordx4 v[192:195], v0, s[64:67], s52 offen offset:1024 sc1
  v_pk_fma_f32 v[28:29], v[184:185], v[30:31], v[28:29]
  v_pk_fma_f32 v[28:29], v[186:187], v[30:31], v[28:29]
  v_pk_fma_f32 v[28:29], v[188:189], v[30:31], v[28:29]
  v_pk_fma_f32 v[28:29], v[190:191], v[30:31], v[28:29]
  v_accvgpr_read_b32 v160, a48
  v_accvgpr_read_b32 v161, a49
  v_accvgpr_read_b32 v162, a50
  v_accvgpr_read_b32 v163, a51
  v_accvgpr_read_b32 v164, a52
  v_accvgpr_read_b32 v165, a53
  v_accvgpr_read_b32 v166, a54
  v_accvgpr_read_b32 v167, a55
  v_pk_mul_f32 v[160:161], v[160:161], v[156:157] op_sel:[0,1] op_sel_hi:[1,1]
  v_pk_mul_f32 v[162:163], v[162:163], v[156:157] op_sel:[0,1] op_sel_hi:[1,1]
  v_pk_mul_f32 v[164:165], v[164:165], v[156:157] op_sel:[0,1] op_sel_hi:[1,1]
  v_pk_mul_f32 v[166:167], v[166:167], v[156:157] op_sel:[0,1] op_sel_hi:[1,1]
  v_cvt_pk_bf16_f32 v168, v160, v161
  v_cvt_pk_bf16_f32 v169, v162, v163
  v_cvt_pk_bf16_f32 v170, v164, v165
  v_cvt_pk_bf16_f32 v171, v166, v167
  buffer_store_dwordx4 v[168:171], v0, s[64:67], s52 offen offset:2048 sc1
  v_pk_fma_f32 v[28:29], v[160:161], v[30:31], v[28:29]
  v_pk_fma_f32 v[28:29], v[162:163], v[30:31], v[28:29]
  v_pk_fma_f32 v[28:29], v[164:165], v[30:31], v[28:29]
  v_pk_fma_f32 v[28:29], v[166:167], v[30:31], v[28:29]
  v_accvgpr_read_b32 v172, a56
  v_accvgpr_read_b32 v173, a57
  v_accvgpr_read_b32 v174, a58
  v_accvgpr_read_b32 v175, a59
  v_accvgpr_read_b32 v176, a60
  v_accvgpr_read_b32 v177, a61
  v_accvgpr_read_b32 v178, a62
  v_accvgpr_read_b32 v179, a63
  v_pk_mul_f32 v[172:173], v[172:173], v[156:157] op_sel:[0,1] op_sel_hi:[1,1]
  v_pk_mul_f32 v[174:175], v[174:175], v[156:157] op_sel:[0,1] op_sel_hi:[1,1]
  v_pk_mul_f32 v[176:177], v[176:177], v[156:157] op_sel:[0,1] op_sel_hi:[1,1]
  v_pk_mul_f32 v[178:179], v[178:179], v[156:157] op_sel:[0,1] op_sel_hi:[1,1]
  v_cvt_pk_bf16_f32 v180, v172, v173
  v_cvt_pk_bf16_f32 v181, v174, v175
  v_cvt_pk_bf16_f32 v182, v176, v177
  v_cvt_pk_bf16_f32 v183, v178, v179
  buffer_store_dwordx4 v[180:183], v0, s[64:67], s52 offen offset:3072 sc1
  v_pk_fma_f32 v[28:29], v[172:173], v[30:31], v[28:29]
  v_pk_fma_f32 v[28:29], v[174:175], v[30:31], v[28:29]
  v_pk_fma_f32 v[28:29], v[176:177], v[30:31], v[28:29]
  v_pk_fma_f32 v[28:29], v[178:179], v[30:31], v[28:29]
  v_cmp_u_f32_e32 vcc, v28, v29
  s_nop 0
  s_or_b64 s[56:57], s[56:57], vcc
  s_cmp_lg_u64 s[56:57], 0
  s_cselect_b32 s50, 1, 0
  s_lshl_b32 s50, s50, s45
  s_or_b32 s46, s46, s50
  s_lshl_b32 s50, s36, 2
  s_add_u32 s50, s50, 151552
  v_mov_b32_e32 v240, s50
  v_mov_b32_e32 v241, s46
  ds_write_b32 v240, v241
  s_add_u32 s45, s45, 1
  s_add_u32 s48, s48, 16384
  s_cmp_ge_u32 s45, 2
  s_cbranch_scc1 .Lidle%=
  v_mov_b32_e32 v32, v80
  v_mov_b32_e32 v33, v81
  v_mov_b32_e32 v34, v82
  v_mov_b32_e32 v35, v83
  v_mov_b32_e32 v36, v84
  v_mov_b32_e32 v37, v85
  v_mov_b32_e32 v38, v86
  v_mov_b32_e32 v39, v87
  v_mov_b32_e32 v40, v88
  v_mov_b32_e32 v41, v89
  v_mov_b32_e32 v42, v90
  v_mov_b32_e32 v43, v91
  v_mov_b32_e32 v44, v92
  v_mov_b32_e32 v45, v93
  v_mov_b32_e32 v46, v94
  v_mov_b32_e32 v47, v95
  v_mov_b32_e32 v48, v96
  v_mov_b32_e32 v49, v97
  v_mov_b32_e32 v50, v98
  v_mov_b32_e32 v51, v99
  v_mov_b32_e32 v52, v100
  v_mov_b32_e32 v53, v101
  v_mov_b32_e32 v54, v102
  v_mov_b32_e32 v55, v103
  v_mov_b32_e32 v56, v104
  v_mov_b32_e32 v57, v105
  v_mov_b32_e32 v58, v106
  v_mov_b32_e32 v59, v107
  v_mov_b32_e32 v60, v108
  v_mov_b32_e32 v61, v109
  v_mov_b32_e32 v62, v110
  v_mov_b32_e32 v63, v111
  v_mov_b32_e32 v64, v112
  v_mov_b32_e32 v65, v113
  v_mov_b32_e32 v66, v114
  v_mov_b32_e32 v67, v115
  v_mov_b32_e32 v68, v116
  v_mov_b32_e32 v69, v117
  v_mov_b32_e32 v70, v118
  v_mov_b32_e32 v71, v119
  v_mov_b32_e32 v72, v120
  v_mov_b32_e32 v73, v121
  v_mov_b32_e32 v74, v122
  v_mov_b32_e32 v75, v123
  v_mov_b32_e32 v76, v124
  v_mov_b32_e32 v77, v125
  v_mov_b32_e32 v78, v126
  v_mov_b32_e32 v79, v127
  s_branch .Litem%=
.Lsig0%=:
  s_mov_b32 s47, 1024
  v_lshlrev_b32_e32 v19, 12, v0
  s_bfe_u32 s68, %[gid], 0x10002
  s_lshr_b32 s69, %[gid], 3
  s_mul_i32 s70, s69, 52
  s_lshr_b32 s70, s70, 8
  s_mul_i32 s71, s70, 5
  s_sub_u32 s71, s69, s71
  s_mul_i32 s72, s70, 43
  s_lshr_b32 s72, s72, 5
  s_min_u32 s69, s69, 29
  s_min_u32 s70, s70, 5
  s_mul_i32 s73, s68, 30
  s_add_u32 s73, s73, s69
  s_mul_i32 s73, s73, 147456
  s_add_u32 s74, %[gid], 1
  s_and_b32 s74, s74, 3
  s_mul_i32 s74, s74, 36864
  s_add_u32 s73, s73, s74
  s_add_u32 s73, s73, 19988480
  s_add_u32 s76, s88, s73
  s_addc_u32 s77, s89, 0
  s_mov_b32 s78, 2048
  s_mov_b32 s79, 0x20000
  s_cmp_lt_u32 %[gid], 240
  s_cselect_b32 s78, s78, 0
  s_cmp_eq_u32 s36, 1
  s_cselect_b32 s75, s47, 0x7c000000
  buffer_load_dword v18, v19, s[76:79], s75 offen
  s_mul_i32 s73, s70, 491520
  s_add_u32 s76, s90, s73
  s_addc_u32 s77, s91, 0
  s_mov_b32 s78, 491520
  s_cmp_eq_u32 s36, 2
  s_cselect_b32 s75, s47, 0x7c000000
  buffer_load_dword v18, v19, s[76:79], s75 offen
  s_bfe_u32 s68, %[gid], 0x10002
  s_lshr_b32 s69, %[gid], 3
  s_mul_i32 s70, s69, 52
  s_lshr_b32 s70, s70, 8
  s_mul_i32 s71, s70, 5
  s_sub_u32 s71, s69, s71
  s_mul_i32 s72, s70, 43
  s_lshr_b32 s72, s72, 5
  v_lshrrev_b32_e32 v248, 6, v0
  v_add_u32_e32 v248, s72, v248
  v_and_b32_e32 v248, 7, v248
  v_bfe_u32 v249, v0, 4, 2
  v_lshl_add_u32 v248, v248, 2, v249
  s_and_b32 s73, %[gid], 7
  v_lshl_add_u32 v248, v248, 3, s73
  v_lshlrev_b32_e32 v26, 6, v248
  s_lshl_b32 s73, %[gid], 6
  v_mov_b32_e32 v27, s73
  v_mov_b32_e32 v252, 0x25000
  ds_read_b128 v[252:255], v252
  s_waitcnt lgkmcnt(0)
  v_or3_b32 v252, v252, v253, v254
  v_or_b32_e32 v252, v252, v255
  s_nop 0
  v_readfirstlane_b32 s79, v252
  s_nop 0
  s_or_b32 s73, s79, s36
  s_cbranch_scc1 .Lbody2%=
  s_mov_b64 exec, 1
  v_mov_b32_e32 v250, s82
  v_mov_b32_e32 v251, s83
  global_store_dwordx2 v27, v[250:251], s[92:93] offset:16 sc1
  s_mov_b64 exec, -1
  s_nop 0
  s_branch .Lbody2%=
.Lidle%=:
  s_waitcnt vmcnt(0) lgkmcnt(0)
  s_barrier
  v_mov_b32_e32 v252, 0x25000
  ds_read_b128 v[252:255], v252
  s_waitcnt lgkmcnt(0)
  v_or3_b32 v252, v252, v253, v254
  v_or_b32_e32 v252, v252, v255
  s_nop 0
  v_readfirstlane_b32 %[bad], v252
  s_nop 0
  s_cmp_lg_u32 %[bad], 0
  s_cbranch_scc1 .Ldone%=
  s_cmp_lg_u32 s36, 0
  s_cbranch_scc1 .Ldone%=
  s_mov_b64 exec, 1
  v_mov_b32_e32 v250, s82
  v_mov_b32_e32 v251, s83
  global_store_dwordx2 v27, v[250:251], s[92:93] sc1
  s_mov_b64 exec, -1
  s_nop 0
  s_cmp_lg_u32 %[gid], 255
  s_cbranch_scc1 .Ldone%=
  s_cmp_lg_u32 s36, 0
  s_cbranch_scc1 .Ldone%=
  v_lshlrev_b32_e32 v26, 2, v0
  s_mov_b64 exec, -1
  s_mov_b32 s73, 0x20000
.Lmon0a%=:
  global_load_dwordx2 v[246:247], v26, s[92:93] sc1
  s_waitcnt vmcnt(0)
.Lmon0c%=:
  v_cmp_ne_u64_e32 vcc, s[82:83], v[246:247]
  s_sub_u32 s73, s73, 1
  s_cbranch_scc1 .Lmon0b%=
  s_nop 3
  s_cbranch_vccnz .Lmon0a%=
.Lmon0b%=:
  s_mov_b64 exec, -1
  v_add_u32_e32 v26, 0x1000, v26
  s_mov_b64 exec, -1
  s_mov_b32 s73, 0x20000
.Lmon1a%=:
  global_load_dwordx2 v[246:247], v26, s[92:93] sc1
  s_waitcnt vmcnt(0)
.Lmon1c%=:
  v_cmp_ne_u64_e32 vcc, s[82:83], v[246:247]
  s_sub_u32 s73, s73, 1
  s_cbranch_scc1 .Lmon1b%=
  s_nop 3
  s_cbranch_vccnz .Lmon1a%=
.Lmon1b%=:
  s_mov_b64 exec, -1
  v_add_u32_e32 v26, 0x1000, v26
  s_mov_b64 exec, -1
  s_mov_b32 s73, 0x20000
.Lmon2a%=:
  global_load_dwordx2 v[246:247], v26, s[92:93] sc1
  s_waitcnt vmcnt(0)
.Lmon2c%=:
  v_cmp_ne_u64_e32 vcc, s[82:83], v[246:247]
  s_sub_u32 s73, s73, 1
  s_cbranch_scc1 .Lmon2b%=
  s_nop 3
  s_cbranch_vccnz .Lmon2a%=
.Lmon2b%=:
  s_mov_b64 exec, -1
  v_add_u32_e32 v26, 0x1000, v26
  s_mov_b64 exec, -1
  s_mov_b32 s73, 0x20000
.Lmon3a%=:
  global_load_dwordx2 v[246:247], v26, s[92:93] sc1
  s_waitcnt vmcnt(0)
.Lmon3c%=:
  v_cmp_ne_u64_e32 vcc, s[82:83], v[246:247]
  s_sub_u32 s73, s73, 1
  s_cbranch_scc1 .Lmon3b%=
  s_nop 3
  s_cbranch_vccnz .Lmon3a%=
.Lmon3b%=:
  s_mov_b64 exec, -1
  v_add_u32_e32 v26, 0x1000, v26
  v_mov_b32_e32 v26, 0xff00
  s_mov_b64 exec, 1
  v_mov_b32_e32 v250, s82
  v_mov_b32_e32 v251, s83
  global_store_dwordx2 v26, v[250:251], s[92:93] sc1
  s_mov_b64 exec, -1
  s_nop 0
  s_branch .Ldone%=
.Lfinal%=:
  s_mov_b64 exec, 0xff
  s_mov_b32 s73, 0x20000
  s_branch .Lo0c%=
.Lo0a%=:
  global_load_dwordx2 v[246:247], v26, s[92:93] offset:16 sc1
  s_waitcnt vmcnt(0)
.Lo0c%=:
  v_cmp_ne_u64_e32 vcc, s[82:83], v[246:247]
  s_sub_u32 s73, s73, 1
  s_cbranch_scc1 .Lo0b%=
  s_nop 3
  s_cbranch_vccnz .Lo0a%=
.Lo0b%=:
  s_mov_b64 exec, -1
  v_mfma_f32_16x16x32_bf16 a[0:3], a[160:163], v[152:155], a[0:3]
  s_add_u32 s38, s86, s96
  v_add_u32_e32 v8, s38, v5
  v_exp_f32_e32 v136, v136
  s_bfe_u32 s68, %[gid], 0x10002
  s_lshr_b32 s69, %[gid], 3
  v_mfma_f32_16x16x32_bf16 a[4:7], a[164:167], v[152:155], a[4:7]
  s_add_u32 s38, s86, s97
  v_add_f32_e32 v157, v136, v157
  s_mul_i32 s70, s69, 52
  v_mfma_f32_16x16x32_bf16 a[8:11], a[168:171], v[152:155], a[8:11]
  v_add_u32_e32 v236, s38, v5
  s_add_u32 s38, s86, s98
  v_exp_f32_e32 v137, v137
  s_lshr_b32 s70, s70, 8
  s_mul_i32 s71, s70, 5
  v_mfma_f32_16x16x32_bf16 a[12:15], a[172:175], v[152:155], a[12:15]
  v_add_u32_e32 v237, s38, v5
  v_add_f32_e32 v157, v137, v157
  s_sub_u32 s71, s69, s71
  v_mfma_f32_16x16x32_bf16 a[16:19], a[176:179], v[152:155], a[16:19]
  s_add_u32 s38, s86, s99
  v_exp_f32_e32 v138, v138
  s_mul_i32 s72, s70, 43
  v_mfma_f32_16x16x32_bf16 a[20:23], a[180:183], v[152:155], a[20:23]
  v_add_u32_e32 v238, s38, v5
  v_add_u32_e32 v9, s85, v6
  v_add_f32_e32 v157, v138, v157
  s_lshr_b32 s72, s72, 5
  s_and_b32 s44, %[gid], 3
  v_mfma_f32_16x16x32_bf16 a[24:27], a[184:187], v[152:155], a[24:27]
  v_add_u32_e32 v10, s85, v7
  v_exp_f32_e32 v139, v139
  s_mul_i32 s47, s68, 30
  v_mfma_f32_16x16x32_bf16 a[28:31], a[188:191], v[152:155], a[28:31]
  ds_read_b128 a[96:99], v8
  v_add_f32_e32 v157, v139, v157
  s_add_u32 s47, s47, s69
  s_mul_i32 s74, s44, 1310720
  v_mfma_f32_16x16x32_bf16 v[144:147], a[128:131], v[32:35], 0
  ds_read_b128 a[100:103], v236
  ds_read_b128 a[104:107], v237
  v_exp_f32_e32 v140, v140
  s_lshl_b32 s75, s71, 8
  v_mfma_f32_16x16x32_bf16 v[144:147], a[132:135], v[36:39], v[144:147]
  ds_read_b128 a[108:111], v238
  v_cvt_pk_bf16_f32 v152, v136, v137
  v_cvt_pk_bf16_f32 v153, v138, v139
  s_add_u32 s74, s74, s75
  v_mfma_f32_16x16x32_bf16 v[144:147], a[136:139], v[40:43], v[144:147]
  ds_read_b64_tr_b16 a[192:193], v9
  ds_read_b64_tr_b16 a[194:195], v9 offset:4096
  v_exp_f32_e32 v141, v141
  s_add_u32 s20, s80, s74
  s_addc_u32 s21, s81, 0
  v_mfma_f32_16x16x32_bf16 v[144:147], a[140:143], v[44:47], v[144:147]
  ds_read_b64_tr_b16 a[196:197], v10
  v_add_f32_e32 v157, v140, v157
  v_cvt_pk_bf16_f32 v154, v140, v141
  s_mul_i32 s74, s68, 72
  v_mfma_f32_16x16x32_bf16 v[148:151], a[144:147], v[32:35], 0
  ds_read_b64_tr_b16 a[198:199], v10 offset:4096
  v_exp_f32_e32 v142, v142
  s_mul_i32 s75, s70, 12
  s_add_u32 s74, s74, s75
  v_mfma_f32_16x16x32_bf16 v[148:151], a[148:151], v[36:39], v[148:151]
  ds_read_b64_tr_b16 a[200:201], v9 offset:512
  ds_read_b64_tr_b16 a[202:203], v9 offset:4608
  v_add_f32_e32 v157, v141, v157
  v_add_f32_e32 v157, v142, v157
  s_mul_i32 s75, s36, 3
  v_mfma_f32_16x16x32_bf16 v[148:151], a[152:155], v[40:43], v[148:151]
  s_waitcnt lgkmcnt(6)
  ds_read_b64_tr_b16 a[204:205], v10 offset:512
  v_exp_f32_e32 v143, v143
  s_add_u32 s74, s74, s75
  v_mfma_f32_16x16x32_bf16 v[148:151], a[156:159], v[44:47], v[148:151]
  ds_read_b64_tr_b16 a[206:207], v10 offset:4608
  v_cvt_pk_bf16_f32 v155, v142, v143
  v_add_f32_e32 v157, v143, v157
  s_nop 0
  v_mfma_f32_16x16x32_bf16 a[32:35], a[160:163], v[152:155], a[32:35]
  ds_read_b64_tr_b16 a[208:209], v9 offset:1024
  v_exp_f32_e32 v144, v144
  s_lshl_b32 s74, s74, 2
  s_add_u32 s74, s74, s44
  v_mfma_f32_16x16x32_bf16 a[36:39], a[164:167], v[152:155], a[36:39]
  ds_read_b64_tr_b16 a[210:211], v9 offset:5120
  v_add_f32_e32 v156, v144, v156
  s_lshl_b32 s74, s74, 15
  v_mfma_f32_16x16x32_bf16 a[40:43], a[168:171], v[152:155], a[40:43]
  ds_read_b64_tr_b16 a[212:213], v10 offset:1024
  v_exp_f32_e32 v145, v145
  s_add_u32 s16, s88, s74
  s_addc_u32 s17, s89, 0
  v_mfma_f32_16x16x32_bf16 a[44:47], a[172:175], v[152:155], a[44:47]
  ds_read_b64_tr_b16 a[214:215], v10 offset:5120
  v_add_f32_e32 v156, v145, v156
  s_lshl_b32 s74, s70, 2
  v_mfma_f32_16x16x32_bf16 a[48:51], a[176:179], v[152:155], a[48:51]
  ds_read_b64_tr_b16 a[216:217], v9 offset:1536
  v_exp_f32_e32 v146, v146
  s_add_u32 s74, s74, s36
  v_mfma_f32_16x16x32_bf16 a[52:55], a[180:183], v[152:155], a[52:55]
  ds_read_b64_tr_b16 a[218:219], v9 offset:5632
  v_add_f32_e32 v156, v146, v156
  s_mul_i32 s74, s74, 122880
  s_mul_i32 s75, s68, 1280
  v_mfma_f32_16x16x32_bf16 a[56:59], a[184:187], v[152:155], a[56:59]
  ds_read_b64_tr_b16 a[220:221], v10 offset:1536
  v_exp_f32_e32 v147, v147
  s_add_u32 s74, s74, s75
  v_mfma_f32_16x16x32_bf16 a[60:63], a[188:191], v[152:155], a[60:63]
  ds_read_b64_tr_b16 a[222:223], v10 offset:5632
  v_add_f32_e32 v156, v147, v156
  s_lshl_b32 s75, s71, 8
  s_add_u32 s74, s74, s75
  v_mfma_f32_16x16x32_bf16 v[128:131], a[128:131], v[48:51], 0
  ds_read_b128 a[112:115], v8 offset:4096
  v_exp_f32_e32 v148, v148
  s_lshl_b32 s75, s44, 6
  v_mfma_f32_16x16x32_bf16 v[128:131], a[132:135], v[52:55], v[128:131]
  ds_read_b128 a[116:119], v236 offset:4096
  v_cvt_pk_bf16_f32 v152, v144, v145
  v_cvt_pk_bf16_f32 v153, v146, v147
  s_add_u32 s74, s74, s75
  v_mfma_f32_16x16x32_bf16 v[128:131], a[136:139], v[56:59], v[128:131]
  ds_read_b128 a[120:123], v237 offset:4096
  v_exp_f32_e32 v149, v149
  s_add_u32 s24, s90, s74
  s_addc_u32 s25, s91, 0
  v_mfma_f32_16x16x32_bf16 v[128:131], a[140:143], v[60:63], v[128:131]
  ds_read_b128 a[124:127], v238 offset:4096
  v_add_f32_e32 v156, v148, v156
  v_cvt_pk_bf16_f32 v154, v148, v149
  s_mul_i32 s74, s47, 147456
  v_mfma_f32_16x16x32_bf16 v[132:135], a[144:147], v[48:51], 0
  v_exp_f32_e32 v150, v150
  s_add_u32 s74, s74, 19988480
  s_add_u32 s28, s88, s74
  v_mfma_f32_16x16x32_bf16 v[132:135], a[148:151], v[52:55], v[132:135]
  v_add_f32_e32 v156, v149, v156
  v_add_f32_e32 v156, v150, v156
  s_addc_u32 s29, s89, 0
  v_mfma_f32_16x16x32_bf16 v[132:135], a[152:155], v[56:59], v[132:135]
  v_exp_f32_e32 v151, v151
  s_mov_b32 s18, 294912
  v_mfma_f32_16x16x32_bf16 v[132:135], a[156:159], v[60:63], v[132:135]
  s_waitcnt lgkmcnt(1)
  v_cvt_pk_bf16_f32 v155, v150, v151
  v_add_f32_e32 v156, v151, v156
  s_nop 0
  v_mfma_f32_16x16x32_bf16 a[0:3], a[192:195], v[152:155], a[0:3]
  s_add_u32 s38, s87, s96
  v_add_u32_e32 v8, s38, v5
  v_exp_f32_e32 v128, v128
  s_mov_b32 s19, 0x20000
  s_mov_b32 s22, 1309696
  v_mfma_f32_16x16x32_bf16 a[4:7], a[196:199], v[152:155], a[4:7]
  s_add_u32 s38, s87, s97
  v_add_f32_e32 v157, v128, v157
  s_mov_b32 s23, 0x20000
  v_mfma_f32_16x16x32_bf16 a[8:11], a[200:203], v[152:155], a[8:11]
  v_add_u32_e32 v236, s38, v5
  s_add_u32 s38, s87, s98
  v_exp_f32_e32 v129, v129
  s_mov_b32 s26, 120384
  s_mov_b32 s27, 0x20000
  v_mfma_f32_16x16x32_bf16 a[12:15], a[204:207], v[152:155], a[12:15]
  v_add_u32_e32 v237, s38, v5
  v_add_f32_e32 v157, v129, v157
  s_mov_b32 s30, 147456
  v_mfma_f32_16x16x32_bf16 a[16:19], a[208:211], v[152:155], a[16:19]
  s_add_u32 s38, s87, s99
  v_exp_f32_e32 v130, v130
  s_mov_b32 s31, 0x20000
  s_lshl_b32 s37, s36, 11
  v_mfma_f32_16x16x32_bf16 a[20:23], a[212:215], v[152:155], a[20:23]
  v_add_u32_e32 v238, s38, v5
  v_add_u32_e32 v9, s86, v6
  v_add_f32_e32 v157, v130, v157
  s_mul_i32 s74, s36, 10240
  v_mfma_f32_16x16x32_bf16 a[24:27], a[216:219], v[152:155], a[24:27]
  v_add_u32_e32 v10, s86, v7
  v_exp_f32_e32 v131, v131
  v_lshrrev_b32_e32 v15, 4, v0
  v_lshrrev_b32_e32 v16, 4, v15
  v_mfma_f32_16x16x32_bf16 a[28:31], a[220:223], v[152:155], a[28:31]
  ds_read_b128 a[128:131], v8
  v_add_f32_e32 v157, v131, v157
  v_and_b32_e32 v17, 15, v15
  v_mfma_f32_16x16x32_bf16 v[136:139], a[96:99], v[32:35], 0
  ds_read_b128 a[132:135], v236
  ds_read_b128 a[136:139], v237
  v_exp_f32_e32 v132, v132
  v_lshlrev_b32_e32 v18, 1, v16
  v_xor_b32_e32 v17, v17, v18
  v_mfma_f32_16x16x32_bf16 v[136:139], a[100:103], v[36:39], v[136:139]
  ds_read_b128 a[140:143], v238
  v_cvt_pk_bf16_f32 v152, v128, v129
  v_cvt_pk_bf16_f32 v153, v130, v131
  v_mul_u32_u24_e32 v16, 0x500, v16
  v_mfma_f32_16x16x32_bf16 v[136:139], a[104:107], v[40:43], v[136:139]
  ds_read_b64_tr_b16 a[160:161], v9
  ds_read_b64_tr_b16 a[162:163], v9 offset:4096
  v_exp_f32_e32 v133, v133
  v_add_u32_e32 v16, s74, v16
  v_lshl_add_u32 v11, v17, 4, v16
  v_mfma_f32_16x16x32_bf16 v[136:139], a[108:111], v[44:47], v[136:139]
  ds_read_b64_tr_b16 a[164:165], v10
  v_add_f32_e32 v157, v132, v157
  v_cvt_pk_bf16_f32 v154, v132, v133
  v_xor_b32_e32 v17, 8, v17
  v_mfma_f32_16x16x32_bf16 v[140:143], a[112:115], v[32:35], 0
  ds_read_b64_tr_b16 a[166:167], v10 offset:4096
  v_exp_f32_e32 v134, v134
  v_add_u32_e32 v16, 4096, v16
  v_lshl_add_u32 v12, v17, 4, v16
  v_mfma_f32_16x16x32_bf16 v[140:143], a[116:119], v[36:39], v[140:143]
  ds_read_b64_tr_b16 a[168:169], v9 offset:512
  ds_read_b64_tr_b16 a[170:171], v9 offset:4608
  v_add_f32_e32 v157, v133, v157
  v_add_f32_e32 v157, v134, v157
  v_add_u32_e32 v13, 0x20000, v0
  v_mfma_f32_16x16x32_bf16 v[140:143], a[120:123], v[40:43], v[140:143]
  s_waitcnt lgkmcnt(6)
  ds_read_b64_tr_b16 a[172:173], v10 offset:512
  v_exp_f32_e32 v135, v135
  v_add_u32_e32 v14, 0x40000, v0
  v_mfma_f32_16x16x32_bf16 v[140:143], a[124:127], v[44:47], v[140:143]
  ds_read_b64_tr_b16 a[174:175], v10 offset:4608
  v_cvt_pk_bf16_f32 v155, v134, v135
  v_add_f32_e32 v157, v135, v157
  s_nop 0
  v_mfma_f32_16x16x32_bf16 a[32:35], a[192:195], v[152:155], a[32:35]
  ds_read_b128 a[144:147], v8 offset:4096
  v_exp_f32_e32 v136, v136
  v_mfma_f32_16x16x32_bf16 a[36:39], a[196:199], v[152:155], a[36:39]
  ds_read_b128 a[148:151], v236 offset:4096
  v_add_f32_e32 v156, v136, v156
  v_mfma_f32_16x16x32_bf16 a[40:43], a[200:203], v[152:155], a[40:43]
  ds_read_b128 a[152:155], v237 offset:4096
  v_exp_f32_e32 v137, v137
  v_mfma_f32_16x16x32_bf16 a[44:47], a[204:207], v[152:155], a[44:47]
  ds_read_b128 a[156:159], v238 offset:4096
  v_add_f32_e32 v156, v137, v156
  v_mfma_f32_16x16x32_bf16 a[48:51], a[208:211], v[152:155], a[48:51]
  ds_read_b64_tr_b16 a[176:177], v9 offset:1024
  v_exp_f32_e32 v138, v138
  v_mfma_f32_16x16x32_bf16 a[52:55], a[212:215], v[152:155], a[52:55]
  ds_read_b64_tr_b16 a[178:179], v9 offset:5120
  v_add_f32_e32 v156, v138, v156
  v_mfma_f32_16x16x32_bf16 a[56:59], a[216:219], v[152:155], a[56:59]
  ds_read_b64_tr_b16 a[180:181], v10 offset:1024
  v_exp_f32_e32 v139, v139
  v_mfma_f32_16x16x32_bf16 a[60:63], a[220:223], v[152:155], a[60:63]
  ds_read_b64_tr_b16 a[182:183], v10 offset:5120
  v_add_f32_e32 v156, v139, v156
  v_mfma_f32_16x16x32_bf16 v[144:147], a[96:99], v[48:51], 0
  ds_read_b64_tr_b16 a[184:185], v9 offset:1536
  v_exp_f32_e32 v140, v140
  v_mfma_f32_16x16x32_bf16 v[144:147], a[100:103], v[52:55], v[144:147]
  ds_read_b64_tr_b16 a[186:187], v9 offset:5632
  v_cvt_pk_bf16_f32 v152, v136, v137
  v_cvt_pk_bf16_f32 v153, v138, v139
  v_mfma_f32_16x16x32_bf16 v[144:147], a[104:107], v[56:59], v[144:147]
  ds_read_b64_tr_b16 a[188:189], v10 offset:1536
  v_exp_f32_e32 v141, v141
  v_mfma_f32_16x16x32_bf16 v[144:147], a[108:111], v[60:63], v[144:147]
  ds_read_b64_tr_b16 a[190:191], v10 offset:5632
  v_add_f32_e32 v156, v140, v156
  v_cvt_pk_bf16_f32 v154, v140, v141
  v_mfma_f32_16x16x32_bf16 v[148:151], a[112:115], v[48:51], 0
  v_exp_f32_e32 v142, v142
  v_mfma_f32_16x16x32_bf16 v[148:151], a[116:119], v[52:55], v[148:151]
  v_add_f32_e32 v156, v141, v156
  v_add_f32_e32 v156, v142, v156
  v_mfma_f32_16x16x32_bf16 v[148:151], a[120:123], v[56:59], v[148:151]
  v_exp_f32_e32 v143, v143
  v_mfma_f32_16x16x32_bf16 v[148:151], a[124:127], v[60:63], v[148:151]
  s_waitcnt lgkmcnt(1)
  v_cvt_pk_bf16_f32 v155, v142, v143
  v_add_f32_e32 v156, v143, v156
  s_nop 0
  v_mfma_f32_16x16x32_bf16 a[0:3], a[160:163], v[152:155], a[0:3]
  v_exp_f32_e32 v144, v144
  s_waitcnt vmcnt(0) lgkmcnt(0)
  s_barrier
  v_mfma_f32_16x16x32_bf16 a[4:7], a[164:167], v[152:155], a[4:7]
  v_add_u32_e32 v9, s87, v6
  v_add_f32_e32 v157, v144, v157
  v_mfma_f32_16x16x32_bf16 a[8:11], a[168:171], v[152:155], a[8:11]
  s_mov_b32 s39, 0
  s_add_u32 m0, s37, 0
  v_add_u32_e32 v10, s87, v7
  v_exp_f32_e32 v145, v145
  v_mfma_f32_16x16x32_bf16 a[12:15], a[172:175], v[152:155], a[12:15]
  ds_read_b64_tr_b16 a[192:193], v9
  buffer_load_dwordx4 v11, s[20:23], s39 offen lds
  v_add_f32_e32 v157, v145, v157
  v_mfma_f32_16x16x32_bf16 a[16:19], a[176:179], v[152:155], a[16:19]
  ds_read_b64_tr_b16 a[194:195], v9 offset:4096
  v_exp_f32_e32 v146, v146
  v_mfma_f32_16x16x32_bf16 a[20:23], a[180:183], v[152:155], a[20:23]
  v_add_f32_e32 v157, v146, v157
  v_mfma_f32_16x16x32_bf16 a[24:27], a[184:187], v[152:155], a[24:27]
  ds_read_b64_tr_b16 a[196:197], v10
  v_exp_f32_e32 v147, v147
  v_mfma_f32_16x16x32_bf16 a[28:31], a[188:191], v[152:155], a[28:31]
  ds_read_b64_tr_b16 a[198:199], v10 offset:4096
  buffer_load_dwordx4 v12, s[20:23], s39 offen offset:1024 lds
  v_add_f32_e32 v157, v147, v157
  v_mfma_f32_16x16x32_bf16 v[128:131], a[128:131], v[32:35], 0
  ds_read_b64_tr_b16 a[200:201], v9 offset:512
  v_exp_f32_e32 v148, v148
  v_mfma_f32_16x16x32_bf16 v[128:131], a[132:135], v[36:39], v[128:131]
  ds_read_b64_tr_b16 a[202:203], v9 offset:4608
  v_cvt_pk_bf16_f32 v152, v144, v145
  v_cvt_pk_bf16_f32 v153, v146, v147
  v_mfma_f32_16x16x32_bf16 v[128:131], a[136:139], v[40:43], v[128:131]
  s_mov_b32 s43, 40960
  s_add_u32 m0, s37, 8192
  v_exp_f32_e32 v149, v149
  v_mfma_f32_16x16x32_bf16 v[128:131], a[140:143], v[44:47], v[128:131]
  ds_read_b64_tr_b16 a[204:205], v10 offset:512
  buffer_load_dwordx4 v11, s[20:23], s43 offen lds
  v_add_f32_e32 v157, v148, v157
  v_cvt_pk_bf16_f32 v154, v148, v149
  v_mfma_f32_16x16x32_bf16 v[132:135], a[144:147], v[32:35], 0
  ds_read_b64_tr_b16 a[206:207], v10 offset:4608
  v_exp_f32_e32 v150, v150
  v_mfma_f32_16x16x32_bf16 v[132:135], a[148:151], v[36:39], v[132:135]
  ds_read_b64_tr_b16 a[208:209], v9 offset:1024
  v_add_f32_e32 v157, v149, v157
  v_add_f32_e32 v157, v150, v157
  v_mfma_f32_16x16x32_bf16 v[132:135], a[152:155], v[40:43], v[132:135]
  ds_read_b64_tr_b16 a[210:211], v9 offset:5120
  v_exp_f32_e32 v151, v151
  v_mfma_f32_16x16x32_bf16 v[132:135], a[156:159], v[44:47], v[132:135]
  buffer_load_dwordx4 v12, s[20:23], s43 offen offset:1024 lds
  v_cvt_pk_bf16_f32 v155, v150, v151
  v_add_f32_e32 v157, v151, v157
  s_nop 0
  v_mfma_f32_16x16x32_bf16 a[32:35], a[160:163], v[152:155], a[32:35]
  ds_read_b64_tr_b16 a[212:213], v10 offset:1024
  v_exp_f32_e32 v128, v128
  v_mfma_f32_16x16x32_bf16 a[36:39], a[164:167], v[152:155], a[36:39]
  ds_read_b64_tr_b16 a[214:215], v10 offset:5120
  v_add_f32_e32 v156, v128, v156
  v_mfma_f32_16x16x32_bf16 a[40:43], a[168:171], v[152:155], a[40:43]
  s_mov_b32 s39, 81920
  s_add_u32 m0, s37, 16384
  ds_read_b64_tr_b16 a[216:217], v9 offset:1536
  v_exp_f32_e32 v129, v129
  v_mfma_f32_16x16x32_bf16 a[44:47], a[172:175], v[152:155], a[44:47]
  buffer_load_dwordx4 v11, s[20:23], s39 offen lds
  v_add_f32_e32 v156, v129, v156
  v_mfma_f32_16x16x32_bf16 a[48:51], a[176:179], v[152:155], a[48:51]
  ds_read_b64_tr_b16 a[218:219], v9 offset:5632
  v_exp_f32_e32 v130, v130
  v_mfma_f32_16x16x32_bf16 a[52:55], a[180:183], v[152:155], a[52:55]
  ds_read_b64_tr_b16 a[220:221], v10 offset:1536
  v_add_f32_e32 v156, v130, v156
  v_mfma_f32_16x16x32_bf16 a[56:59], a[184:187], v[152:155], a[56:59]
  ds_read_b64_tr_b16 a[222:223], v10 offset:5632
  v_exp_f32_e32 v131, v131
  v_mfma_f32_16x16x32_bf16 a[60:63], a[188:191], v[152:155], a[60:63]
  buffer_load_dwordx4 v12, s[20:23], s39 offen offset:1024 lds
  v_add_f32_e32 v156, v131, v156
  v_mfma_f32_16x16x32_bf16 v[136:139], a[128:131], v[48:51], 0
  v_exp_f32_e32 v132, v132
  v_mfma_f32_16x16x32_bf16 v[136:139], a[132:135], v[52:55], v[136:139]
  v_cvt_pk_bf16_f32 v152, v128, v129
  v_cvt_pk_bf16_f32 v153, v130, v131
  v_mfma_f32_16x16x32_bf16 v[136:139], a[136:139], v[56:59], v[136:139]
  s_mov_b32 s43, 122880
  s_add_u32 m0, s37, 24576
  v_exp_f32_e32 v133, v133
  v_mfma_f32_16x16x32_bf16 v[136:139], a[140:143], v[60:63], v[136:139]
  buffer_load_dwordx4 v11, s[20:23], s43 offen lds
  v_add_f32_e32 v156, v132, v156
  v_cvt_pk_bf16_f32 v154, v132, v133
  v_mfma_f32_16x16x32_bf16 v[140:143], a[144:147], v[48:51], 0
  v_exp_f32_e32 v134, v134
  v_mfma_f32_16x16x32_bf16 v[140:143], a[148:151], v[52:55], v[140:143]
  v_add_f32_e32 v156, v133, v156
  v_add_f32_e32 v156, v134, v156
  v_mfma_f32_16x16x32_bf16 v[140:143], a[152:155], v[56:59], v[140:143]
  v_exp_f32_e32 v135, v135
  v_mfma_f32_16x16x32_bf16 v[140:143], a[156:159], v[60:63], v[140:143]
  s_waitcnt lgkmcnt(0)
  buffer_load_dwordx4 v12, s[20:23], s43 offen offset:1024 lds
  v_cvt_pk_bf16_f32 v155, v134, v135
  v_add_f32_e32 v156, v135, v156
  s_nop 0
  v_mfma_f32_16x16x32_bf16 a[0:3], a[192:195], v[152:155], a[0:3]
  v_exp_f32_e32 v136, v136
  v_lshrrev_b32_e32 v16, 2, v15
  v_lshlrev_b32_e32 v19, 8, v16
  v_mfma_f32_16x16x32_bf16 a[4:7], a[196:199], v[152:155], a[4:7]
  v_add_f32_e32 v157, v136, v157
  v_and_b32_e32 v17, 1, v15
  v_lshl_add_u32 v19, v17, 3, v19
  v_mfma_f32_16x16x32_bf16 a[8:11], a[200:203], v[152:155], a[8:11]
  s_mov_b32 s39, 163840
  s_add_u32 m0, s37, 32768
  v_exp_f32_e32 v137, v137
  v_and_b32_e32 v16, 7, v16
  v_mfma_f32_16x16x32_bf16 a[12:15], a[204:207], v[152:155], a[12:15]
  buffer_load_dwordx4 v11, s[20:23], s39 offen lds
  v_add_f32_e32 v157, v137, v157
  v_lshlrev_b32_e32 v16, 1, v16
  v_bfe_u32 v17, v15, 1, 1
  v_mfma_f32_16x16x32_bf16 a[16:19], a[208:211], v[152:155], a[16:19]
  v_exp_f32_e32 v138, v138
  v_xor_b32_e32 v16, v16, v17
  v_mfma_f32_16x16x32_bf16 a[20:23], a[212:215], v[152:155], a[20:23]
  v_add_f32_e32 v157, v138, v157
  s_lshl_b32 s74, s44, 2
  s_add_u32 s75, s74, 0
  v_mfma_f32_16x16x32_bf16 a[24:27], a[216:219], v[152:155], a[24:27]
  v_exp_f32_e32 v139, v139
  s_and_b32 s75, s75, 15
  v_mfma_f32_16x16x32_bf16 a[28:31], a[220:223], v[152:155], a[28:31]
  buffer_load_dwordx4 v12, s[20:23], s39 offen offset:1024 lds
  v_add_f32_e32 v157, v139, v157
  v_xor_b32_e32 v3, s75, v16
  v_lshl_add_u32 v3, v3, 4, v19
  v_mfma_f32_16x16x32_bf16 v[144:147], a[128:131], v[64:67], 0
  v_exp_f32_e32 v140, v140
  s_add_u32 s75, s74, 2
  v_mfma_f32_16x16x32_bf16 v[144:147], a[132:135], v[68:71], v[144:147]
  v_cvt_pk_bf16_f32 v152, v136, v137
  v_cvt_pk_bf16_f32 v153, v138, v139
  s_and_b32 s75, s75, 15
  v_xor_b32_e32 v4, s75, v16
  v_mfma_f32_16x16x32_bf16 v[144:147], a[136:139], v[72:75], v[144:147]
  s_mov_b32 s43, 204800
  s_add_u32 m0, s37, 40960
  v_exp_f32_e32 v141, v141
  v_lshl_add_u32 v4, v4, 4, v19
  v_mfma_f32_16x16x32_bf16 v[144:147], a[140:143], v[76:79], v[144:147]
  buffer_load_dwordx4 v11, s[20:23], s43 offen lds
  v_add_f32_e32 v157, v140, v157
  v_cvt_pk_bf16_f32 v154, v140, v141
  s_add_u32 s75, s74, 4
  s_and_b32 s75, s75, 15
  v_mfma_f32_16x16x32_bf16 v[148:151], a[144:147], v[64:67], 0
  v_exp_f32_e32 v142, v142
  v_xor_b32_e32 v5, s75, v16
  v_mfma_f32_16x16x32_bf16 v[148:151], a[148:151], v[68:71], v[148:151]
  v_add_f32_e32 v157, v141, v157
  v_add_f32_e32 v157, v142, v157
  v_lshl_add_u32 v5, v5, 4, v19
  s_add_u32 s75, s74, 6
  v_mfma_f32_16x16x32_bf16 v[148:151], a[152:155], v[72:75], v[148:151]
  v_exp_f32_e32 v143, v143
  s_and_b32 s75, s75, 15
  v_mfma_f32_16x16x32_bf16 v[148:151], a[156:159], v[76:79], v[148:151]
  buffer_load_dwordx4 v12, s[20:23], s43 offen offset:1024 lds
  v_cvt_pk_bf16_f32 v155, v142, v143
  v_add_f32_e32 v157, v143, v157
  s_nop 0
  v_mfma_f32_16x16x32_bf16 a[32:35], a[192:195], v[152:155], a[32:35]
  s_mov_b32 s41, 0
  v_exp_f32_e32 v144, v144
  v_xor_b32_e32 v6, s75, v16
  v_lshl_add_u32 v6, v6, 4, v19
  v_mfma_f32_16x16x32_bf16 a[36:39], a[196:199], v[152:155], a[36:39]
  buffer_load_dwordx4 v[32:35], v0, s[16:19], s41 offen sc1
  v_exp_f32_e32 v145, v145
  s_add_u32 s75, s74, 8
  v_mfma_f32_16x16x32_bf16 a[40:43], a[200:203], v[152:155], a[40:43]
  v_exp_f32_e32 v146, v146
  s_and_b32 s75, s75, 15
  v_xor_b32_e32 v7, s75, v16
  v_mfma_f32_16x16x32_bf16 a[44:47], a[204:207], v[152:155], a[44:47]
  buffer_load_dwordx4 v[36:39], v13, s[16:19], s41 offen sc1
  v_exp_f32_e32 v147, v147
  v_lshl_add_u32 v7, v7, 4, v19
  v_mfma_f32_16x16x32_bf16 a[48:51], a[208:211], v[152:155], a[48:51]
  v_exp_f32_e32 v148, v148
  v_mfma_f32_16x16x32_bf16 a[52:55], a[212:215], v[152:155], a[52:55]
  v_exp_f32_e32 v149, v149
  v_mfma_f32_16x16x32_bf16 a[56:59], a[216:219], v[152:155], a[56:59]
  v_exp_f32_e32 v150, v150
  v_mfma_f32_16x16x32_bf16 a[60:63], a[220:223], v[152:155], a[60:63]
  v_exp_f32_e32 v151, v151
  v_add_f32_e32 v158, v144, v158
  v_cvt_pk_bf16_f32 v152, v144, v145
  v_cvt_pk_bf16_f32 v153, v146, v147
  v_cvt_pk_bf16_f32 v154, v148, v149
  v_cvt_pk_bf16_f32 v155, v150, v151
  v_add_f32_e32 v158, v145, v158
  v_add_f32_e32 v158, v146, v158
  v_mfma_f32_16x16x32_bf16 a[64:67], a[192:195], v[152:155], a[64:67]
  v_add_f32_e32 v158, v147, v158
  s_add_u32 s75, s74, 10
  s_and_b32 s75, s75, 15
  v_xor_b32_e32 v8, s75, v16
  v_mfma_f32_16x16x32_bf16 a[68:71], a[196:199], v[152:155], a[68:71]
  buffer_load_dwordx4 v[40:43], v14, s[16:19], s41 offen sc1
  v_add_f32_e32 v158, v148, v158
  v_lshl_add_u32 v8, v8, 4, v19
  s_add_u32 s75, s74, 12
  s_and_b32 s75, s75, 15
  v_mfma_f32_16x16x32_bf16 a[72:75], a[200:203], v[152:155], a[72:75]
  s_mov_b32 s42, 1024
  v_add_f32_e32 v158, v149, v158
  v_xor_b32_e32 v9, s75, v16
  v_lshl_add_u32 v9, v9, 4, v19
  v_mfma_f32_16x16x32_bf16 a[76:79], a[204:207], v[152:155], a[76:79]
  buffer_load_dwordx4 v[44:47], v0, s[16:19], s42 offen sc1
  v_add_f32_e32 v158, v150, v158
  s_add_u32 s75, s74, 14
  s_and_b32 s75, s75, 15
  v_xor_b32_e32 v10, s75, v16
  v_mfma_f32_16x16x32_bf16 a[80:83], a[208:211], v[152:155], a[80:83]
  v_add_f32_e32 v158, v151, v158
  v_lshl_add_u32 v10, v10, 4, v19
  v_and_b32_e32 v16, 15, v15
  v_mfma_f32_16x16x32_bf16 a[84:87], a[212:215], v[152:155], a[84:87]
  buffer_load_dwordx4 v[48:51], v13, s[16:19], s42 offen sc1
  v_mul_u32_u24_e32 v16, 0xa00, v16
  v_bfe_u32 v17, v15, 4, 1
  v_lshl_add_u32 v16, v17, 5, v16
  v_mfma_f32_16x16x32_bf16 a[88:91], a[216:219], v[152:155], a[88:91]
  v_lshrrev_b32_e32 v17, 5, v15
  v_lshl_add_u32 v1, v17, 4, v16
  v_mfma_f32_16x16x32_bf16 a[92:95], a[220:223], v[152:155], a[92:95]
  buffer_load_dwordx4 v[52:55], v14, s[16:19], s42 offen sc1
  s_nop 7
  s_mov_b32 s41, 2048
  buffer_load_dwordx4 v[56:59], v0, s[16:19], s41 offen sc1
  s_lshl_b32 s50, s36, 12
  s_add_u32 s50, s50, 131072
  s_lshl_b32 s51, s36, 10
  s_add_u32 s51, s51, 131072
  v_add_u32_e32 v240, s50, v0
  v_add_u32_e32 v241, s51, v0
  v_and_b32_e32 v242, 0xf0, v0
  s_lshl_b32 s50, s36, 2
  buffer_load_dwordx4 v[60:63], v13, s[16:19], s41 offen sc1
  v_add_u32_e32 v242, 147456, v242
  v_add_u32_e32 v243, s50, v242
  v_mov_b32_e32 v160, v156
  v_mov_b32_e32 v161, v157
  v_mov_b32_e32 v162, v158
  s_nop 0
  v_permlane16_swap_b32_e32 v160, v156
  v_permlane16_swap_b32_e32 v161, v157
  buffer_load_dwordx4 v[64:67], v14, s[16:19], s41 offen sc1
  v_permlane16_swap_b32_e32 v162, v158
  v_add_f32_e32 v156, v160, v156
  v_add_f32_e32 v157, v161, v157
  v_add_f32_e32 v158, v162, v158
  v_mov_b32_e32 v160, v156
  v_mov_b32_e32 v161, v157
  v_mov_b32_e32 v162, v158
  s_nop 0
  s_mov_b32 s42, 3072
  buffer_load_dwordx4 v[68:71], v0, s[16:19], s42 offen sc1
  v_permlane32_swap_b32_e32 v160, v156
  v_permlane32_swap_b32_e32 v161, v157
  v_permlane32_swap_b32_e32 v162, v158
  v_add_f32_e32 v156, v160, v156
  v_add_f32_e32 v157, v161, v157
  v_add_f32_e32 v158, v162, v158
  ds_write_b32 v243, v158
  v_accvgpr_read_b32 v160, a64
  buffer_load_dwordx4 v[72:75], v13, s[16:19], s42 offen sc1
  v_accvgpr_read_b32 v161, a65
  v_accvgpr_read_b32 v162, a66
  v_accvgpr_read_b32 v163, a67
  v_accvgpr_read_b32 v164, a68
  v_accvgpr_read_b32 v165, a69
  v_accvgpr_read_b32 v166, a70
  v_accvgpr_read_b32 v167, a71
  v_cvt_pk_bf16_f32 v168, v160, v161
  buffer_load_dwordx4 v[76:79], v14, s[16:19], s42 offen sc1
  v_cvt_pk_bf16_f32 v169, v162, v163
  v_cvt_pk_bf16_f32 v170, v164, v165
  v_cvt_pk_bf16_f32 v171, v166, v167
  ds_write_b128 v240, v[168:171]
  v_accvgpr_read_b32 v172, a72
  v_accvgpr_read_b32 v173, a73
  v_accvgpr_read_b32 v174, a74
  v_accvgpr_read_b32 v175, a75
  s_mov_b32 s41, 4096
  buffer_load_dwordx4 v[80:83], v0, s[16:19], s41 offen sc1
  v_accvgpr_read_b32 v176, a76
  v_accvgpr_read_b32 v177, a77
  v_accvgpr_read_b32 v178, a78
  v_accvgpr_read_b32 v179, a79
  v_cvt_pk_bf16_f32 v180, v172, v173
  v_cvt_pk_bf16_f32 v181, v174, v175
  v_cvt_pk_bf16_f32 v182, v176, v177
  v_cvt_pk_bf16_f32 v183, v178, v179
  buffer_load_dwordx4 v[84:87], v13, s[16:19], s41 offen sc1
  ds_write_b128 v240, v[180:183] offset:1024
  v_accvgpr_read_b32 v160, a80
  v_accvgpr_read_b32 v161, a81
  v_accvgpr_read_b32 v162, a82
  v_accvgpr_read_b32 v163, a83
  v_accvgpr_read_b32 v164, a84
  v_accvgpr_read_b32 v165, a85
  v_accvgpr_read_b32 v166, a86
  buffer_load_dwordx4 v[88:91], v14, s[16:19], s41 offen sc1
  v_accvgpr_read_b32 v167, a87
  v_cvt_pk_bf16_f32 v168, v160, v161
  v_cvt_pk_bf16_f32 v169, v162, v163
  v_cvt_pk_bf16_f32 v170, v164, v165
  v_cvt_pk_bf16_f32 v171, v166, v167
  ds_write_b128 v240, v[168:171] offset:2048
  v_accvgpr_read_b32 v172, a88
  v_accvgpr_read_b32 v173, a89
  s_mov_b32 s42, 5120
  buffer_load_dwordx4 v[92:95], v0, s[16:19], s42 offen sc1
  v_accvgpr_read_b32 v174, a90
  v_accvgpr_read_b32 v175, a91
  v_accvgpr_read_b32 v176, a92
  v_accvgpr_read_b32 v177, a93
  v_accvgpr_read_b32 v178, a94
  v_accvgpr_read_b32 v179, a95
  v_cvt_pk_bf16_f32 v180, v172, v173
  v_cvt_pk_bf16_f32 v181, v174, v175
  buffer_load_dwordx4 v[96:99], v13, s[16:19], s42 offen sc1
  v_cvt_pk_bf16_f32 v182, v176, v177
  v_cvt_pk_bf16_f32 v183, v178, v179
  ds_write_b128 v240, v[180:183] offset:3072
  v_min_f32_e32 v244, v156, v157
  v_max_f32_e32 v245, v156, v157
  v_rcp_f32_e32 v156, v156
  v_rcp_f32_e32 v157, v157
  v_mov_b32_e32 v28, 0
  buffer_load_dwordx4 v[100:103], v14, s[16:19], s42 offen sc1
  v_mov_b32_e32 v29, 0
  v_mov_b32_e32 v30, 0
  v_mov_b32_e32 v31, 0
  s_lshl_b32 s50, s36, 18
  s_add_u32 s50, s50, s48
  s_add_u32 s52, s50, 131072
  s_lshl_b32 s51, s36, 10
  s_add_u32 s51, s51, s48
  s_add_u32 s51, s51, 1048576
  s_waitcnt lgkmcnt(0)
  s_barrier
  ds_read_b128 v[208:211], v241
  ds_read_b128 v[212:215], v241 offset:4096
  ds_read_b128 v[216:219], v241 offset:8192
  ds_read_b128 v[220:223], v241 offset:12288
  ds_read_b128 v[224:227], v242
  v_accvgpr_read_b32 v160, a0
  v_accvgpr_read_b32 v161, a1
  v_accvgpr_read_b32 v162, a2
  v_accvgpr_read_b32 v163, a3
  v_accvgpr_read_b32 v164, a4
  v_accvgpr_read_b32 v165, a5
  v_accvgpr_read_b32 v166, a6
  v_accvgpr_read_b32 v167, a7
  v_pk_mul_f32 v[160:161], v[160:161], v[156:157] op_sel:[0,0] op_sel_hi:[1,0]
  v_pk_mul_f32 v[162:163], v[162:163], v[156:157] op_sel:[0,0] op_sel_hi:[1,0]
  v_pk_mul_f32 v[164:165], v[164:165], v[156:157] op_sel:[0,0] op_sel_hi:[1,0]
  v_pk_mul_f32 v[166:167], v[166:167], v[156:157] op_sel:[0,0] op_sel_hi:[1,0]
  v_cvt_pk_bf16_f32 v168, v160, v161
  v_cvt_pk_bf16_f32 v169, v162, v163
  v_cvt_pk_bf16_f32 v170, v164, v165
  v_cvt_pk_bf16_f32 v171, v166, v167
  buffer_store_dwordx4 v[168:171], v0, s[64:67], s50 offen sc1
  v_pk_fma_f32 v[28:29], v[160:161], v[30:31], v[28:29]
  v_pk_fma_f32 v[28:29], v[162:163], v[30:31], v[28:29]
  v_pk_fma_f32 v[28:29], v[164:165], v[30:31], v[28:29]
  v_pk_fma_f32 v[28:29], v[166:167], v[30:31], v[28:29]
  s_waitcnt lgkmcnt(0)
  v_mfma_f32_16x16x32_bf16 v[228:231], v[22:25], v[208:211], 0
  v_mfma_f32_16x16x32_bf16 v[232:235], v[20:23], v[208:211], 0
  v_mfma_f32_16x16x32_bf16 v[228:231], v[22:25], v[212:215], v[228:231]
  v_mfma_f32_16x16x32_bf16 v[232:235], v[20:23], v[212:215], v[232:235]
  v_mfma_f32_16x16x32_bf16 v[228:231], v[22:25], v[216:219], v[228:231]
  v_mfma_f32_16x16x32_bf16 v[232:235], v[20:23], v[216:219], v[232:235]
  v_mfma_f32_16x16x32_bf16 v[228:231], v[22:25], v[220:223], v[228:231]
  v_mfma_f32_16x16x32_bf16 v[232:235], v[20:23], v[220:223], v[232:235]
  v_pk_add_f32 v[224:225], v[224:225], v[226:227]
  s_nop 0
  v_add_f32_e32 v158, v224, v225
  v_min_f32_e32 v244, v244, v158
  v_max_f32_e32 v245, v245, v158
  v_rcp_f32_e32 v158, v158
  v_cmp_nle_f32_e32 vcc, 0x3a800000, v244
  s_nop 0
  s_mov_b64 s[56:57], vcc
  v_cmp_nge_f32_e32 vcc, 0x5f800000, v245
  s_nop 0
  s_or_b64 s[56:57], s[56:57], vcc
  v_accvgpr_read_b32 v172, a8
  v_accvgpr_read_b32 v173, a9
  v_accvgpr_read_b32 v174, a10
  v_accvgpr_read_b32 v175, a11
  v_accvgpr_read_b32 v176, a12
  v_accvgpr_read_b32 v177, a13
  v_accvgpr_read_b32 v178, a14
  v_accvgpr_read_b32 v179, a15
  v_pk_mul_f32 v[172:173], v[172:173], v[156:157] op_sel:[0,0] op_sel_hi:[1,0]
  v_pk_mul_f32 v[174:175], v[174:175], v[156:157] op_sel:[0,0] op_sel_hi:[1,0]
  v_pk_mul_f32 v[176:177], v[176:177], v[156:157] op_sel:[0,0] op_sel_hi:[1,0]
  v_pk_mul_f32 v[178:179], v[178:179], v[156:157] op_sel:[0,0] op_sel_hi:[1,0]
  v_cvt_pk_bf16_f32 v180, v172, v173
  v_cvt_pk_bf16_f32 v181, v174, v175
  v_cvt_pk_bf16_f32 v182, v176, v177
  v_cvt_pk_bf16_f32 v183, v178, v179
  buffer_store_dwordx4 v[180:183], v0, s[64:67], s50 offen offset:1024 sc1
  v_pk_fma_f32 v[28:29], v[172:173], v[30:31], v[28:29]
  v_pk_fma_f32 v[28:29], v[174:175], v[30:31], v[28:29]
  v_pk_fma_f32 v[28:29], v[176:177], v[30:31], v[28:29]
  v_pk_fma_f32 v[28:29], v[178:179], v[30:31], v[28:29]
  v_pk_mul_f32 v[196:197], v[228:229], v[158:159] op_sel:[0,0] op_sel_hi:[1,0]
  v_pk_mul_f32 v[198:199], v[230:231], v[158:159] op_sel:[0,0] op_sel_hi:[1,0]
  v_pk_mul_f32 v[200:201], v[232:233], v[158:159] op_sel:[0,0] op_sel_hi:[1,0]
  v_pk_mul_f32 v[202:203], v[234:235], v[158:159] op_sel:[0,0] op_sel_hi:[1,0]
  v_cvt_pk_bf16_f32 v204, v196, v197
  v_cvt_pk_bf16_f32 v205, v198, v199
  v_cvt_pk_bf16_f32 v206, v200, v201
  v_cvt_pk_bf16_f32 v207, v202, v203
  buffer_store_dwordx4 v[204:207], v0, s[64:67], s51 offen sc1
  v_pk_fma_f32 v[28:29], v[196:197], v[30:31], v[28:29]
  v_pk_fma_f32 v[28:29], v[198:199], v[30:31], v[28:29]
  v_pk_fma_f32 v[28:29], v[200:201], v[30:31], v[28:29]
  v_pk_fma_f32 v[28:29], v[202:203], v[30:31], v[28:29]
  v_accvgpr_read_b32 v184, a16
  v_accvgpr_read_b32 v185, a17
  v_accvgpr_read_b32 v186, a18
  v_accvgpr_read_b32 v187, a19
  v_accvgpr_read_b32 v188, a20
  v_accvgpr_read_b32 v189, a21
  v_accvgpr_read_b32 v190, a22
  v_accvgpr_read_b32 v191, a23
  v_pk_mul_f32 v[184:185], v[184:185], v[156:157] op_sel:[0,0] op_sel_hi:[1,0]
  v_pk_mul_f32 v[186:187], v[186:187], v[156:157] op_sel:[0,0] op_sel_hi:[1,0]
  v_pk_mul_f32 v[188:189], v[188:189], v[156:157] op_sel:[0,0] op_sel_hi:[1,0]
  v_pk_mul_f32 v[190:191], v[190:191], v[156:157] op_sel:[0,0] op_sel_hi:[1,0]
  v_cvt_pk_bf16_f32 v192, v184, v185
  v_cvt_pk_bf16_f32 v193, v186, v187
  v_cvt_pk_bf16_f32 v194, v188, v189
  v_cvt_pk_bf16_f32 v195, v190, v191
  buffer_store_dwordx4 v[192:195], v0, s[64:67], s50 offen offset:2048 sc1
  v_pk_fma_f32 v[28:29], v[184:185], v[30:31], v[28:29]
  v_pk_fma_f32 v[28:29], v[186:187], v[30:31], v[28:29]
  v_pk_fma_f32 v[28:29], v[188:189], v[30:31], v[28:29]
  v_pk_fma_f32 v[28:29], v[190:191], v[30:31], v[28:29]
  v_accvgpr_read_b32 v160, a24
  v_accvgpr_read_b32 v161, a25
  v_accvgpr_read_b32 v162, a26
  v_accvgpr_read_b32 v163, a27
  v_accvgpr_read_b32 v164, a28
  v_accvgpr_read_b32 v165, a29
  v_accvgpr_read_b32 v166, a30
  v_accvgpr_read_b32 v167, a31
  v_pk_mul_f32 v[160:161], v[160:161], v[156:157] op_sel:[0,0] op_sel_hi:[1,0]
  v_pk_mul_f32 v[162:163], v[162:163], v[156:157] op_sel:[0,0] op_sel_hi:[1,0]
  v_pk_mul_f32 v[164:165], v[164:165], v[156:157] op_sel:[0,0] op_sel_hi:[1,0]
  v_pk_mul_f32 v[166:167], v[166:167], v[156:157] op_sel:[0,0] op_sel_hi:[1,0]
  v_cvt_pk_bf16_f32 v168, v160, v161
  v_cvt_pk_bf16_f32 v169, v162, v163
  v_cvt_pk_bf16_f32 v170, v164, v165
  v_cvt_pk_bf16_f32 v171, v166, v167
  buffer_store_dwordx4 v[168:171], v0, s[64:67], s50 offen offset:3072 sc1
  v_pk_fma_f32 v[28:29], v[160:161], v[30:31], v[28:29]
  v_pk_fma_f32 v[28:29], v[162:163], v[30:31], v[28:29]
  v_pk_fma_f32 v[28:29], v[164:165], v[30:31], v[28:29]
  v_pk_fma_f32 v[28:29], v[166:167], v[30:31], v[28:29]
  v_accvgpr_read_b32 v172, a32
  v_accvgpr_read_b32 v173, a33
  v_accvgpr_read_b32 v174, a34
  v_accvgpr_read_b32 v175, a35
  v_accvgpr_read_b32 v176, a36
  v_accvgpr_read_b32 v177, a37
  v_accvgpr_read_b32 v178, a38
  v_accvgpr_read_b32 v179, a39
  v_pk_mul_f32 v[172:173], v[172:173], v[156:157] op_sel:[0,1] op_sel_hi:[1,1]
  v_pk_mul_f32 v[174:175], v[174:175], v[156:157] op_sel:[0,1] op_sel_hi:[1,1]
  v_pk_mul_f32 v[176:177], v[176:177], v[156:157] op_sel:[0,1] op_sel_hi:[1,1]
  v_pk_mul_f32 v[178:179], v[178:179], v[156:157] op_sel:[0,1] op_sel_hi:[1,1]
  v_cvt_pk_bf16_f32 v180, v172, v173
  v_cvt_pk_bf16_f32 v181, v174, v175
  v_cvt_pk_bf16_f32 v182, v176, v177
  v_cvt_pk_bf16_f32 v183, v178, v179
  buffer_store_dwordx4 v[180:183], v0, s[64:67], s52 offen sc1
  v_pk_fma_f32 v[28:29], v[172:173], v[30:31], v[28:29]
  v_pk_fma_f32 v[28:29], v[174:175], v[30:31], v[28:29]
  v_pk_fma_f32 v[28:29], v[176:177], v[30:31], v[28:29]
  v_pk_fma_f32 v[28:29], v[178:179], v[30:31], v[28:29]
  v_accvgpr_read_b32 v184, a40
  v_accvgpr_read_b32 v185, a41
  v_accvgpr_read_b32 v186, a42
  v_accvgpr_read_b32 v187, a43
  v_accvgpr_read_b32 v188, a44
  v_accvgpr_read_b32 v189, a45
  v_accvgpr_read_b32 v190, a46
  v_accvgpr_read_b32 v191, a47
  v_pk_mul_f32 v[184:185], v[184:185], v[156:157] op_sel:[0,1] op_sel_hi:[1,1]
  v_pk_mul_f32 v[186:187], v[186:187], v[156:157] op_sel:[0,1] op_sel_hi:[1,1]
  v_pk_mul_f32 v[188:189], v[188:189], v[156:157] op_sel:[0,1] op_sel_hi:[1,1]
  v_pk_mul_f32 v[190:191], v[190:191], v[156:157] op_sel:[0,1] op_sel_hi:[1,1]
  v_cvt_pk_bf16_f32 v192, v184, v185
  v_cvt_pk_bf16_f32 v193, v186, v187
  v_cvt_pk_bf16_f32 v194, v188, v189
  v_cvt_pk_bf16_f32 v195, v190, v191
  buffer_store_dwordx4 v[192:195], v0, s[64:67], s52 offen offset:1024 sc1
  v_pk_fma_f32 v[28:29], v[184:185], v[30:31], v[28:29]
  v_pk_fma_f32 v[28:29], v[186:187], v[30:31], v[28:29]
  v_pk_fma_f32 v[28:29], v[188:189], v[30:31], v[28:29]
  v_pk_fma_f32 v[28:29], v[190:191], v[30:31], v[28:29]
  v_accvgpr_read_b32 v160, a48
  v_accvgpr_read_b32 v161, a49
  v_accvgpr_read_b32 v162, a50
  v_accvgpr_read_b32 v163, a51
  v_accvgpr_read_b32 v164, a52
  v_accvgpr_read_b32 v165, a53
  v_accvgpr_read_b32 v166, a54
  v_accvgpr_read_b32 v167, a55
  v_pk_mul_f32 v[160:161], v[160:161], v[156:157] op_sel:[0,1] op_sel_hi:[1,1]
  v_pk_mul_f32 v[162:163], v[162:163], v[156:157] op_sel:[0,1] op_sel_hi:[1,1]
  v_pk_mul_f32 v[164:165], v[164:165], v[156:157] op_sel:[0,1] op_sel_hi:[1,1]
  v_pk_mul_f32 v[166:167], v[166:167], v[156:157] op_sel:[0,1] op_sel_hi:[1,1]
  v_cvt_pk_bf16_f32 v168, v160, v161
  v_cvt_pk_bf16_f32 v169, v162, v163
  v_cvt_pk_bf16_f32 v170, v164, v165
  v_cvt_pk_bf16_f32 v171, v166, v167
  buffer_store_dwordx4 v[168:171], v0, s[64:67], s52 offen offset:2048 sc1
  v_pk_fma_f32 v[28:29], v[160:161], v[30:31], v[28:29]
  v_pk_fma_f32 v[28:29], v[162:163], v[30:31], v[28:29]
  v_pk_fma_f32 v[28:29], v[164:165], v[30:31], v[28:29]
  v_pk_fma_f32 v[28:29], v[166:167], v[30:31], v[28:29]
  v_accvgpr_read_b32 v172, a56
  v_accvgpr_read_b32 v173, a57
  v_accvgpr_read_b32 v174, a58
  v_accvgpr_read_b32 v175, a59
  v_accvgpr_read_b32 v176, a60
  v_accvgpr_read_b32 v177, a61
  v_accvgpr_read_b32 v178, a62
  v_accvgpr_read_b32 v179, a63
  v_pk_mul_f32 v[172:173], v[172:173], v[156:157] op_sel:[0,1] op_sel_hi:[1,1]
  v_pk_mul_f32 v[174:175], v[174:175], v[156:157] op_sel:[0,1] op_sel_hi:[1,1]
  v_pk_mul_f32 v[176:177], v[176:177], v[156:157] op_sel:[0,1] op_sel_hi:[1,1]
  v_pk_mul_f32 v[178:179], v[178:179], v[156:157] op_sel:[0,1] op_sel_hi:[1,1]
  v_cvt_pk_bf16_f32 v180, v172, v173
  v_cvt_pk_bf16_f32 v181, v174, v175
  v_cvt_pk_bf16_f32 v182, v176, v177
  v_cvt_pk_bf16_f32 v183, v178, v179
  buffer_store_dwordx4 v[180:183], v0, s[64:67], s52 offen offset:3072 sc1
  v_pk_fma_f32 v[28:29], v[172:173], v[30:31], v[28:29]
  v_pk_fma_f32 v[28:29], v[174:175], v[30:31], v[28:29]
  v_pk_fma_f32 v[28:29], v[176:177], v[30:31], v[28:29]
  v_pk_fma_f32 v[28:29], v[178:179], v[30:31], v[28:29]
  v_cmp_u_f32_e32 vcc, v28, v29
  s_nop 0
  s_or_b64 s[56:57], s[56:57], vcc
  s_cmp_lg_u64 s[56:57], 0
  s_cselect_b32 s50, 1, 0
  s_lshl_b32 s50, s50, s45
  s_or_b32 s46, s46, s50
  s_lshl_b32 s50, s36, 2
  s_add_u32 s50, s50, 151552
  v_mov_b32_e32 v240, s50
  v_mov_b32_e32 v241, s46
  ds_write_b32 v240, v241
  s_add_u32 s45, s45, 1
  s_add_u32 s48, s48, 16384
  v_add_u32_e32 v224, 0x10000, v3
  v_add_u32_e32 v225, 0x10000, v4
  v_add_u32_e32 v226, 0x10000, v5
  v_add_u32_e32 v227, 0x10000, v6
  v_add_u32_e32 v228, 0x10000, v7
  v_add_u32_e32 v229, 0x10000, v8
  v_add_u32_e32 v230, 0x10000, v9
  v_add_u32_e32 v231, 0x10000, v10
  s_waitcnt vmcnt(9) lgkmcnt(0)
  s_barrier
  ds_read_b64_tr_b16 a[192:193], v3
  ds_read_b64_tr_b16 a[194:195], v3 offset:4096
  ds_read_b64_tr_b16 a[196:197], v4
  ds_read_b64_tr_b16 a[198:199], v4 offset:4096
  ds_read_b64_tr_b16 a[200:201], v5
  ds_read_b64_tr_b16 a[202:203], v5 offset:4096
  ds_read_b64_tr_b16 a[204:205], v6
  ds_read_b64_tr_b16 a[206:207], v6 offset:4096
  ds_read_b64_tr_b16 a[208:209], v7
  ds_read_b64_tr_b16 a[210:211], v7 offset:4096
  ds_read_b64_tr_b16 a[212:213], v8
  ds_read_b64_tr_b16 a[214:215], v8 offset:4096
  ds_read_b64_tr_b16 a[216:217], v9
  ds_read_b64_tr_b16 a[218:219], v9 offset:4096
  ds_read_b64_tr_b16 a[220:221], v10
  ds_read_b64_tr_b16 a[222:223], v10 offset:4096
  s_waitcnt lgkmcnt(0)
  v_mfma_f32_16x16x32_bf16 v[128:131], a[192:195], v[32:35], 0
  ds_read_b64_tr_b16 a[224:225], v3 offset:8192
  v_mfma_f32_16x16x32_bf16 v[160:163], a[192:195], v[36:39], 0
  s_mov_b32 s39, 245760
  s_add_u32 m0, s37, 49152
  ds_read_b64_tr_b16 a[226:227], v3 offset:12288
  v_mfma_f32_16x16x32_bf16 v[192:195], a[192:195], v[40:43], 0
  buffer_load_dwordx4 v11, s[20:23], s39 offen lds
  v_mfma_f32_16x16x32_bf16 v[132:135], a[196:199], v[32:35], 0
  ds_read_b64_tr_b16 a[228:229], v4 offset:8192
  v_mfma_f32_16x16x32_bf16 v[164:167], a[196:199], v[36:39], 0
  ds_read_b64_tr_b16 a[230:231], v4 offset:12288
  v_mfma_f32_16x16x32_bf16 v[196:199], a[196:199], v[40:43], 0
  ds_read_b64_tr_b16 a[232:233], v5 offset:8192
  v_mfma_f32_16x16x32_bf16 v[136:139], a[200:203], v[32:35], 0
  buffer_load_dwordx4 v12, s[20:23], s39 offen offset:1024 lds
  v_mfma_f32_16x16x32_bf16 v[168:171], a[200:203], v[36:39], 0
  ds_read_b64_tr_b16 a[234:235], v5 offset:12288
  v_mfma_f32_16x16x32_bf16 v[200:203], a[200:203], v[40:43], 0
  ds_read_b64_tr_b16 a[236:237], v6 offset:8192
  v_mfma_f32_16x16x32_bf16 v[140:143], a[204:207], v[32:35], 0
  s_mov_b32 s43, 286720
  s_add_u32 m0, s37, 57344
  ds_read_b64_tr_b16 a[238:239], v6 offset:12288
  v_mfma_f32_16x16x32_bf16 v[172:175], a[204:207], v[36:39], 0
  buffer_load_dwordx4 v11, s[20:23], s43 offen lds
  v_mfma_f32_16x16x32_bf16 v[204:207], a[204:207], v[40:43], 0
  ds_read_b64_tr_b16 a[240:241], v7 offset:8192
  v_mfma_f32_16x16x32_bf16 v[144:147], a[208:211], v[32:35], 0
  ds_read_b64_tr_b16 a[242:243], v7 offset:12288
  v_mfma_f32_16x16x32_bf16 v[176:179], a[208:211], v[36:39], 0
  ds_read_b64_tr_b16 a[244:245], v8 offset:8192
  v_mfma_f32_16x16x32_bf16 v[208:211], a[208:211], v[40:43], 0
  buffer_load_dwordx4 v12, s[20:23], s43 offen offset:1024 lds
  v_mfma_f32_16x16x32_bf16 v[148:151], a[212:215], v[32:35], 0
  ds_read_b64_tr_b16 a[246:247], v8 offset:12288
  v_mfma_f32_16x16x32_bf16 v[180:183], a[212:215], v[36:39], 0
  ds_read_b64_tr_b16 a[248:249], v9 offset:8192
  v_mfma_f32_16x16x32_bf16 v[212:215], a[212:215], v[40:43], 0
  ds_read_b64_tr_b16 a[250:251], v9 offset:12288
  v_mfma_f32_16x16x32_bf16 v[152:155], a[216:219], v[32:35], 0
  s_mov_b32 s41, 6144
  ds_read_b64_tr_b16 a[252:253], v10 offset:8192
  v_mfma_f32_16x16x32_bf16 v[184:187], a[216:219], v[36:39], 0
  buffer_load_dwordx4 v[104:107], v0, s[16:19], s41 offen sc1
  v_mfma_f32_16x16x32_bf16 v[216:219], a[216:219], v[40:43], 0
  ds_read_b64_tr_b16 a[254:255], v10 offset:12288
  v_mfma_f32_16x16x32_bf16 v[156:159], a[220:223], v[32:35], 0
  v_mfma_f32_16x16x32_bf16 v[188:191], a[220:223], v[36:39], 0
  v_mfma_f32_16x16x32_bf16 v[220:223], a[220:223], v[40:43], 0
  s_waitcnt lgkmcnt(2)
  buffer_load_dwordx4 v[108:111], v13, s[16:19], s41 offen sc1
  v_mfma_f32_16x16x32_bf16 v[128:131], a[224:227], v[44:47], v[128:131]
  ds_read_b64_tr_b16 a[192:193], v3 offset:16384
  v_mfma_f32_16x16x32_bf16 v[160:163], a[224:227], v[48:51], v[160:163]
  ds_read_b64_tr_b16 a[194:195], v3 offset:20480
  v_mfma_f32_16x16x32_bf16 v[192:195], a[224:227], v[52:55], v[192:195]
  ds_read_b64_tr_b16 a[196:197], v4 offset:16384
  v_mfma_f32_16x16x32_bf16 v[132:135], a[228:231], v[44:47], v[132:135]
  buffer_load_dwordx4 v[112:115], v14, s[16:19], s41 offen sc1
  v_mfma_f32_16x16x32_bf16 v[164:167], a[228:231], v[48:51], v[164:167]
  ds_read_b64_tr_b16 a[198:199], v4 offset:20480
  v_mfma_f32_16x16x32_bf16 v[196:199], a[228:231], v[52:55], v[196:199]
  ds_read_b64_tr_b16 a[200:201], v5 offset:16384
  v_mfma_f32_16x16x32_bf16 v[136:139], a[232:235], v[44:47], v[136:139]
  ds_read_b64_tr_b16 a[202:203], v5 offset:20480
  v_mfma_f32_16x16x32_bf16 v[168:171], a[232:235], v[48:51], v[168:171]
  s_mov_b32 s42, 7168
  ds_read_b64_tr_b16 a[204:205], v6 offset:16384
  v_mfma_f32_16x16x32_bf16 v[200:203], a[232:235], v[52:55], v[200:203]
  buffer_load_dwordx4 v[116:119], v0, s[16:19], s42 offen sc1
  v_mfma_f32_16x16x32_bf16 v[140:143], a[236:239], v[44:47], v[140:143]
  ds_read_b64_tr_b16 a[206:207], v6 offset:20480
  v_mfma_f32_16x16x32_bf16 v[172:175], a[236:239], v[48:51], v[172:175]
  ds_read_b64_tr_b16 a[208:209], v7 offset:16384
  v_mfma_f32_16x16x32_bf16 v[204:207], a[236:239], v[52:55], v[204:207]
  ds_read_b64_tr_b16 a[210:211], v7 offset:20480
  v_mfma_f32_16x16x32_bf16 v[144:147], a[240:243], v[44:47], v[144:147]
  buffer_load_dwordx4 v[120:123], v13, s[16:19], s42 offen sc1
  v_mfma_f32_16x16x32_bf16 v[176:179], a[240:243], v[48:51], v[176:179]
  ds_read_b64_tr_b16 a[212:213], v8 offset:16384
  v_mfma_f32_16x16x32_bf16 v[208:211], a[240:243], v[52:55], v[208:211]
  ds_read_b64_tr_b16 a[214:215], v8 offset:20480
  v_mfma_f32_16x16x32_bf16 v[148:151], a[244:247], v[44:47], v[148:151]
  ds_read_b64_tr_b16 a[216:217], v9 offset:16384
  v_mfma_f32_16x16x32_bf16 v[180:183], a[244:247], v[48:51], v[180:183]
  buffer_load_dwordx4 v[124:127], v14, s[16:19], s42 offen sc1
  v_mfma_f32_16x16x32_bf16 v[212:215], a[244:247], v[52:55], v[212:215]
  ds_read_b64_tr_b16 a[218:219], v9 offset:20480
  v_mfma_f32_16x16x32_bf16 v[152:155], a[248:251], v[44:47], v[152:155]
  ds_read_b64_tr_b16 a[220:221], v10 offset:16384
  v_mfma_f32_16x16x32_bf16 v[184:187], a[248:251], v[48:51], v[184:187]
  ds_read_b64_tr_b16 a[222:223], v10 offset:20480
  v_mfma_f32_16x16x32_bf16 v[216:219], a[248:251], v[52:55], v[216:219]
  s_waitcnt lgkmcnt(4)
  v_mfma_f32_16x16x32_bf16 v[156:159], a[252:255], v[44:47], v[156:159]
  v_mfma_f32_16x16x32_bf16 v[188:191], a[252:255], v[48:51], v[188:191]
  v_mfma_f32_16x16x32_bf16 v[220:223], a[252:255], v[52:55], v[220:223]
  v_mfma_f32_16x16x32_bf16 v[128:131], a[192:195], v[56:59], v[128:131]
  ds_read_b64_tr_b16 a[224:225], v3 offset:24576
  v_mfma_f32_16x16x32_bf16 v[160:163], a[192:195], v[60:63], v[160:163]
  s_mov_b32 s39, 327680
  s_add_u32 m0, s37, 65536
  ds_read_b64_tr_b16 a[226:227], v3 offset:28672
  v_mfma_f32_16x16x32_bf16 v[192:195], a[192:195], v[64:67], v[192:195]
  buffer_load_dwordx4 v11, s[20:23], s39 offen lds
  v_mfma_f32_16x16x32_bf16 v[132:135], a[196:199], v[56:59], v[132:135]
  ds_read_b64_tr_b16 a[228:229], v4 offset:24576
  v_mfma_f32_16x16x32_bf16 v[164:167], a[196:199], v[60:63], v[164:167]
  ds_read_b64_tr_b16 a[230:231], v4 offset:28672
  v_mfma_f32_16x16x32_bf16 v[196:199], a[196:199], v[64:67], v[196:199]
  ds_read_b64_tr_b16 a[232:233], v5 offset:24576
  v_mfma_f32_16x16x32_bf16 v[136:139], a[200:203], v[56:59], v[136:139]
  buffer_load_dwordx4 v12, s[20:23], s39 offen offset:1024 lds
  v_mfma_f32_16x16x32_bf16 v[168:171], a[200:203], v[60:63], v[168:171]
  ds_read_b64_tr_b16 a[234:235], v5 offset:28672
  v_mfma_f32_16x16x32_bf16 v[200:203], a[200:203], v[64:67], v[200:203]
  ds_read_b64_tr_b16 a[236:237], v6 offset:24576
  v_mfma_f32_16x16x32_bf16 v[140:143], a[204:207], v[56:59], v[140:143]
  s_mov_b32 s43, 368640
  s_add_u32 m0, s37, 73728
  ds_read_b64_tr_b16 a[238:239], v6 offset:28672
  v_mfma_f32_16x16x32_bf16 v[172:175], a[204:207], v[60:63], v[172:175]
  buffer_load_dwordx4 v11, s[20:23], s43 offen lds
  v_mfma_f32_16x16x32_bf16 v[204:207], a[204:207], v[64:67], v[204:207]
  ds_read_b64_tr_b16 a[240:241], v7 offset:24576
  v_mfma_f32_16x16x32_bf16 v[144:147], a[208:211], v[56:59], v[144:147]
  ds_read_b64_tr_b16 a[242:243], v7 offset:28672
  v_mfma_f32_16x16x32_bf16 v[176:179], a[208:211], v[60:63], v[176:179]
  ds_read_b64_tr_b16 a[244:245], v8 offset:24576
  v_mfma_f32_16x16x32_bf16 v[208:211], a[208:211], v[64:67], v[208:211]
  buffer_load_dwordx4 v12, s[20:23], s43 offen offset:1024 lds
  v_mfma_f32_16x16x32_bf16 v[148:151], a[212:215], v[56:59], v[148:151]
  ds_read_b64_tr_b16 a[246:247], v8 offset:28672
  v_mfma_f32_16x16x32_bf16 v[180:183], a[212:215], v[60:63], v[180:183]
  ds_read_b64_tr_b16 a[248:249], v9 offset:24576
  v_mfma_f32_16x16x32_bf16 v[212:215], a[212:215], v[64:67], v[212:215]
  s_waitcnt lgkmcnt(4)
  ds_read_b64_tr_b16 a[250:251], v9 offset:28672
  v_mfma_f32_16x16x32_bf16 v[152:155], a[216:219], v[56:59], v[152:155]
  s_mov_b32 s41, 8192
  ds_read_b64_tr_b16 a[252:253], v10 offset:24576
  v_mfma_f32_16x16x32_bf16 v[184:187], a[216:219], v[60:63], v[184:187]
  buffer_load_dwordx4 a[0:3], v0, s[16:19], s41 offen sc1
  v_mfma_f32_16x16x32_bf16 v[216:219], a[216:219], v[64:67], v[216:219]
  ds_read_b64_tr_b16 a[254:255], v10 offset:28672
  v_mfma_f32_16x16x32_bf16 v[156:159], a[220:223], v[56:59], v[156:159]
  v_mfma_f32_16x16x32_bf16 v[188:191], a[220:223], v[60:63], v[188:191]
  v_mfma_f32_16x16x32_bf16 v[220:223], a[220:223], v[64:67], v[220:223]
  buffer_load_dwordx4 a[4:7], v13, s[16:19], s41 offen sc1
  v_mfma_f32_16x16x32_bf16 v[128:131], a[224:227], v[68:71], v[128:131]
  ds_read_b64_tr_b16 a[192:193], v3 offset:32768
  v_mfma_f32_16x16x32_bf16 v[160:163], a[224:227], v[72:75], v[160:163]
  ds_read_b64_tr_b16 a[194:195], v3 offset:36864
  v_mfma_f32_16x16x32_bf16 v[192:195], a[224:227], v[76:79], v[192:195]
  ds_read_b64_tr_b16 a[196:197], v4 offset:32768
  v_mfma_f32_16x16x32_bf16 v[132:135], a[228:231], v[68:71], v[132:135]
  buffer_load_dwordx4 a[8:11], v14, s[16:19], s41 offen sc1
  v_mfma_f32_16x16x32_bf16 v[164:167], a[228:231], v[72:75], v[164:167]
  ds_read_b64_tr_b16 a[198:199], v4 offset:36864
  v_mfma_f32_16x16x32_bf16 v[196:199], a[228:231], v[76:79], v[196:199]
  ds_read_b64_tr_b16 a[200:201], v5 offset:32768
  v_mfma_f32_16x16x32_bf16 v[136:139], a[232:235], v[68:71], v[136:139]
  ds_read_b64_tr_b16 a[202:203], v5 offset:36864
  v_mfma_f32_16x16x32_bf16 v[168:171], a[232:235], v[72:75], v[168:171]
  s_mov_b32 s42, 9216
  ds_read_b64_tr_b16 a[204:205], v6 offset:32768
  v_mfma_f32_16x16x32_bf16 v[200:203], a[232:235], v[76:79], v[200:203]
  buffer_load_dwordx4 a[12:15], v0, s[16:19], s42 offen sc1
  v_mfma_f32_16x16x32_bf16 v[140:143], a[236:239], v[68:71], v[140:143]
  ds_read_b64_tr_b16 a[206:207], v6 offset:36864
  v_mfma_f32_16x16x32_bf16 v[172:175], a[236:239], v[72:75], v[172:175]
  ds_read_b64_tr_b16 a[208:209], v7 offset:32768
  v_mfma_f32_16x16x32_bf16 v[204:207], a[236:239], v[76:79], v[204:207]
  s_waitcnt lgkmcnt(4)
  ds_read_b64_tr_b16 a[210:211], v7 offset:36864
  v_mfma_f32_16x16x32_bf16 v[144:147], a[240:243], v[68:71], v[144:147]
  buffer_load_dwordx4 a[16:19], v13, s[16:19], s42 offen sc1
  v_mfma_f32_16x16x32_bf16 v[176:179], a[240:243], v[72:75], v[176:179]
  ds_read_b64_tr_b16 a[212:213], v8 offset:32768
  v_mfma_f32_16x16x32_bf16 v[208:211], a[240:243], v[76:79], v[208:211]
  ds_read_b64_tr_b16 a[214:215], v8 offset:36864
  v_mfma_f32_16x16x32_bf16 v[148:151], a[244:247], v[68:71], v[148:151]
  ds_read_b64_tr_b16 a[216:217], v9 offset:32768
  v_mfma_f32_16x16x32_bf16 v[180:183], a[244:247], v[72:75], v[180:183]
  buffer_load_dwordx4 a[20:23], v14, s[16:19], s42 offen sc1
  v_mfma_f32_16x16x32_bf16 v[212:215], a[244:247], v[76:79], v[212:215]
  ds_read_b64_tr_b16 a[218:219], v9 offset:36864
  v_mfma_f32_16x16x32_bf16 v[152:155], a[248:251], v[68:71], v[152:155]
  ds_read_b64_tr_b16 a[220:221], v10 offset:32768
  v_mfma_f32_16x16x32_bf16 v[184:187], a[248:251], v[72:75], v[184:187]
  ds_read_b64_tr_b16 a[222:223], v10 offset:36864
  v_mfma_f32_16x16x32_bf16 v[216:219], a[248:251], v[76:79], v[216:219]
  v_mfma_f32_16x16x32_bf16 v[156:159], a[252:255], v[68:71], v[156:159]
  v_mfma_f32_16x16x32_bf16 v[188:191], a[252:255], v[72:75], v[188:191]
  v_mfma_f32_16x16x32_bf16 v[220:223], a[252:255], v[76:79], v[220:223]
  s_waitcnt vmcnt(16)
  s_barrier
  v_mov_b32_e32 v252, 0x25000
  ds_read_b128 v[252:255], v252
  s_waitcnt lgkmcnt(0)
  v_or3_b32 v252, v252, v253, v254
  v_or_b32_e32 v252, v252, v255
  s_nop 0
  v_readfirstlane_b32 %[bad], v252
  s_nop 0
  s_cmp_lg_u32 %[bad], 0
  s_cbranch_scc1 .Ldone%=
  s_cmp_lg_u32 s36, 0
  s_cbranch_scc1 .Lnosig%=
  s_mov_b64 exec, 1
  v_mov_b32_e32 v250, s82
  v_mov_b32_e32 v251, s83
  global_store_dwordx2 v27, v[250:251], s[92:93] sc1
  s_mov_b64 exec, -1
  s_nop 0
.Lnosig%=:
  v_mfma_f32_16x16x32_bf16 v[128:131], a[192:195], v[80:83], v[128:131]
  ds_read_b64_tr_b16 a[224:225], v3 offset:40960
  v_mfma_f32_16x16x32_bf16 v[160:163], a[192:195], v[84:87], v[160:163]
  s_mov_b32 s39, 409600
  s_add_u32 m0, s37, 81920
  ds_read_b64_tr_b16 a[226:227], v3 offset:45056
  v_mfma_f32_16x16x32_bf16 v[192:195], a[192:195], v[88:91], v[192:195]
  buffer_load_dwordx4 v11, s[20:23], s39 offen lds
  v_mfma_f32_16x16x32_bf16 v[132:135], a[196:199], v[80:83], v[132:135]
  ds_read_b64_tr_b16 a[228:229], v4 offset:40960
  v_mfma_f32_16x16x32_bf16 v[164:167], a[196:199], v[84:87], v[164:167]
  ds_read_b64_tr_b16 a[230:231], v4 offset:45056
  v_mfma_f32_16x16x32_bf16 v[196:199], a[196:199], v[88:91], v[196:199]
  ds_read_b64_tr_b16 a[232:233], v5 offset:40960
  v_mfma_f32_16x16x32_bf16 v[136:139], a[200:203], v[80:83], v[136:139]
  buffer_load_dwordx4 v12, s[20:23], s39 offen offset:1024 lds
  v_mfma_f32_16x16x32_bf16 v[168:171], a[200:203], v[84:87], v[168:171]
  ds_read_b64_tr_b16 a[234:235], v5 offset:45056
  v_mfma_f32_16x16x32_bf16 v[200:203], a[200:203], v[88:91], v[200:203]
  ds_read_b64_tr_b16 a[236:237], v6 offset:40960
  v_mfma_f32_16x16x32_bf16 v[140:143], a[204:207], v[80:83], v[140:143]
  s_mov_b32 s43, 450560
  s_add_u32 m0, s37, 90112
  ds_read_b64_tr_b16 a[238:239], v6 offset:45056
  v_mfma_f32_16x16x32_bf16 v[172:175], a[204:207], v[84:87], v[172:175]
  buffer_load_dwordx4 v11, s[20:23], s43 offen lds
  v_mfma_f32_16x16x32_bf16 v[204:207], a[204:207], v[88:91], v[204:207]
  ds_read_b64_tr_b16 a[240:241], v7 offset:40960
  v_mfma_f32_16x16x32_bf16 v[144:147], a[208:211], v[80:83], v[144:147]
  ds_read_b64_tr_b16 a[242:243], v7 offset:45056
  v_mfma_f32_16x16x32_bf16 v[176:179], a[208:211], v[84:87], v[176:179]
  ds_read_b64_tr_b16 a[244:245], v8 offset:40960
  v_mfma_f32_16x16x32_bf16 v[208:211], a[208:211], v[88:91], v[208:211]
  buffer_load_dwordx4 v12, s[20:23], s43 offen offset:1024 lds
  v_mfma_f32_16x16x32_bf16 v[148:151], a[212:215], v[80:83], v[148:151]
  ds_read_b64_tr_b16 a[246:247], v8 offset:45056
  v_mfma_f32_16x16x32_bf16 v[180:183], a[212:215], v[84:87], v[180:183]
  ds_read_b64_tr_b16 a[248:249], v9 offset:40960
  v_mfma_f32_16x16x32_bf16 v[212:215], a[212:215], v[88:91], v[212:215]
  ds_read_b64_tr_b16 a[250:251], v9 offset:45056
  v_mfma_f32_16x16x32_bf16 v[152:155], a[216:219], v[80:83], v[152:155]
  s_mov_b32 s41, 10240
  ds_read_b64_tr_b16 a[252:253], v10 offset:40960
  v_mfma_f32_16x16x32_bf16 v[184:187], a[216:219], v[84:87], v[184:187]
  buffer_load_dwordx4 a[24:27], v0, s[16:19], s41 offen sc1
  v_mfma_f32_16x16x32_bf16 v[216:219], a[216:219], v[88:91], v[216:219]
  ds_read_b64_tr_b16 a[254:255], v10 offset:45056
  v_mfma_f32_16x16x32_bf16 v[156:159], a[220:223], v[80:83], v[156:159]
  v_mfma_f32_16x16x32_bf16 v[188:191], a[220:223], v[84:87], v[188:191]
  v_mfma_f32_16x16x32_bf16 v[220:223], a[220:223], v[88:91], v[220:223]
  s_waitcnt lgkmcnt(2)
  buffer_load_dwordx4 a[28:31], v13, s[16:19], s41 offen sc1
  v_mfma_f32_16x16x32_bf16 v[128:131], a[224:227], v[92:95], v[128:131]
  ds_read_b64_tr_b16 a[192:193], v3 offset:49152
  v_mfma_f32_16x16x32_bf16 v[160:163], a[224:227], v[96:99], v[160:163]
  ds_read_b64_tr_b16 a[194:195], v3 offset:53248
  v_mfma_f32_16x16x32_bf16 v[192:195], a[224:227], v[100:103], v[192:195]
  ds_read_b64_tr_b16 a[196:197], v4 offset:49152
  v_mfma_f32_16x16x32_bf16 v[132:135], a[228:231], v[92:95], v[132:135]
  buffer_load_dwordx4 a[32:35], v14, s[16:19], s41 offen sc1
  v_mfma_f32_16x16x32_bf16 v[164:167], a[228:231], v[96:99], v[164:167]
  ds_read_b64_tr_b16 a[198:199], v4 offset:53248
  v_mfma_f32_16x16x32_bf16 v[196:199], a[228:231], v[100:103], v[196:199]
  ds_read_b64_tr_b16 a[200:201], v5 offset:49152
  v_mfma_f32_16x16x32_bf16 v[136:139], a[232:235], v[92:95], v[136:139]
  ds_read_b64_tr_b16 a[202:203], v5 offset:53248
  v_mfma_f32_16x16x32_bf16 v[168:171], a[232:235], v[96:99], v[168:171]
  s_mov_b32 s42, 11264
  ds_read_b64_tr_b16 a[204:205], v6 offset:49152
  v_mfma_f32_16x16x32_bf16 v[200:203], a[232:235], v[100:103], v[200:203]
  buffer_load_dwordx4 a[36:39], v0, s[16:19], s42 offen sc1
  v_mfma_f32_16x16x32_bf16 v[140:143], a[236:239], v[92:95], v[140:143]
  ds_read_b64_tr_b16 a[206:207], v6 offset:53248
  v_mfma_f32_16x16x32_bf16 v[172:175], a[236:239], v[96:99], v[172:175]
  ds_read_b64_tr_b16 a[208:209], v7 offset:49152
  v_mfma_f32_16x16x32_bf16 v[204:207], a[236:239], v[100:103], v[204:207]
  ds_read_b64_tr_b16 a[210:211], v7 offset:53248
  v_mfma_f32_16x16x32_bf16 v[144:147], a[240:243], v[92:95], v[144:147]
  buffer_load_dwordx4 a[40:43], v13, s[16:19], s42 offen sc1
  v_mfma_f32_16x16x32_bf16 v[176:179], a[240:243], v[96:99], v[176:179]
  ds_read_b64_tr_b16 a[212:213], v8 offset:49152
  v_mfma_f32_16x16x32_bf16 v[208:211], a[240:243], v[100:103], v[208:211]
  ds_read_b64_tr_b16 a[214:215], v8 offset:53248
  v_mfma_f32_16x16x32_bf16 v[148:151], a[244:247], v[92:95], v[148:151]
  ds_read_b64_tr_b16 a[216:217], v9 offset:49152
  v_mfma_f32_16x16x32_bf16 v[180:183], a[244:247], v[96:99], v[180:183]
  buffer_load_dwordx4 a[44:47], v14, s[16:19], s42 offen sc1
  v_mfma_f32_16x16x32_bf16 v[212:215], a[244:247], v[100:103], v[212:215]
  ds_read_b64_tr_b16 a[218:219], v9 offset:53248
  v_mfma_f32_16x16x32_bf16 v[152:155], a[248:251], v[92:95], v[152:155]
  ds_read_b64_tr_b16 a[220:221], v10 offset:49152
  v_mfma_f32_16x16x32_bf16 v[184:187], a[248:251], v[96:99], v[184:187]
  ds_read_b64_tr_b16 a[222:223], v10 offset:53248
  v_mfma_f32_16x16x32_bf16 v[216:219], a[248:251], v[100:103], v[216:219]
  s_waitcnt lgkmcnt(4)
  v_mfma_f32_16x16x32_bf16 v[156:159], a[252:255], v[92:95], v[156:159]
  v_mfma_f32_16x16x32_bf16 v[188:191], a[252:255], v[96:99], v[188:191]
  v_mfma_f32_16x16x32_bf16 v[220:223], a[252:255], v[100:103], v[220:223]
  s_waitcnt vmcnt(16)
  s_barrier
  v_mfma_f32_16x16x32_bf16 v[128:131], a[192:195], v[104:107], v[128:131]
  ds_read_b64_tr_b16 a[224:225], v3 offset:57344
  v_mfma_f32_16x16x32_bf16 v[160:163], a[192:195], v[108:111], v[160:163]
  s_mov_b32 s39, 491520
  s_add_u32 m0, s37, 98304
  ds_read_b64_tr_b16 a[226:227], v3 offset:61440
  v_mfma_f32_16x16x32_bf16 v[192:195], a[192:195], v[112:115], v[192:195]
  buffer_load_dwordx4 v11, s[20:23], s39 offen lds
  v_mfma_f32_16x16x32_bf16 v[132:135], a[196:199], v[104:107], v[132:135]
  ds_read_b64_tr_b16 a[228:229], v4 offset:57344
  v_mfma_f32_16x16x32_bf16 v[164:167], a[196:199], v[108:111], v[164:167]
  ds_read_b64_tr_b16 a[230:231], v4 offset:61440
  v_mfma_f32_16x16x32_bf16 v[196:199], a[196:199], v[112:115], v[196:199]
  ds_read_b64_tr_b16 a[232:233], v5 offset:57344
  v_mfma_f32_16x16x32_bf16 v[136:139], a[200:203], v[104:107], v[136:139]
  buffer_load_dwordx4 v12, s[20:23], s39 offen offset:1024 lds
  v_mfma_f32_16x16x32_bf16 v[168:171], a[200:203], v[108:111], v[168:171]
  ds_read_b64_tr_b16 a[234:235], v5 offset:61440
  v_mfma_f32_16x16x32_bf16 v[200:203], a[200:203], v[112:115], v[200:203]
  ds_read_b64_tr_b16 a[236:237], v6 offset:57344
  v_mfma_f32_16x16x32_bf16 v[140:143], a[204:207], v[104:107], v[140:143]
  s_mov_b32 s43, 532480
  s_add_u32 m0, s37, 106496
  ds_read_b64_tr_b16 a[238:239], v6 offset:61440
  v_mfma_f32_16x16x32_bf16 v[172:175], a[204:207], v[108:111], v[172:175]
  buffer_load_dwordx4 v11, s[20:23], s43 offen lds
  v_mfma_f32_16x16x32_bf16 v[204:207], a[204:207], v[112:115], v[204:207]
  ds_read_b64_tr_b16 a[240:241], v7 offset:57344
  v_mfma_f32_16x16x32_bf16 v[144:147], a[208:211], v[104:107], v[144:147]
  ds_read_b64_tr_b16 a[242:243], v7 offset:61440
  v_mfma_f32_16x16x32_bf16 v[176:179], a[208:211], v[108:111], v[176:179]
  ds_read_b64_tr_b16 a[244:245], v8 offset:57344
  v_mfma_f32_16x16x32_bf16 v[208:211], a[208:211], v[112:115], v[208:211]
  buffer_load_dwordx4 v12, s[20:23], s43 offen offset:1024 lds
  v_mfma_f32_16x16x32_bf16 v[148:151], a[212:215], v[104:107], v[148:151]
  ds_read_b64_tr_b16 a[246:247], v8 offset:61440
  v_mfma_f32_16x16x32_bf16 v[180:183], a[212:215], v[108:111], v[180:183]
  ds_read_b64_tr_b16 a[248:249], v9 offset:57344
  v_mfma_f32_16x16x32_bf16 v[212:215], a[212:215], v[112:115], v[212:215]
  s_waitcnt lgkmcnt(4)
  ds_read_b64_tr_b16 a[250:251], v9 offset:61440
  v_mfma_f32_16x16x32_bf16 v[152:155], a[216:219], v[104:107], v[152:155]
  s_mov_b32 s41, 12288
  ds_read_b64_tr_b16 a[252:253], v10 offset:57344
  v_mfma_f32_16x16x32_bf16 v[184:187], a[216:219], v[108:111], v[184:187]
  buffer_load_dwordx4 a[48:51], v0, s[16:19], s41 offen sc1
  v_mfma_f32_16x16x32_bf16 v[216:219], a[216:219], v[112:115], v[216:219]
  ds_read_b64_tr_b16 a[254:255], v10 offset:61440
  v_mfma_f32_16x16x32_bf16 v[156:159], a[220:223], v[104:107], v[156:159]
  v_mfma_f32_16x16x32_bf16 v[188:191], a[220:223], v[108:111], v[188:191]
  v_mfma_f32_16x16x32_bf16 v[220:223], a[220:223], v[112:115], v[220:223]
  buffer_load_dwordx4 a[52:55], v13, s[16:19], s41 offen sc1
  v_mfma_f32_16x16x32_bf16 v[128:131], a[224:227], v[116:119], v[128:131]
  ds_read_b64_tr_b16 a[192:193], v224
  v_mfma_f32_16x16x32_bf16 v[160:163], a[224:227], v[120:123], v[160:163]
  ds_read_b64_tr_b16 a[194:195], v224 offset:4096
  v_mfma_f32_16x16x32_bf16 v[192:195], a[224:227], v[124:127], v[192:195]
  ds_read_b64_tr_b16 a[196:197], v225
  v_mfma_f32_16x16x32_bf16 v[132:135], a[228:231], v[116:119], v[132:135]
  buffer_load_dwordx4 a[56:59], v14, s[16:19], s41 offen sc1
  v_mfma_f32_16x16x32_bf16 v[164:167], a[228:231], v[120:123], v[164:167]
  ds_read_b64_tr_b16 a[198:199], v225 offset:4096
  v_mfma_f32_16x16x32_bf16 v[196:199], a[228:231], v[124:127], v[196:199]
  ds_read_b64_tr_b16 a[200:201], v226
  v_mfma_f32_16x16x32_bf16 v[136:139], a[232:235], v[116:119], v[136:139]
  ds_read_b64_tr_b16 a[202:203], v226 offset:4096
  v_mfma_f32_16x16x32_bf16 v[168:171], a[232:235], v[120:123], v[168:171]
  s_mov_b32 s42, 13312
  ds_read_b64_tr_b16 a[204:205], v227
  v_mfma_f32_16x16x32_bf16 v[200:203], a[232:235], v[124:127], v[200:203]
  buffer_load_dwordx4 a[60:63], v0, s[16:19], s42 offen sc1
  v_mfma_f32_16x16x32_bf16 v[140:143], a[236:239], v[116:119], v[140:143]
  ds_read_b64_tr_b16 a[206:207], v227 offset:4096
  v_mfma_f32_16x16x32_bf16 v[172:175], a[236:239], v[120:123], v[172:175]
  ds_read_b64_tr_b16 a[208:209], v228
  v_mfma_f32_16x16x32_bf16 v[204:207], a[236:239], v[124:127], v[204:207]
  s_waitcnt lgkmcnt(4)
  ds_read_b64_tr_b16 a[210:211], v228 offset:4096
  v_mfma_f32_16x16x32_bf16 v[144:147], a[240:243], v[116:119], v[144:147]
  buffer_load_dwordx4 a[64:67], v13, s[16:19], s42 offen sc1
  v_mfma_f32_16x16x32_bf16 v[176:179], a[240:243], v[120:123], v[176:179]
  ds_read_b64_tr_b16 a[212:213], v229
  v_mfma_f32_16x16x32_bf16 v[208:211], a[240:243], v[124:127], v[208:211]
  ds_read_b64_tr_b16 a[214:215], v229 offset:4096
  v_mfma_f32_16x16x32_bf16 v[148:151], a[244:247], v[116:119], v[148:151]
  ds_read_b64_tr_b16 a[216:217], v230
  v_mfma_f32_16x16x32_bf16 v[180:183], a[244:247], v[120:123], v[180:183]
  buffer_load_dwordx4 a[68:71], v14, s[16:19], s42 offen sc1
  v_mfma_f32_16x16x32_bf16 v[212:215], a[244:247], v[124:127], v[212:215]
  ds_read_b64_tr_b16 a[218:219], v230 offset:4096
  v_mfma_f32_16x16x32_bf16 v[152:155], a[248:251], v[116:119], v[152:155]
  ds_read_b64_tr_b16 a[220:221], v231
  v_mfma_f32_16x16x32_bf16 v[184:187], a[248:251], v[120:123], v[184:187]
  ds_read_b64_tr_b16 a[222:223], v231 offset:4096
  v_mfma_f32_16x16x32_bf16 v[216:219], a[248:251], v[124:127], v[216:219]
  v_mfma_f32_16x16x32_bf16 v[156:159], a[252:255], v[116:119], v[156:159]
  v_mfma_f32_16x16x32_bf16 v[188:191], a[252:255], v[120:123], v[188:191]
  v_mfma_f32_16x16x32_bf16 v[220:223], a[252:255], v[124:127], v[220:223]
  s_waitcnt vmcnt(16)
  s_barrier
  v_mfma_f32_16x16x32_bf16 v[128:131], a[192:195], a[0:3], v[128:131]
  ds_read_b64_tr_b16 a[224:225], v224 offset:8192
  v_mfma_f32_16x16x32_bf16 v[160:163], a[192:195], a[4:7], v[160:163]
  s_mov_b32 s39, 573440
  s_add_u32 m0, s37, 114688
  ds_read_b64_tr_b16 a[226:227], v224 offset:12288
  v_mfma_f32_16x16x32_bf16 v[192:195], a[192:195], a[8:11], v[192:195]
  buffer_load_dwordx4 v11, s[20:23], s39 offen lds
  v_mfma_f32_16x16x32_bf16 v[132:135], a[196:199], a[0:3], v[132:135]
  ds_read_b64_tr_b16 a[228:229], v225 offset:8192
  v_mfma_f32_16x16x32_bf16 v[164:167], a[196:199], a[4:7], v[164:167]
  ds_read_b64_tr_b16 a[230:231], v225 offset:12288
  v_mfma_f32_16x16x32_bf16 v[196:199], a[196:199], a[8:11], v[196:199]
  s_waitcnt lgkmcnt(4)
  ds_read_b64_tr_b16 a[232:233], v226 offset:8192
  v_mfma_f32_16x16x32_bf16 v[136:139], a[200:203], a[0:3], v[136:139]
  buffer_load_dwordx4 v12, s[20:23], s39 offen offset:1024 lds
  v_mfma_f32_16x16x32_bf16 v[168:171], a[200:203], a[4:7], v[168:171]
  ds_read_b64_tr_b16 a[234:235], v226 offset:12288
  v_mfma_f32_16x16x32_bf16 v[200:203], a[200:203], a[8:11], v[200:203]
  ds_read_b64_tr_b16 a[236:237], v227 offset:8192
  v_mfma_f32_16x16x32_bf16 v[140:143], a[204:207], a[0:3], v[140:143]
  s_mov_b32 s43, 614400
  s_add_u32 m0, s37, 122880
  ds_read_b64_tr_b16 a[238:239], v227 offset:12288
  v_mfma_f32_16x16x32_bf16 v[172:175], a[204:207], a[4:7], v[172:175]
  buffer_load_dwordx4 v11, s[20:23], s43 offen lds
  v_mfma_f32_16x16x32_bf16 v[204:207], a[204:207], a[8:11], v[204:207]
  ds_read_b64_tr_b16 a[240:241], v228 offset:8192
  v_mfma_f32_16x16x32_bf16 v[144:147], a[208:211], a[0:3], v[144:147]
  ds_read_b64_tr_b16 a[242:243], v228 offset:12288
  v_mfma_f32_16x16x32_bf16 v[176:179], a[208:211], a[4:7], v[176:179]
  ds_read_b64_tr_b16 a[244:245], v229 offset:8192
  v_mfma_f32_16x16x32_bf16 v[208:211], a[208:211], a[8:11], v[208:211]
  buffer_load_dwordx4 v12, s[20:23], s43 offen offset:1024 lds
  v_mfma_f32_16x16x32_bf16 v[148:151], a[212:215], a[0:3], v[148:151]
  ds_read_b64_tr_b16 a[246:247], v229 offset:12288
  v_mfma_f32_16x16x32_bf16 v[180:183], a[212:215], a[4:7], v[180:183]
  ds_read_b64_tr_b16 a[248:249], v230 offset:8192
  v_mfma_f32_16x16x32_bf16 v[212:215], a[212:215], a[8:11], v[212:215]
  ds_read_b64_tr_b16 a[250:251], v230 offset:12288
  v_mfma_f32_16x16x32_bf16 v[152:155], a[216:219], a[0:3], v[152:155]
  s_mov_b32 s41, 14336
  ds_read_b64_tr_b16 a[252:253], v231 offset:8192
  v_mfma_f32_16x16x32_bf16 v[184:187], a[216:219], a[4:7], v[184:187]
  buffer_load_dwordx4 a[72:75], v0, s[16:19], s41 offen sc1
  v_mfma_f32_16x16x32_bf16 v[216:219], a[216:219], a[8:11], v[216:219]
  ds_read_b64_tr_b16 a[254:255], v231 offset:12288
  v_mfma_f32_16x16x32_bf16 v[156:159], a[220:223], a[0:3], v[156:159]
  v_mfma_f32_16x16x32_bf16 v[188:191], a[220:223], a[4:7], v[188:191]
  v_mfma_f32_16x16x32_bf16 v[220:223], a[220:223], a[8:11], v[220:223]
  s_waitcnt lgkmcnt(2)
  buffer_load_dwordx4 a[76:79], v13, s[16:19], s41 offen sc1
  v_mfma_f32_16x16x32_bf16 v[128:131], a[224:227], a[12:15], v[128:131]
  ds_read_b64_tr_b16 a[192:193], v224 offset:16384
  v_mfma_f32_16x16x32_bf16 v[160:163], a[224:227], a[16:19], v[160:163]
  ds_read_b64_tr_b16 a[194:195], v224 offset:20480
  v_mfma_f32_16x16x32_bf16 v[192:195], a[224:227], a[20:23], v[192:195]
  ds_read_b64_tr_b16 a[196:197], v225 offset:16384
  v_mfma_f32_16x16x32_bf16 v[132:135], a[228:231], a[12:15], v[132:135]
  buffer_load_dwordx4 a[80:83], v14, s[16:19], s41 offen sc1
  v_mfma_f32_16x16x32_bf16 v[164:167], a[228:231], a[16:19], v[164:167]
  ds_read_b64_tr_b16 a[198:199], v225 offset:20480
  v_mfma_f32_16x16x32_bf16 v[196:199], a[228:231], a[20:23], v[196:199]
  ds_read_b64_tr_b16 a[200:201], v226 offset:16384
  v_mfma_f32_16x16x32_bf16 v[136:139], a[232:235], a[12:15], v[136:139]
  ds_read_b64_tr_b16 a[202:203], v226 offset:20480
  v_mfma_f32_16x16x32_bf16 v[168:171], a[232:235], a[16:19], v[168:171]
  s_mov_b32 s42, 15360
  ds_read_b64_tr_b16 a[204:205], v227 offset:16384
  v_mfma_f32_16x16x32_bf16 v[200:203], a[232:235], a[20:23], v[200:203]
  buffer_load_dwordx4 a[84:87], v0, s[16:19], s42 offen sc1
  v_mfma_f32_16x16x32_bf16 v[140:143], a[236:239], a[12:15], v[140:143]
  ds_read_b64_tr_b16 a[206:207], v227 offset:20480
  v_mfma_f32_16x16x32_bf16 v[172:175], a[236:239], a[16:19], v[172:175]
  ds_read_b64_tr_b16 a[208:209], v228 offset:16384
  v_mfma_f32_16x16x32_bf16 v[204:207], a[236:239], a[20:23], v[204:207]
  ds_read_b64_tr_b16 a[210:211], v228 offset:20480
  v_mfma_f32_16x16x32_bf16 v[144:147], a[240:243], a[12:15], v[144:147]
  buffer_load_dwordx4 a[88:91], v13, s[16:19], s42 offen sc1
  v_mfma_f32_16x16x32_bf16 v[176:179], a[240:243], a[16:19], v[176:179]
  ds_read_b64_tr_b16 a[212:213], v229 offset:16384
  v_mfma_f32_16x16x32_bf16 v[208:211], a[240:243], a[20:23], v[208:211]
  ds_read_b64_tr_b16 a[214:215], v229 offset:20480
  v_mfma_f32_16x16x32_bf16 v[148:151], a[244:247], a[12:15], v[148:151]
  ds_read_b64_tr_b16 a[216:217], v230 offset:16384
  v_mfma_f32_16x16x32_bf16 v[180:183], a[244:247], a[16:19], v[180:183]
  buffer_load_dwordx4 a[92:95], v14, s[16:19], s42 offen sc1
  v_mfma_f32_16x16x32_bf16 v[212:215], a[244:247], a[20:23], v[212:215]
  ds_read_b64_tr_b16 a[218:219], v230 offset:20480
  v_mfma_f32_16x16x32_bf16 v[152:155], a[248:251], a[12:15], v[152:155]
  ds_read_b64_tr_b16 a[220:221], v231 offset:16384
  v_mfma_f32_16x16x32_bf16 v[184:187], a[248:251], a[16:19], v[184:187]
  ds_read_b64_tr_b16 a[222:223], v231 offset:20480
  v_mfma_f32_16x16x32_bf16 v[216:219], a[248:251], a[20:23], v[216:219]
  s_waitcnt lgkmcnt(4)
  v_mfma_f32_16x16x32_bf16 v[156:159], a[252:255], a[12:15], v[156:159]
  v_mfma_f32_16x16x32_bf16 v[188:191], a[252:255], a[16:19], v[188:191]
  s_mov_b64 exec, 0xff
  global_load_dwordx2 v[246:247], v26, s[92:93] sc1
  s_mov_b64 exec, -1
  v_mfma_f32_16x16x32_bf16 v[220:223], a[252:255], a[20:23], v[220:223]
  s_waitcnt vmcnt(17)
  s_barrier
  v_mfma_f32_16x16x32_bf16 v[128:131], a[192:195], a[24:27], v[128:131]
  ds_read_b64_tr_b16 a[224:225], v224 offset:24576
  v_mfma_f32_16x16x32_bf16 v[160:163], a[192:195], a[28:31], v[160:163]
  s_mov_b32 s39, 655360
  s_add_u32 m0, s37, 0
  ds_read_b64_tr_b16 a[226:227], v224 offset:28672
  v_mfma_f32_16x16x32_bf16 v[192:195], a[192:195], a[32:35], v[192:195]
  buffer_load_dwordx4 v11, s[20:23], s39 offen lds
  v_mfma_f32_16x16x32_bf16 v[132:135], a[196:199], a[24:27], v[132:135]
  ds_read_b64_tr_b16 a[228:229], v225 offset:24576
  v_mfma_f32_16x16x32_bf16 v[164:167], a[196:199], a[28:31], v[164:167]
  ds_read_b64_tr_b16 a[230:231], v225 offset:28672
  v_mfma_f32_16x16x32_bf16 v[196:199], a[196:199], a[32:35], v[196:199]
  ds_read_b64_tr_b16 a[232:233], v226 offset:24576
  v_mfma_f32_16x16x32_bf16 v[136:139], a[200:203], a[24:27], v[136:139]
  buffer_load_dwordx4 v12, s[20:23], s39 offen offset:1024 lds
  v_mfma_f32_16x16x32_bf16 v[168:171], a[200:203], a[28:31], v[168:171]
  ds_read_b64_tr_b16 a[234:235], v226 offset:28672
  v_mfma_f32_16x16x32_bf16 v[200:203], a[200:203], a[32:35], v[200:203]
  ds_read_b64_tr_b16 a[236:237], v227 offset:24576
  v_mfma_f32_16x16x32_bf16 v[140:143], a[204:207], a[24:27], v[140:143]
  ds_read_b64_tr_b16 a[238:239], v227 offset:28672
  v_mfma_f32_16x16x32_bf16 v[172:175], a[204:207], a[28:31], v[172:175]
  s_mov_b32 s43, 696320
  s_add_u32 m0, s37, 8192
  ds_read_b64_tr_b16 a[240:241], v228 offset:24576
  v_mfma_f32_16x16x32_bf16 v[204:207], a[204:207], a[32:35], v[204:207]
  buffer_load_dwordx4 v11, s[20:23], s43 offen lds
  v_mfma_f32_16x16x32_bf16 v[144:147], a[208:211], a[24:27], v[144:147]
  ds_read_b64_tr_b16 a[242:243], v228 offset:28672
  v_mfma_f32_16x16x32_bf16 v[176:179], a[208:211], a[28:31], v[176:179]
  ds_read_b64_tr_b16 a[244:245], v229 offset:24576
  v_mfma_f32_16x16x32_bf16 v[208:211], a[208:211], a[32:35], v[208:211]
  ds_read_b64_tr_b16 a[246:247], v229 offset:28672
  v_mfma_f32_16x16x32_bf16 v[148:151], a[212:215], a[24:27], v[148:151]
  buffer_load_dwordx4 v12, s[20:23], s43 offen offset:1024 lds
  v_mfma_f32_16x16x32_bf16 v[180:183], a[212:215], a[28:31], v[180:183]
  ds_read_b64_tr_b16 a[248:249], v230 offset:24576
  v_mfma_f32_16x16x32_bf16 v[212:215], a[212:215], a[32:35], v[212:215]
  s_waitcnt lgkmcnt(4)
  ds_read_b64_tr_b16 a[250:251], v230 offset:28672
  v_mfma_f32_16x16x32_bf16 v[152:155], a[216:219], a[24:27], v[152:155]
  ds_read_b64_tr_b16 a[252:253], v231 offset:24576
  v_mfma_f32_16x16x32_bf16 v[184:187], a[216:219], a[28:31], v[184:187]
  ds_read_b64_tr_b16 a[254:255], v231 offset:28672
  v_mfma_f32_16x16x32_bf16 v[216:219], a[216:219], a[32:35], v[216:219]
  v_mfma_f32_16x16x32_bf16 v[156:159], a[220:223], a[24:27], v[156:159]
  v_mfma_f32_16x16x32_bf16 v[188:191], a[220:223], a[28:31], v[188:191]
  v_mfma_f32_16x16x32_bf16 v[220:223], a[220:223], a[32:35], v[220:223]
  v_mfma_f32_16x16x32_bf16 v[128:131], a[224:227], a[36:39], v[128:131]
  ds_read_b64_tr_b16 a[192:193], v224 offset:32768
  v_mfma_f32_16x16x32_bf16 v[160:163], a[224:227], a[40:43], v[160:163]
  ds_read_b64_tr_b16 a[194:195], v224 offset:36864
  v_mfma_f32_16x16x32_bf16 v[192:195], a[224:227], a[44:47], v[192:195]
  ds_read_b64_tr_b16 a[196:197], v225 offset:32768
  v_mfma_f32_16x16x32_bf16 v[132:135], a[228:231], a[36:39], v[132:135]
  ds_read_b64_tr_b16 a[198:199], v225 offset:36864
  v_mfma_f32_16x16x32_bf16 v[164:167], a[228:231], a[40:43], v[164:167]
  ds_read_b64_tr_b16 a[200:201], v226 offset:32768
  v_mfma_f32_16x16x32_bf16 v[196:199], a[228:231], a[44:47], v[196:199]
  ds_read_b64_tr_b16 a[202:203], v226 offset:36864
  v_mfma_f32_16x16x32_bf16 v[136:139], a[232:235], a[36:39], v[136:139]
  ds_read_b64_tr_b16 a[204:205], v227 offset:32768
  v_mfma_f32_16x16x32_bf16 v[168:171], a[232:235], a[40:43], v[168:171]
  ds_read_b64_tr_b16 a[206:207], v227 offset:36864
  v_mfma_f32_16x16x32_bf16 v[200:203], a[232:235], a[44:47], v[200:203]
  ds_read_b64_tr_b16 a[208:209], v228 offset:32768
  v_mfma_f32_16x16x32_bf16 v[140:143], a[236:239], a[36:39], v[140:143]
  ds_read_b64_tr_b16 a[210:211], v228 offset:36864
  v_mfma_f32_16x16x32_bf16 v[172:175], a[236:239], a[40:43], v[172:175]
  ds_read_b64_tr_b16 a[212:213], v229 offset:32768
  v_mfma_f32_16x16x32_bf16 v[204:207], a[236:239], a[44:47], v[204:207]
  s_waitcnt lgkmcnt(5)
  ds_read_b64_tr_b16 a[214:215], v229 offset:36864
  v_mfma_f32_16x16x32_bf16 v[144:147], a[240:243], a[36:39], v[144:147]
  ds_read_b64_tr_b16 a[216:217], v230 offset:32768
  v_mfma_f32_16x16x32_bf16 v[176:179], a[240:243], a[40:43], v[176:179]
  ds_read_b64_tr_b16 a[218:219], v230 offset:36864
  v_mfma_f32_16x16x32_bf16 v[208:211], a[240:243], a[44:47], v[208:211]
  ds_read_b64_tr_b16 a[220:221], v231 offset:32768
  v_mfma_f32_16x16x32_bf16 v[148:151], a[244:247], a[36:39], v[148:151]
  ds_read_b64_tr_b16 a[222:223], v231 offset:36864
  v_mfma_f32_16x16x32_bf16 v[180:183], a[244:247], a[40:43], v[180:183]
  v_mfma_f32_16x16x32_bf16 v[212:215], a[244:247], a[44:47], v[212:215]
  v_mfma_f32_16x16x32_bf16 v[152:155], a[248:251], a[36:39], v[152:155]
  v_mfma_f32_16x16x32_bf16 v[184:187], a[248:251], a[40:43], v[184:187]
  v_mfma_f32_16x16x32_bf16 v[216:219], a[248:251], a[44:47], v[216:219]
  v_mfma_f32_16x16x32_bf16 v[156:159], a[252:255], a[36:39], v[156:159]
  v_mfma_f32_16x16x32_bf16 v[188:191], a[252:255], a[40:43], v[188:191]
  v_mfma_f32_16x16x32_bf16 v[220:223], a[252:255], a[44:47], v[220:223]
  s_waitcnt vmcnt(4)
  s_barrier
  s_mov_b64 exec, 0xff
  s_mov_b32 s73, 0x20000
  s_branch .Lo1c%=
.Lo1a%=:
  global_load_dwordx2 v[246:247], v26, s[92:93] sc1
  s_waitcnt vmcnt(0)
.Lo1c%=:
  v_cmp_ne_u64_e32 vcc, s[82:83], v[246:247]
  s_sub_u32 s73, s73, 1
  s_cbranch_scc1 .Lo1b%=
  s_nop 3
  s_cbranch_vccnz .Lo1a%=
.Lo1b%=:
  s_mov_b64 exec, -1
  v_mfma_f32_16x16x32_bf16 v[128:131], a[192:195], a[48:51], v[128:131]
  ds_read_b64_tr_b16 a[224:225], v224 offset:40960
  v_mfma_f32_16x16x32_bf16 v[160:163], a[192:195], a[52:55], v[160:163]
  s_mov_b32 s41, 16384
  ds_read_b64_tr_b16 a[226:227], v224 offset:45056
  v_mfma_f32_16x16x32_bf16 v[192:195], a[192:195], a[56:59], v[192:195]
  ds_read_b64_tr_b16 a[228:229], v225 offset:40960
  buffer_load_dwordx4 v[32:35], v0, s[16:19], s41 offen sc1
  v_mfma_f32_16x16x32_bf16 v[132:135], a[196:199], a[48:51], v[132:135]
  ds_read_b64_tr_b16 a[230:231], v225 offset:45056
  v_mfma_f32_16x16x32_bf16 v[164:167], a[196:199], a[52:55], v[164:167]
  ds_read_b64_tr_b16 a[232:233], v226 offset:40960
  buffer_load_dwordx4 v[36:39], v13, s[16:19], s41 offen sc1
  v_mfma_f32_16x16x32_bf16 v[196:199], a[196:199], a[56:59], v[196:199]
  ds_read_b64_tr_b16 a[234:235], v226 offset:45056
  v_mfma_f32_16x16x32_bf16 v[136:139], a[200:203], a[48:51], v[136:139]
  ds_read_b64_tr_b16 a[236:237], v227 offset:40960
  v_mfma_f32_16x16x32_bf16 v[168:171], a[200:203], a[52:55], v[168:171]
  ds_read_b64_tr_b16 a[238:239], v227 offset:45056
  buffer_load_dwordx4 v[40:43], v14, s[16:19], s41 offen sc1
  v_mfma_f32_16x16x32_bf16 v[200:203], a[200:203], a[56:59], v[200:203]
  s_waitcnt lgkmcnt(5)
  ds_read_b64_tr_b16 a[240:241], v228 offset:40960
  v_mfma_f32_16x16x32_bf16 v[140:143], a[204:207], a[48:51], v[140:143]
  s_mov_b32 s42, 17408
  ds_read_b64_tr_b16 a[242:243], v228 offset:45056
  v_mfma_f32_16x16x32_bf16 v[172:175], a[204:207], a[52:55], v[172:175]
  ds_read_b64_tr_b16 a[244:245], v229 offset:40960
  buffer_load_dwordx4 v[44:47], v0, s[16:19], s42 offen sc1
  v_mfma_f32_16x16x32_bf16 v[204:207], a[204:207], a[56:59], v[204:207]
  ds_read_b64_tr_b16 a[246:247], v229 offset:45056
  v_mfma_f32_16x16x32_bf16 v[144:147], a[208:211], a[48:51], v[144:147]
  ds_read_b64_tr_b16 a[248:249], v230 offset:40960
  buffer_load_dwordx4 v[48:51], v13, s[16:19], s42 offen sc1
  v_mfma_f32_16x16x32_bf16 v[176:179], a[208:211], a[52:55], v[176:179]
  ds_read_b64_tr_b16 a[250:251], v230 offset:45056
  v_mfma_f32_16x16x32_bf16 v[208:211], a[208:211], a[56:59], v[208:211]
  ds_read_b64_tr_b16 a[252:253], v231 offset:40960
  v_mfma_f32_16x16x32_bf16 v[148:151], a[212:215], a[48:51], v[148:151]
  ds_read_b64_tr_b16 a[254:255], v231 offset:45056
  buffer_load_dwordx4 v[52:55], v14, s[16:19], s42 offen sc1
  v_mfma_f32_16x16x32_bf16 v[180:183], a[212:215], a[52:55], v[180:183]
  v_mfma_f32_16x16x32_bf16 v[212:215], a[212:215], a[56:59], v[212:215]
  s_mov_b32 s39, 737280
  s_add_u32 m0, s37, 16384
  v_mfma_f32_16x16x32_bf16 v[152:155], a[216:219], a[48:51], v[152:155]
  buffer_load_dwordx4 v11, s[20:23], s39 offen lds
  v_mfma_f32_16x16x32_bf16 v[184:187], a[216:219], a[52:55], v[184:187]
  v_mfma_f32_16x16x32_bf16 v[216:219], a[216:219], a[56:59], v[216:219]
  buffer_load_dwordx4 v12, s[20:23], s39 offen offset:1024 lds
  v_mfma_f32_16x16x32_bf16 v[156:159], a[220:223], a[48:51], v[156:159]
  v_mfma_f32_16x16x32_bf16 v[188:191], a[220:223], a[52:55], v[188:191]
  s_mov_b32 s43, 778240
  s_add_u32 m0, s37, 24576
  v_mfma_f32_16x16x32_bf16 v[220:223], a[220:223], a[56:59], v[220:223]
  buffer_load_dwordx4 v11, s[20:23], s43 offen lds
  v_mfma_f32_16x16x32_bf16 v[128:131], a[224:227], a[60:63], v[128:131]
  ds_read_b64_tr_b16 a[192:193], v224 offset:49152
  v_mfma_f32_16x16x32_bf16 v[160:163], a[224:227], a[64:67], v[160:163]
  ds_read_b64_tr_b16 a[194:195], v224 offset:53248
  v_mfma_f32_16x16x32_bf16 v[192:195], a[224:227], a[68:71], v[192:195]
  s_waitcnt lgkmcnt(2)
  ds_read_b64_tr_b16 a[196:197], v225 offset:49152
  buffer_load_dwordx4 v12, s[20:23], s43 offen offset:1024 lds
  v_mfma_f32_16x16x32_bf16 v[132:135], a[228:231], a[60:63], v[132:135]
  s_mov_b32 s41, 18432
  ds_read_b64_tr_b16 a[198:199], v225 offset:53248
  v_mfma_f32_16x16x32_bf16 v[164:167], a[228:231], a[64:67], v[164:167]
  ds_read_b64_tr_b16 a[200:201], v226 offset:49152
  buffer_load_dwordx4 v[56:59], v0, s[16:19], s41 offen sc1
  v_mfma_f32_16x16x32_bf16 v[196:199], a[228:231], a[68:71], v[196:199]
  ds_read_b64_tr_b16 a[202:203], v226 offset:53248
  v_mfma_f32_16x16x32_bf16 v[136:139], a[232:235], a[60:63], v[136:139]
  ds_read_b64_tr_b16 a[204:205], v227 offset:49152
  v_mfma_f32_16x16x32_bf16 v[168:171], a[232:235], a[64:67], v[168:171]
  ds_read_b64_tr_b16 a[206:207], v227 offset:53248
  buffer_load_dwordx4 v[60:63], v13, s[16:19], s41 offen sc1
  v_mfma_f32_16x16x32_bf16 v[200:203], a[232:235], a[68:71], v[200:203]
  ds_read_b64_tr_b16 a[208:209], v228 offset:49152
  v_mfma_f32_16x16x32_bf16 v[140:143], a[236:239], a[60:63], v[140:143]
  ds_read_b64_tr_b16 a[210:211], v228 offset:53248
  v_mfma_f32_16x16x32_bf16 v[172:175], a[236:239], a[64:67], v[172:175]
  ds_read_b64_tr_b16 a[212:213], v229 offset:49152
  buffer_load_dwordx4 v[64:67], v14, s[16:19], s41 offen sc1
  v_mfma_f32_16x16x32_bf16 v[204:207], a[236:239], a[68:71], v[204:207]
  s_mov_b32 s42, 19456
  ds_read_b64_tr_b16 a[214:215], v229 offset:53248
  v_mfma_f32_16x16x32_bf16 v[144:147], a[240:243], a[60:63], v[144:147]
  ds_read_b64_tr_b16 a[216:217], v230 offset:49152
  buffer_load_dwordx4 v[68:71], v0, s[16:19], s42 offen sc1
  v_mfma_f32_16x16x32_bf16 v[176:179], a[240:243], a[64:67], v[176:179]
  ds_read_b64_tr_b16 a[218:219], v230 offset:53248
  v_mfma_f32_16x16x32_bf16 v[208:211], a[240:243], a[68:71], v[208:211]
  ds_read_b64_tr_b16 a[220:221], v231 offset:49152
  v_mfma_f32_16x16x32_bf16 v[148:151], a[244:247], a[60:63], v[148:151]
  ds_read_b64_tr_b16 a[222:223], v231 offset:53248
  buffer_load_dwordx4 v[72:75], v13, s[16:19], s42 offen sc1
  v_mfma_f32_16x16x32_bf16 v[180:183], a[244:247], a[64:67], v[180:183]
  v_mfma_f32_16x16x32_bf16 v[212:215], a[244:247], a[68:71], v[212:215]
  v_mfma_f32_16x16x32_bf16 v[152:155], a[248:251], a[60:63], v[152:155]
  buffer_load_dwordx4 v[76:79], v14, s[16:19], s42 offen sc1
  v_mfma_f32_16x16x32_bf16 v[184:187], a[248:251], a[64:67], v[184:187]
  v_mfma_f32_16x16x32_bf16 v[216:219], a[248:251], a[68:71], v[216:219]
  v_mfma_f32_16x16x32_bf16 v[156:159], a[252:255], a[60:63], v[156:159]
  v_mfma_f32_16x16x32_bf16 v[188:191], a[252:255], a[64:67], v[188:191]
  v_mfma_f32_16x16x32_bf16 v[220:223], a[252:255], a[68:71], v[220:223]
  s_waitcnt lgkmcnt(0)
  s_waitcnt vmcnt(16)
  s_barrier
  v_mfma_f32_16x16x32_bf16 v[128:131], a[192:195], a[72:75], v[128:131]
  ds_read_b64_tr_b16 a[224:225], v224 offset:57344
  v_mfma_f32_16x16x32_bf16 v[160:163], a[192:195], a[76:79], v[160:163]
  s_mov_b32 s39, 819200
  s_add_u32 m0, s37, 32768
  ds_read_b64_tr_b16 a[226:227], v224 offset:61440
  v_mfma_f32_16x16x32_bf16 v[192:195], a[192:195], a[80:83], v[192:195]
  buffer_load_dwordx4 v11, s[20:23], s39 offen lds
  v_mfma_f32_16x16x32_bf16 v[132:135], a[196:199], a[72:75], v[132:135]
  ds_read_b64_tr_b16 a[228:229], v225 offset:57344
  v_mfma_f32_16x16x32_bf16 v[164:167], a[196:199], a[76:79], v[164:167]
  ds_read_b64_tr_b16 a[230:231], v225 offset:61440
  v_mfma_f32_16x16x32_bf16 v[196:199], a[196:199], a[80:83], v[196:199]
  ds_read_b64_tr_b16 a[232:233], v226 offset:57344
  v_mfma_f32_16x16x32_bf16 v[136:139], a[200:203], a[72:75], v[136:139]
  buffer_load_dwordx4 v12, s[20:23], s39 offen offset:1024 lds
  v_mfma_f32_16x16x32_bf16 v[168:171], a[200:203], a[76:79], v[168:171]
  ds_read_b64_tr_b16 a[234:235], v226 offset:61440
  v_mfma_f32_16x16x32_bf16 v[200:203], a[200:203], a[80:83], v[200:203]
  ds_read_b64_tr_b16 a[236:237], v227 offset:57344
  v_mfma_f32_16x16x32_bf16 v[140:143], a[204:207], a[72:75], v[140:143]
  s_mov_b32 s43, 860160
  s_add_u32 m0, s37, 40960
  ds_read_b64_tr_b16 a[238:239], v227 offset:61440
  v_mfma_f32_16x16x32_bf16 v[172:175], a[204:207], a[76:79], v[172:175]
  buffer_load_dwordx4 v11, s[20:23], s43 offen lds
  v_mfma_f32_16x16x32_bf16 v[204:207], a[204:207], a[80:83], v[204:207]
  ds_read_b64_tr_b16 a[240:241], v228 offset:57344
  v_mfma_f32_16x16x32_bf16 v[144:147], a[208:211], a[72:75], v[144:147]
  ds_read_b64_tr_b16 a[242:243], v228 offset:61440
  v_mfma_f32_16x16x32_bf16 v[176:179], a[208:211], a[76:79], v[176:179]
  ds_read_b64_tr_b16 a[244:245], v229 offset:57344
  v_mfma_f32_16x16x32_bf16 v[208:211], a[208:211], a[80:83], v[208:211]
  buffer_load_dwordx4 v12, s[20:23], s43 offen offset:1024 lds
  v_mfma_f32_16x16x32_bf16 v[148:151], a[212:215], a[72:75], v[148:151]
  ds_read_b64_tr_b16 a[246:247], v229 offset:61440
  v_mfma_f32_16x16x32_bf16 v[180:183], a[212:215], a[76:79], v[180:183]
  ds_read_b64_tr_b16 a[248:249], v230 offset:57344
  v_mfma_f32_16x16x32_bf16 v[212:215], a[212:215], a[80:83], v[212:215]
  ds_read_b64_tr_b16 a[250:251], v230 offset:61440
  v_mfma_f32_16x16x32_bf16 v[152:155], a[216:219], a[72:75], v[152:155]
  s_mov_b32 s41, 20480
  ds_read_b64_tr_b16 a[252:253], v231 offset:57344
  v_mfma_f32_16x16x32_bf16 v[184:187], a[216:219], a[76:79], v[184:187]
  buffer_load_dwordx4 v[80:83], v0, s[16:19], s41 offen sc1
  v_mfma_f32_16x16x32_bf16 v[216:219], a[216:219], a[80:83], v[216:219]
  ds_read_b64_tr_b16 a[254:255], v231 offset:61440
  v_mfma_f32_16x16x32_bf16 v[156:159], a[220:223], a[72:75], v[156:159]
  v_mfma_f32_16x16x32_bf16 v[188:191], a[220:223], a[76:79], v[188:191]
  v_mfma_f32_16x16x32_bf16 v[220:223], a[220:223], a[80:83], v[220:223]
  s_waitcnt lgkmcnt(2)
  buffer_load_dwordx4 v[84:87], v13, s[16:19], s41 offen sc1
  v_mfma_f32_16x16x32_bf16 v[128:131], a[224:227], a[84:87], v[128:131]
  ds_read_b64_tr_b16 a[200:201], v5
  v_mfma_f32_16x16x32_bf16 v[160:163], a[224:227], a[88:91], v[160:163]
  ds_read_b64_tr_b16 a[202:203], v5 offset:4096
  v_mfma_f32_16x16x32_bf16 v[192:195], a[224:227], a[92:95], v[192:195]
  ds_read_b64_tr_b16 a[204:205], v6
  v_mfma_f32_16x16x32_bf16 v[132:135], a[228:231], a[84:87], v[132:135]
  buffer_load_dwordx4 v[88:91], v14, s[16:19], s41 offen sc1
  v_mfma_f32_16x16x32_bf16 v[164:167], a[228:231], a[88:91], v[164:167]
  ds_read_b64_tr_b16 a[206:207], v6 offset:4096
  v_mfma_f32_16x16x32_bf16 v[196:199], a[228:231], a[92:95], v[196:199]
  ds_read_b64_tr_b16 a[208:209], v7
  v_mfma_f32_16x16x32_bf16 v[136:139], a[232:235], a[84:87], v[136:139]
  ds_read_b64_tr_b16 a[210:211], v7 offset:4096
  v_mfma_f32_16x16x32_bf16 v[168:171], a[232:235], a[88:91], v[168:171]
  s_mov_b32 s42, 21504
  ds_read_b64_tr_b16 a[212:213], v8
  v_mfma_f32_16x16x32_bf16 v[200:203], a[232:235], a[92:95], v[200:203]
  buffer_load_dwordx4 v[92:95], v0, s[16:19], s42 offen sc1
  v_mfma_f32_16x16x32_bf16 v[140:143], a[236:239], a[84:87], v[140:143]
  ds_read_b64_tr_b16 a[214:215], v8 offset:4096
  v_mfma_f32_16x16x32_bf16 v[172:175], a[236:239], a[88:91], v[172:175]
  ds_read_b64_tr_b16 a[216:217], v9
  v_mfma_f32_16x16x32_bf16 v[204:207], a[236:239], a[92:95], v[204:207]
  ds_read_b64_tr_b16 a[218:219], v9 offset:4096
  v_mfma_f32_16x16x32_bf16 v[144:147], a[240:243], a[84:87], v[144:147]
  buffer_load_dwordx4 v[96:99], v13, s[16:19], s42 offen sc1
  v_mfma_f32_16x16x32_bf16 v[176:179], a[240:243], a[88:91], v[176:179]
  ds_read_b64_tr_b16 a[220:221], v10
  v_mfma_f32_16x16x32_bf16 v[208:211], a[240:243], a[92:95], v[208:211]
  ds_read_b64_tr_b16 a[222:223], v10 offset:4096
  v_mfma_f32_16x16x32_bf16 v[148:151], a[244:247], a[84:87], v[148:151]
  v_mfma_f32_16x16x32_bf16 v[180:183], a[244:247], a[88:91], v[180:183]
  buffer_load_dwordx4 v[100:103], v14, s[16:19], s42 offen sc1
  v_mfma_f32_16x16x32_bf16 v[212:215], a[244:247], a[92:95], v[212:215]
  v_mfma_f32_16x16x32_bf16 v[152:155], a[248:251], a[84:87], v[152:155]
  v_mfma_f32_16x16x32_bf16 v[184:187], a[248:251], a[88:91], v[184:187]
  v_mfma_f32_16x16x32_bf16 v[216:219], a[248:251], a[92:95], v[216:219]
  s_waitcnt lgkmcnt(0)
  v_mfma_f32_16x16x32_bf16 v[156:159], a[252:255], a[84:87], v[156:159]
  v_mfma_f32_16x16x32_bf16 v[188:191], a[252:255], a[88:91], v[188:191]
  v_mfma_f32_16x16x32_bf16 v[220:223], a[252:255], a[92:95], v[220:223]
  s_waitcnt vmcnt(16)
  s_barrier
  v_mfma_f32_16x16x32_bf16 v[136:139], a[200:203], v[32:35], v[136:139]
  ds_read_b64_tr_b16 a[232:233], v5 offset:8192
  v_mfma_f32_16x16x32_bf16 v[168:171], a[200:203], v[36:39], v[168:171]
  s_mov_b32 s39, 901120
  s_add_u32 m0, s37, 49152
  ds_read_b64_tr_b16 a[234:235], v5 offset:12288
  v_mfma_f32_16x16x32_bf16 v[200:203], a[200:203], v[40:43], v[200:203]
  ds_read_b64_tr_b16 a[236:237], v6 offset:8192
  buffer_load_dwordx4 v11, s[20:23], s39 offen lds
  v_mfma_f32_16x16x32_bf16 v[140:143], a[204:207], v[32:35], v[140:143]
  ds_read_b64_tr_b16 a[238:239], v6 offset:12288
  v_mfma_f32_16x16x32_bf16 v[172:175], a[204:207], v[36:39], v[172:175]
  ds_read_b64_tr_b16 a[240:241], v7 offset:8192
  v_mfma_f32_16x16x32_bf16 v[204:207], a[204:207], v[40:43], v[204:207]
  ds_read_b64_tr_b16 a[242:243], v7 offset:12288
  buffer_load_dwordx4 v12, s[20:23], s39 offen offset:1024 lds
  v_mfma_f32_16x16x32_bf16 v[144:147], a[208:211], v[32:35], v[144:147]
  ds_read_b64_tr_b16 a[244:245], v8 offset:8192
  v_mfma_f32_16x16x32_bf16 v[176:179], a[208:211], v[36:39], v[176:179]
  s_mov_b32 s43, 942080
  s_add_u32 m0, s37, 57344
  ds_read_b64_tr_b16 a[246:247], v8 offset:12288
  v_mfma_f32_16x16x32_bf16 v[208:211], a[208:211], v[40:43], v[208:211]
  ds_read_b64_tr_b16 a[248:249], v9 offset:8192
  buffer_load_dwordx4 v11, s[20:23], s43 offen lds
  v_mfma_f32_16x16x32_bf16 v[148:151], a[212:215], v[32:35], v[148:151]
  ds_read_b64_tr_b16 a[250:251], v9 offset:12288
  v_mfma_f32_16x16x32_bf16 v[180:183], a[212:215], v[36:39], v[180:183]
  ds_read_b64_tr_b16 a[252:253], v10 offset:8192
  v_mfma_f32_16x16x32_bf16 v[212:215], a[212:215], v[40:43], v[212:215]
  ds_read_b64_tr_b16 a[254:255], v10 offset:12288
  buffer_load_dwordx4 v12, s[20:23], s43 offen offset:1024 lds
  v_mfma_f32_16x16x32_bf16 v[152:155], a[216:219], v[32:35], v[152:155]
  v_mfma_f32_16x16x32_bf16 v[184:187], a[216:219], v[36:39], v[184:187]
  s_mov_b32 s41, 22528
  v_mfma_f32_16x16x32_bf16 v[216:219], a[216:219], v[40:43], v[216:219]
  buffer_load_dwordx4 v[104:107], v0, s[16:19], s41 offen sc1
  v_mfma_f32_16x16x32_bf16 v[156:159], a[220:223], v[32:35], v[156:159]
  v_mfma_f32_16x16x32_bf16 v[188:191], a[220:223], v[36:39], v[188:191]
  v_mfma_f32_16x16x32_bf16 v[220:223], a[220:223], v[40:43], v[220:223]
  s_waitcnt lgkmcnt(0)
  buffer_load_dwordx4 v[108:111], v13, s[16:19], s41 offen sc1
  v_mfma_f32_16x16x32_bf16 v[136:139], a[232:235], v[44:47], v[136:139]
  ds_read_b64_tr_b16 a[200:201], v5 offset:16384
  v_mfma_f32_16x16x32_bf16 v[168:171], a[232:235], v[48:51], v[168:171]
  ds_read_b64_tr_b16 a[202:203], v5 offset:20480
  v_mfma_f32_16x16x32_bf16 v[200:203], a[232:235], v[52:55], v[200:203]
  buffer_load_dwordx4 v[112:115], v14, s[16:19], s41 offen sc1
  v_mfma_f32_16x16x32_bf16 v[140:143], a[236:239], v[44:47], v[140:143]
  ds_read_b64_tr_b16 a[204:205], v6 offset:16384
  v_mfma_f32_16x16x32_bf16 v[172:175], a[236:239], v[48:51], v[172:175]
  s_mov_b32 s42, 23552
  ds_read_b64_tr_b16 a[206:207], v6 offset:20480
  v_mfma_f32_16x16x32_bf16 v[204:207], a[236:239], v[52:55], v[204:207]
  buffer_load_dwordx4 v[116:119], v0, s[16:19], s42 offen sc1
  v_mfma_f32_16x16x32_bf16 v[144:147], a[240:243], v[44:47], v[144:147]
  ds_read_b64_tr_b16 a[208:209], v7 offset:16384
  v_mfma_f32_16x16x32_bf16 v[176:179], a[240:243], v[48:51], v[176:179]
  ds_read_b64_tr_b16 a[210:211], v7 offset:20480
  v_mfma_f32_16x16x32_bf16 v[208:211], a[240:243], v[52:55], v[208:211]
  buffer_load_dwordx4 v[120:123], v13, s[16:19], s42 offen sc1
  v_mfma_f32_16x16x32_bf16 v[148:151], a[244:247], v[44:47], v[148:151]
  ds_read_b64_tr_b16 a[212:213], v8 offset:16384
  v_mfma_f32_16x16x32_bf16 v[180:183], a[244:247], v[48:51], v[180:183]
  ds_read_b64_tr_b16 a[214:215], v8 offset:20480
  v_mfma_f32_16x16x32_bf16 v[212:215], a[244:247], v[52:55], v[212:215]
  buffer_load_dwordx4 v[124:127], v14, s[16:19], s42 offen sc1
  v_mfma_f32_16x16x32_bf16 v[152:155], a[248:251], v[44:47], v[152:155]
  ds_read_b64_tr_b16 a[216:217], v9 offset:16384
  v_mfma_f32_16x16x32_bf16 v[184:187], a[248:251], v[48:51], v[184:187]
  ds_read_b64_tr_b16 a[218:219], v9 offset:20480
  v_mfma_f32_16x16x32_bf16 v[216:219], a[248:251], v[52:55], v[216:219]
  ds_read_b64_tr_b16 a[220:221], v10 offset:16384
  v_mfma_f32_16x16x32_bf16 v[156:159], a[252:255], v[44:47], v[156:159]
  ds_read_b64_tr_b16 a[222:223], v10 offset:20480
  v_mfma_f32_16x16x32_bf16 v[188:191], a[252:255], v[48:51], v[188:191]
  v_mfma_f32_16x16x32_bf16 v[220:223], a[252:255], v[52:55], v[220:223]
  s_waitcnt lgkmcnt(4)
  s_waitcnt vmcnt(16)
  s_barrier
  v_mfma_f32_16x16x32_bf16 v[136:139], a[200:203], v[56:59], v[136:139]
  ds_read_b64_tr_b16 a[232:233], v5 offset:24576
  v_mfma_f32_16x16x32_bf16 v[168:171], a[200:203], v[60:63], v[168:171]
  s_mov_b32 s39, 983040
  s_add_u32 m0, s37, 65536
  ds_read_b64_tr_b16 a[234:235], v5 offset:28672
  v_mfma_f32_16x16x32_bf16 v[200:203], a[200:203], v[64:67], v[200:203]
  ds_read_b64_tr_b16 a[236:237], v6 offset:24576
  buffer_load_dwordx4 v11, s[20:23], s39 offen lds
  v_mfma_f32_16x16x32_bf16 v[140:143], a[204:207], v[56:59], v[140:143]
  ds_read_b64_tr_b16 a[238:239], v6 offset:28672
  v_mfma_f32_16x16x32_bf16 v[172:175], a[204:207], v[60:63], v[172:175]
  ds_read_b64_tr_b16 a[240:241], v7 offset:24576
  v_mfma_f32_16x16x32_bf16 v[204:207], a[204:207], v[64:67], v[204:207]
  ds_read_b64_tr_b16 a[242:243], v7 offset:28672
  buffer_load_dwordx4 v12, s[20:23], s39 offen offset:1024 lds
  v_mfma_f32_16x16x32_bf16 v[144:147], a[208:211], v[56:59], v[144:147]
  ds_read_b64_tr_b16 a[244:245], v8 offset:24576
  v_mfma_f32_16x16x32_bf16 v[176:179], a[208:211], v[60:63], v[176:179]
  s_mov_b32 s43, 1024000
  s_add_u32 m0, s37, 73728
  ds_read_b64_tr_b16 a[246:247], v8 offset:28672
  v_mfma_f32_16x16x32_bf16 v[208:211], a[208:211], v[64:67], v[208:211]
  ds_read_b64_tr_b16 a[248:249], v9 offset:24576
  buffer_load_dwordx4 v11, s[20:23], s43 offen lds
  v_mfma_f32_16x16x32_bf16 v[148:151], a[212:215], v[56:59], v[148:151]
  ds_read_b64_tr_b16 a[250:251], v9 offset:28672
  v_mfma_f32_16x16x32_bf16 v[180:183], a[212:215], v[60:63], v[180:183]
  ds_read_b64_tr_b16 a[252:253], v10 offset:24576
  v_mfma_f32_16x16x32_bf16 v[212:215], a[212:215], v[64:67], v[212:215]
  s_waitcnt lgkmcnt(5)
  ds_read_b64_tr_b16 a[254:255], v10 offset:28672
  buffer_load_dwordx4 v12, s[20:23], s43 offen offset:1024 lds
  v_mfma_f32_16x16x32_bf16 v[152:155], a[216:219], v[56:59], v[152:155]
  v_mfma_f32_16x16x32_bf16 v[184:187], a[216:219], v[60:63], v[184:187]
  s_mov_b32 s41, 24576
  v_mfma_f32_16x16x32_bf16 v[216:219], a[216:219], v[64:67], v[216:219]
  buffer_load_dwordx4 a[0:3], v0, s[16:19], s41 offen sc1
  v_mfma_f32_16x16x32_bf16 v[156:159], a[220:223], v[56:59], v[156:159]
  v_mfma_f32_16x16x32_bf16 v[188:191], a[220:223], v[60:63], v[188:191]
  v_mfma_f32_16x16x32_bf16 v[220:223], a[220:223], v[64:67], v[220:223]
  buffer_load_dwordx4 a[4:7], v13, s[16:19], s41 offen sc1
  v_mfma_f32_16x16x32_bf16 v[136:139], a[232:235], v[68:71], v[136:139]
  ds_read_b64_tr_b16 a[200:201], v5 offset:32768
  v_mfma_f32_16x16x32_bf16 v[168:171], a[232:235], v[72:75], v[168:171]
  ds_read_b64_tr_b16 a[202:203], v5 offset:36864
  v_mfma_f32_16x16x32_bf16 v[200:203], a[232:235], v[76:79], v[200:203]
  buffer_load_dwordx4 a[8:11], v14, s[16:19], s41 offen sc1
  v_mfma_f32_16x16x32_bf16 v[140:143], a[236:239], v[68:71], v[140:143]
  ds_read_b64_tr_b16 a[204:205], v6 offset:32768
  v_mfma_f32_16x16x32_bf16 v[172:175], a[236:239], v[72:75], v[172:175]
  s_mov_b32 s42, 25600
  ds_read_b64_tr_b16 a[206:207], v6 offset:36864
  v_mfma_f32_16x16x32_bf16 v[204:207], a[236:239], v[76:79], v[204:207]
  buffer_load_dwordx4 a[12:15], v0, s[16:19], s42 offen sc1
  v_mfma_f32_16x16x32_bf16 v[144:147], a[240:243], v[68:71], v[144:147]
  ds_read_b64_tr_b16 a[208:209], v7 offset:32768
  v_mfma_f32_16x16x32_bf16 v[176:179], a[240:243], v[72:75], v[176:179]
  ds_read_b64_tr_b16 a[210:211], v7 offset:36864
  v_mfma_f32_16x16x32_bf16 v[208:211], a[240:243], v[76:79], v[208:211]
  s_waitcnt lgkmcnt(4)
  buffer_load_dwordx4 a[16:19], v13, s[16:19], s42 offen sc1
  v_mfma_f32_16x16x32_bf16 v[148:151], a[244:247], v[68:71], v[148:151]
  ds_read_b64_tr_b16 a[212:213], v8 offset:32768
  v_mfma_f32_16x16x32_bf16 v[180:183], a[244:247], v[72:75], v[180:183]
  ds_read_b64_tr_b16 a[214:215], v8 offset:36864
  v_mfma_f32_16x16x32_bf16 v[212:215], a[244:247], v[76:79], v[212:215]
  buffer_load_dwordx4 a[20:23], v14, s[16:19], s42 offen sc1
  v_mfma_f32_16x16x32_bf16 v[152:155], a[248:251], v[68:71], v[152:155]
  ds_read_b64_tr_b16 a[216:217], v9 offset:32768
  v_mfma_f32_16x16x32_bf16 v[184:187], a[248:251], v[72:75], v[184:187]
  ds_read_b64_tr_b16 a[218:219], v9 offset:36864
  v_mfma_f32_16x16x32_bf16 v[216:219], a[248:251], v[76:79], v[216:219]
  ds_read_b64_tr_b16 a[220:221], v10 offset:32768
  v_mfma_f32_16x16x32_bf16 v[156:159], a[252:255], v[68:71], v[156:159]
  ds_read_b64_tr_b16 a[222:223], v10 offset:36864
  v_mfma_f32_16x16x32_bf16 v[188:191], a[252:255], v[72:75], v[188:191]
  v_mfma_f32_16x16x32_bf16 v[220:223], a[252:255], v[76:79], v[220:223]
  s_waitcnt vmcnt(16)
  s_barrier
  v_mfma_f32_16x16x32_bf16 v[136:139], a[200:203], v[80:83], v[136:139]
  ds_read_b64_tr_b16 a[232:233], v5 offset:40960
  v_mfma_f32_16x16x32_bf16 v[168:171], a[200:203], v[84:87], v[168:171]
  s_mov_b32 s39, 1064960
  s_add_u32 m0, s37, 81920
  ds_read_b64_tr_b16 a[234:235], v5 offset:45056
  v_mfma_f32_16x16x32_bf16 v[200:203], a[200:203], v[88:91], v[200:203]
  s_waitcnt lgkmcnt(3)
  ds_read_b64_tr_b16 a[236:237], v6 offset:40960
  buffer_load_dwordx4 v11, s[20:23], s39 offen lds
  v_mfma_f32_16x16x32_bf16 v[140:143], a[204:207], v[80:83], v[140:143]
  ds_read_b64_tr_b16 a[238:239], v6 offset:45056
  v_mfma_f32_16x16x32_bf16 v[172:175], a[204:207], v[84:87], v[172:175]
  ds_read_b64_tr_b16 a[240:241], v7 offset:40960
  v_mfma_f32_16x16x32_bf16 v[204:207], a[204:207], v[88:91], v[204:207]
  ds_read_b64_tr_b16 a[242:243], v7 offset:45056
  buffer_load_dwordx4 v12, s[20:23], s39 offen offset:1024 lds
  v_mfma_f32_16x16x32_bf16 v[144:147], a[208:211], v[80:83], v[144:147]
  ds_read_b64_tr_b16 a[244:245], v8 offset:40960
  v_mfma_f32_16x16x32_bf16 v[176:179], a[208:211], v[84:87], v[176:179]
  s_mov_b32 s43, 1105920
  s_add_u32 m0, s37, 90112
  ds_read_b64_tr_b16 a[246:247], v8 offset:45056
  v_mfma_f32_16x16x32_bf16 v[208:211], a[208:211], v[88:91], v[208:211]
  ds_read_b64_tr_b16 a[248:249], v9 offset:40960
  buffer_load_dwordx4 v11, s[20:23], s43 offen lds
  v_mfma_f32_16x16x32_bf16 v[148:151], a[212:215], v[80:83], v[148:151]
  ds_read_b64_tr_b16 a[250:251], v9 offset:45056
  v_mfma_f32_16x16x32_bf16 v[180:183], a[212:215], v[84:87], v[180:183]
  ds_read_b64_tr_b16 a[252:253], v10 offset:40960
  v_mfma_f32_16x16x32_bf16 v[212:215], a[212:215], v[88:91], v[212:215]
  ds_read_b64_tr_b16 a[254:255], v10 offset:45056
  buffer_load_dwordx4 v12, s[20:23], s43 offen offset:1024 lds
  v_mfma_f32_16x16x32_bf16 v[152:155], a[216:219], v[80:83], v[152:155]
  v_mfma_f32_16x16x32_bf16 v[184:187], a[216:219], v[84:87], v[184:187]
  s_mov_b32 s41, 26624
  v_mfma_f32_16x16x32_bf16 v[216:219], a[216:219], v[88:91], v[216:219]
  s_waitcnt lgkmcnt(3)
  buffer_load_dwordx4 a[24:27], v0, s[16:19], s41 offen sc1
  v_mfma_f32_16x16x32_bf16 v[156:159], a[220:223], v[80:83], v[156:159]
  v_mfma_f32_16x16x32_bf16 v[188:191], a[220:223], v[84:87], v[188:191]
  v_mfma_f32_16x16x32_bf16 v[220:223], a[220:223], v[88:91], v[220:223]
  buffer_load_dwordx4 a[28:31], v13, s[16:19], s41 offen sc1
  v_mfma_f32_16x16x32_bf16 v[136:139], a[232:235], v[92:95], v[136:139]
  ds_read_b64_tr_b16 a[200:201], v5 offset:49152
  v_mfma_f32_16x16x32_bf16 v[168:171], a[232:235], v[96:99], v[168:171]
  ds_read_b64_tr_b16 a[202:203], v5 offset:53248
  v_mfma_f32_16x16x32_bf16 v[200:203], a[232:235], v[100:103], v[200:203]
  buffer_load_dwordx4 a[32:35], v14, s[16:19], s41 offen sc1
  v_mfma_f32_16x16x32_bf16 v[140:143], a[236:239], v[92:95], v[140:143]
  ds_read_b64_tr_b16 a[204:205], v6 offset:49152
  v_mfma_f32_16x16x32_bf16 v[172:175], a[236:239], v[96:99], v[172:175]
  s_mov_b32 s42, 27648
  ds_read_b64_tr_b16 a[206:207], v6 offset:53248
  v_mfma_f32_16x16x32_bf16 v[204:207], a[236:239], v[100:103], v[204:207]
  buffer_load_dwordx4 a[36:39], v0, s[16:19], s42 offen sc1
  v_mfma_f32_16x16x32_bf16 v[144:147], a[240:243], v[92:95], v[144:147]
  ds_read_b64_tr_b16 a[208:209], v7 offset:49152
  v_mfma_f32_16x16x32_bf16 v[176:179], a[240:243], v[96:99], v[176:179]
  ds_read_b64_tr_b16 a[210:211], v7 offset:53248
  v_mfma_f32_16x16x32_bf16 v[208:211], a[240:243], v[100:103], v[208:211]
  buffer_load_dwordx4 a[40:43], v13, s[16:19], s42 offen sc1
  v_mfma_f32_16x16x32_bf16 v[148:151], a[244:247], v[92:95], v[148:151]
  ds_read_b64_tr_b16 a[212:213], v8 offset:49152
  v_mfma_f32_16x16x32_bf16 v[180:183], a[244:247], v[96:99], v[180:183]
  ds_read_b64_tr_b16 a[214:215], v8 offset:53248
  v_mfma_f32_16x16x32_bf16 v[212:215], a[244:247], v[100:103], v[212:215]
  s_waitcnt lgkmcnt(4)
  buffer_load_dwordx4 a[44:47], v14, s[16:19], s42 offen sc1
  v_mfma_f32_16x16x32_bf16 v[152:155], a[248:251], v[92:95], v[152:155]
  ds_read_b64_tr_b16 a[216:217], v9 offset:49152
  v_mfma_f32_16x16x32_bf16 v[184:187], a[248:251], v[96:99], v[184:187]
  ds_read_b64_tr_b16 a[218:219], v9 offset:53248
  v_mfma_f32_16x16x32_bf16 v[216:219], a[248:251], v[100:103], v[216:219]
  ds_read_b64_tr_b16 a[220:221], v10 offset:49152
  v_mfma_f32_16x16x32_bf16 v[156:159], a[252:255], v[92:95], v[156:159]
  ds_read_b64_tr_b16 a[222:223], v10 offset:53248
  v_mfma_f32_16x16x32_bf16 v[188:191], a[252:255], v[96:99], v[188:191]
  v_mfma_f32_16x16x32_bf16 v[220:223], a[252:255], v[100:103], v[220:223]
  s_waitcnt vmcnt(16)
  s_barrier
  v_mfma_f32_16x16x32_bf16 v[136:139], a[200:203], v[104:107], v[136:139]
  ds_read_b64_tr_b16 a[232:233], v5 offset:57344
  v_mfma_f32_16x16x32_bf16 v[168:171], a[200:203], v[108:111], v[168:171]
  s_mov_b32 s39, 1146880
  s_add_u32 m0, s37, 98304
  ds_read_b64_tr_b16 a[234:235], v5 offset:61440
  v_mfma_f32_16x16x32_bf16 v[200:203], a[200:203], v[112:115], v[200:203]
  ds_read_b64_tr_b16 a[236:237], v6 offset:57344
  buffer_load_dwordx4 v11, s[20:23], s39 offen lds
  v_mfma_f32_16x16x32_bf16 v[140:143], a[204:207], v[104:107], v[140:143]
  ds_read_b64_tr_b16 a[238:239], v6 offset:61440
  v_mfma_f32_16x16x32_bf16 v[172:175], a[204:207], v[108:111], v[172:175]
  ds_read_b64_tr_b16 a[240:241], v7 offset:57344
  v_mfma_f32_16x16x32_bf16 v[204:207], a[204:207], v[112:115], v[204:207]
  s_waitcnt lgkmcnt(5)
  ds_read_b64_tr_b16 a[242:243], v7 offset:61440
  buffer_load_dwordx4 v12, s[20:23], s39 offen offset:1024 lds
  v_mfma_f32_16x16x32_bf16 v[144:147], a[208:211], v[104:107], v[144:147]
  ds_read_b64_tr_b16 a[244:245], v8 offset:57344
  v_mfma_f32_16x16x32_bf16 v[176:179], a[208:211], v[108:111], v[176:179]
  s_mov_b32 s43, 1187840
  s_add_u32 m0, s37, 106496
  ds_read_b64_tr_b16 a[246:247], v8 offset:61440
  v_mfma_f32_16x16x32_bf16 v[208:211], a[208:211], v[112:115], v[208:211]
  ds_read_b64_tr_b16 a[248:249], v9 offset:57344
  buffer_load_dwordx4 v11, s[20:23], s43 offen lds
  v_mfma_f32_16x16x32_bf16 v[148:151], a[212:215], v[104:107], v[148:151]
  ds_read_b64_tr_b16 a[250:251], v9 offset:61440
  v_mfma_f32_16x16x32_bf16 v[180:183], a[212:215], v[108:111], v[180:183]
  ds_read_b64_tr_b16 a[252:253], v10 offset:57344
  v_mfma_f32_16x16x32_bf16 v[212:215], a[212:215], v[112:115], v[212:215]
  ds_read_b64_tr_b16 a[254:255], v10 offset:61440
  buffer_load_dwordx4 v12, s[20:23], s43 offen offset:1024 lds
  v_mfma_f32_16x16x32_bf16 v[152:155], a[216:219], v[104:107], v[152:155]
  v_mfma_f32_16x16x32_bf16 v[184:187], a[216:219], v[108:111], v[184:187]
  s_mov_b32 s41, 28672
  v_mfma_f32_16x16x32_bf16 v[216:219], a[216:219], v[112:115], v[216:219]
  buffer_load_dwordx4 a[48:51], v0, s[16:19], s41 offen sc1
  v_mfma_f32_16x16x32_bf16 v[156:159], a[220:223], v[104:107], v[156:159]
  v_mfma_f32_16x16x32_bf16 v[188:191], a[220:223], v[108:111], v[188:191]
  v_mfma_f32_16x16x32_bf16 v[220:223], a[220:223], v[112:115], v[220:223]
  s_waitcnt lgkmcnt(0)
  buffer_load_dwordx4 a[52:55], v13, s[16:19], s41 offen sc1
  v_mfma_f32_16x16x32_bf16 v[136:139], a[232:235], v[116:119], v[136:139]
  ds_read_b64_tr_b16 a[200:201], v226
  v_mfma_f32_16x16x32_bf16 v[168:171], a[232:235], v[120:123], v[168:171]
  ds_read_b64_tr_b16 a[202:203], v226 offset:4096
  v_mfma_f32_16x16x32_bf16 v[200:203], a[232:235], v[124:127], v[200:203]
  buffer_load_dwordx4 a[56:59], v14, s[16:19], s41 offen sc1
  v_mfma_f32_16x16x32_bf16 v[140:143], a[236:239], v[116:119], v[140:143]
  ds_read_b64_tr_b16 a[204:205], v227
  v_mfma_f32_16x16x32_bf16 v[172:175], a[236:239], v[120:123], v[172:175]
  s_mov_b32 s42, 29696
  ds_read_b64_tr_b16 a[206:207], v227 offset:4096
  v_mfma_f32_16x16x32_bf16 v[204:207], a[236:239], v[124:127], v[204:207]
  buffer_load_dwordx4 a[60:63], v0, s[16:19], s42 offen sc1
  v_mfma_f32_16x16x32_bf16 v[144:147], a[240:243], v[116:119], v[144:147]
  ds_read_b64_tr_b16 a[208:209], v228
  v_mfma_f32_16x16x32_bf16 v[176:179], a[240:243], v[120:123], v[176:179]
  ds_read_b64_tr_b16 a[210:211], v228 offset:4096
  v_mfma_f32_16x16x32_bf16 v[208:211], a[240:243], v[124:127], v[208:211]
  buffer_load_dwordx4 a[64:67], v13, s[16:19], s42 offen sc1
  v_mfma_f32_16x16x32_bf16 v[148:151], a[244:247], v[116:119], v[148:151]
  ds_read_b64_tr_b16 a[212:213], v229
  v_mfma_f32_16x16x32_bf16 v[180:183], a[244:247], v[120:123], v[180:183]
  ds_read_b64_tr_b16 a[214:215], v229 offset:4096
  v_mfma_f32_16x16x32_bf16 v[212:215], a[244:247], v[124:127], v[212:215]
  buffer_load_dwordx4 a[68:71], v14, s[16:19], s42 offen sc1
  v_mfma_f32_16x16x32_bf16 v[152:155], a[248:251], v[116:119], v[152:155]
  ds_read_b64_tr_b16 a[216:217], v230
  v_mfma_f32_16x16x32_bf16 v[184:187], a[248:251], v[120:123], v[184:187]
  ds_read_b64_tr_b16 a[218:219], v230 offset:4096
  v_mfma_f32_16x16x32_bf16 v[216:219], a[248:251], v[124:127], v[216:219]
  ds_read_b64_tr_b16 a[220:221], v231
  v_mfma_f32_16x16x32_bf16 v[156:159], a[252:255], v[116:119], v[156:159]
  ds_read_b64_tr_b16 a[222:223], v231 offset:4096
  v_mfma_f32_16x16x32_bf16 v[188:191], a[252:255], v[120:123], v[188:191]
  v_mfma_f32_16x16x32_bf16 v[220:223], a[252:255], v[124:127], v[220:223]
  s_waitcnt lgkmcnt(4)
  s_waitcnt vmcnt(16)
  s_barrier
  v_mfma_f32_16x16x32_bf16 v[136:139], a[200:203], a[0:3], v[136:139]
  ds_read_b64_tr_b16 a[232:233], v226 offset:8192
  v_mfma_f32_16x16x32_bf16 v[168:171], a[200:203], a[4:7], v[168:171]
  s_mov_b32 s39, 1228800
  s_add_u32 m0, s37, 114688
  ds_read_b64_tr_b16 a[234:235], v226 offset:12288
  v_mfma_f32_16x16x32_bf16 v[200:203], a[200:203], a[8:11], v[200:203]
  ds_read_b64_tr_b16 a[236:237], v227 offset:8192
  buffer_load_dwordx4 v11, s[20:23], s39 offen lds
  v_mfma_f32_16x16x32_bf16 v[140:143], a[204:207], a[0:3], v[140:143]
  ds_read_b64_tr_b16 a[238:239], v227 offset:12288
  v_mfma_f32_16x16x32_bf16 v[172:175], a[204:207], a[4:7], v[172:175]
  ds_read_b64_tr_b16 a[240:241], v228 offset:8192
  v_mfma_f32_16x16x32_bf16 v[204:207], a[204:207], a[8:11], v[204:207]
  ds_read_b64_tr_b16 a[242:243], v228 offset:12288
  buffer_load_dwordx4 v12, s[20:23], s39 offen offset:1024 lds
  v_mfma_f32_16x16x32_bf16 v[144:147], a[208:211], a[0:3], v[144:147]
  ds_read_b64_tr_b16 a[244:245], v229 offset:8192
  v_mfma_f32_16x16x32_bf16 v[176:179], a[208:211], a[4:7], v[176:179]
  s_mov_b32 s43, 1269760
  s_add_u32 m0, s37, 122880
  ds_read_b64_tr_b16 a[246:247], v229 offset:12288
  v_mfma_f32_16x16x32_bf16 v[208:211], a[208:211], a[8:11], v[208:211]
  ds_read_b64_tr_b16 a[248:249], v230 offset:8192
  buffer_load_dwordx4 v11, s[20:23], s43 offen lds
  v_mfma_f32_16x16x32_bf16 v[148:151], a[212:215], a[0:3], v[148:151]
  ds_read_b64_tr_b16 a[250:251], v230 offset:12288
  v_mfma_f32_16x16x32_bf16 v[180:183], a[212:215], a[4:7], v[180:183]
  ds_read_b64_tr_b16 a[252:253], v231 offset:8192
  v_mfma_f32_16x16x32_bf16 v[212:215], a[212:215], a[8:11], v[212:215]
  s_waitcnt lgkmcnt(5)
  ds_read_b64_tr_b16 a[254:255], v231 offset:12288
  buffer_load_dwordx4 v12, s[20:23], s43 offen offset:1024 lds
  v_mfma_f32_16x16x32_bf16 v[152:155], a[216:219], a[0:3], v[152:155]
  v_mfma_f32_16x16x32_bf16 v[184:187], a[216:219], a[4:7], v[184:187]
  s_mov_b32 s41, 30720
  v_mfma_f32_16x16x32_bf16 v[216:219], a[216:219], a[8:11], v[216:219]
  buffer_load_dwordx4 a[72:75], v0, s[16:19], s41 offen sc1
  v_mfma_f32_16x16x32_bf16 v[156:159], a[220:223], a[0:3], v[156:159]
  v_mfma_f32_16x16x32_bf16 v[188:191], a[220:223], a[4:7], v[188:191]
  v_mfma_f32_16x16x32_bf16 v[220:223], a[220:223], a[8:11], v[220:223]
  buffer_load_dwordx4 a[76:79], v13, s[16:19], s41 offen sc1
  v_mfma_f32_16x16x32_bf16 v[136:139], a[232:235], a[12:15], v[136:139]
  ds_read_b64_tr_b16 a[200:201], v226 offset:16384
  v_mfma_f32_16x16x32_bf16 v[168:171], a[232:235], a[16:19], v[168:171]
  ds_read_b64_tr_b16 a[202:203], v226 offset:20480
  v_mfma_f32_16x16x32_bf16 v[200:203], a[232:235], a[20:23], v[200:203]
  buffer_load_dwordx4 a[80:83], v14, s[16:19], s41 offen sc1
  v_mfma_f32_16x16x32_bf16 v[140:143], a[236:239], a[12:15], v[140:143]
  ds_read_b64_tr_b16 a[204:205], v227 offset:16384
  v_mfma_f32_16x16x32_bf16 v[172:175], a[236:239], a[16:19], v[172:175]
  s_mov_b32 s42, 31744
  ds_read_b64_tr_b16 a[206:207], v227 offset:20480
  v_mfma_f32_16x16x32_bf16 v[204:207], a[236:239], a[20:23], v[204:207]
  buffer_load_dwordx4 a[84:87], v0, s[16:19], s42 offen sc1
  v_mfma_f32_16x16x32_bf16 v[144:147], a[240:243], a[12:15], v[144:147]
  ds_read_b64_tr_b16 a[208:209], v228 offset:16384
  v_mfma_f32_16x16x32_bf16 v[176:179], a[240:243], a[16:19], v[176:179]
  ds_read_b64_tr_b16 a[210:211], v228 offset:20480
  v_mfma_f32_16x16x32_bf16 v[208:211], a[240:243], a[20:23], v[208:211]
  s_waitcnt lgkmcnt(4)
  buffer_load_dwordx4 a[88:91], v13, s[16:19], s42 offen sc1
  v_mfma_f32_16x16x32_bf16 v[148:151], a[244:247], a[12:15], v[148:151]
  ds_read_b64_tr_b16 a[212:213], v229 offset:16384
  v_mfma_f32_16x16x32_bf16 v[180:183], a[244:247], a[16:19], v[180:183]
  ds_read_b64_tr_b16 a[214:215], v229 offset:20480
  v_mfma_f32_16x16x32_bf16 v[212:215], a[244:247], a[20:23], v[212:215]
  buffer_load_dwordx4 a[92:95], v14, s[16:19], s42 offen sc1
  v_mfma_f32_16x16x32_bf16 v[152:155], a[248:251], a[12:15], v[152:155]
  ds_read_b64_tr_b16 a[216:217], v230 offset:16384
  v_mfma_f32_16x16x32_bf16 v[184:187], a[248:251], a[16:19], v[184:187]
  ds_read_b64_tr_b16 a[218:219], v230 offset:20480
  v_mfma_f32_16x16x32_bf16 v[216:219], a[248:251], a[20:23], v[216:219]
  ds_read_b64_tr_b16 a[220:221], v231 offset:16384
  v_mfma_f32_16x16x32_bf16 v[156:159], a[252:255], a[12:15], v[156:159]
  ds_read_b64_tr_b16 a[222:223], v231 offset:20480
  v_mfma_f32_16x16x32_bf16 v[188:191], a[252:255], a[16:19], v[188:191]
  v_mfma_f32_16x16x32_bf16 v[220:223], a[252:255], a[20:23], v[220:223]
  s_waitcnt vmcnt(16)
  s_barrier
  v_mfma_f32_16x16x32_bf16 v[136:139], a[200:203], a[24:27], v[136:139]
  ds_read_b64_tr_b16 a[232:233], v226 offset:24576
  v_mfma_f32_16x16x32_bf16 v[168:171], a[200:203], a[28:31], v[168:171]
  ds_read_b64_tr_b16 a[234:235], v226 offset:28672
  v_mfma_f32_16x16x32_bf16 v[200:203], a[200:203], a[32:35], v[200:203]
  s_waitcnt lgkmcnt(3)
  ds_read_b64_tr_b16 a[236:237], v227 offset:24576
  v_mfma_f32_16x16x32_bf16 v[140:143], a[204:207], a[24:27], v[140:143]
  ds_read_b64_tr_b16 a[238:239], v227 offset:28672
  v_mfma_f32_16x16x32_bf16 v[172:175], a[204:207], a[28:31], v[172:175]
  ds_read_b64_tr_b16 a[240:241], v228 offset:24576
  v_mfma_f32_16x16x32_bf16 v[204:207], a[204:207], a[32:35], v[204:207]
  ds_read_b64_tr_b16 a[242:243], v228 offset:28672
  v_mfma_f32_16x16x32_bf16 v[144:147], a[208:211], a[24:27], v[144:147]
  ds_read_b64_tr_b16 a[244:245], v229 offset:24576
  v_mfma_f32_16x16x32_bf16 v[176:179], a[208:211], a[28:31], v[176:179]
  ds_read_b64_tr_b16 a[246:247], v229 offset:28672
  v_mfma_f32_16x16x32_bf16 v[208:211], a[208:211], a[32:35], v[208:211]
  ds_read_b64_tr_b16 a[248:249], v230 offset:24576
  v_mfma_f32_16x16x32_bf16 v[148:151], a[212:215], a[24:27], v[148:151]
  ds_read_b64_tr_b16 a[250:251], v230 offset:28672
  v_mfma_f32_16x16x32_bf16 v[180:183], a[212:215], a[28:31], v[180:183]
  ds_read_b64_tr_b16 a[252:253], v231 offset:24576
  v_mfma_f32_16x16x32_bf16 v[212:215], a[212:215], a[32:35], v[212:215]
  ds_read_b64_tr_b16 a[254:255], v231 offset:28672
  v_mfma_f32_16x16x32_bf16 v[152:155], a[216:219], a[24:27], v[152:155]
  v_mfma_f32_16x16x32_bf16 v[184:187], a[216:219], a[28:31], v[184:187]
  v_mfma_f32_16x16x32_bf16 v[216:219], a[216:219], a[32:35], v[216:219]
  s_waitcnt lgkmcnt(3)
  v_mfma_f32_16x16x32_bf16 v[156:159], a[220:223], a[24:27], v[156:159]
  v_mfma_f32_16x16x32_bf16 v[188:191], a[220:223], a[28:31], v[188:191]
  v_mfma_f32_16x16x32_bf16 v[220:223], a[220:223], a[32:35], v[220:223]
  v_mfma_f32_16x16x32_bf16 v[136:139], a[232:235], a[36:39], v[136:139]
  ds_read_b64_tr_b16 a[200:201], v226 offset:32768
  v_mfma_f32_16x16x32_bf16 v[168:171], a[232:235], a[40:43], v[168:171]
  ds_read_b64_tr_b16 a[202:203], v226 offset:36864
  v_mfma_f32_16x16x32_bf16 v[200:203], a[232:235], a[44:47], v[200:203]
  ds_read_b64_tr_b16 a[204:205], v227 offset:32768
  v_mfma_f32_16x16x32_bf16 v[140:143], a[236:239], a[36:39], v[140:143]
  ds_read_b64_tr_b16 a[206:207], v227 offset:36864
  v_mfma_f32_16x16x32_bf16 v[172:175], a[236:239], a[40:43], v[172:175]
  ds_read_b64_tr_b16 a[208:209], v228 offset:32768
  v_mfma_f32_16x16x32_bf16 v[204:207], a[236:239], a[44:47], v[204:207]
  ds_read_b64_tr_b16 a[210:211], v228 offset:36864
  v_mfma_f32_16x16x32_bf16 v[144:147], a[240:243], a[36:39], v[144:147]
  ds_read_b64_tr_b16 a[212:213], v229 offset:32768
  v_mfma_f32_16x16x32_bf16 v[176:179], a[240:243], a[40:43], v[176:179]
  ds_read_b64_tr_b16 a[214:215], v229 offset:36864
  v_mfma_f32_16x16x32_bf16 v[208:211], a[240:243], a[44:47], v[208:211]
  ds_read_b64_tr_b16 a[216:217], v230 offset:32768
  v_mfma_f32_16x16x32_bf16 v[148:151], a[244:247], a[36:39], v[148:151]
  ds_read_b64_tr_b16 a[218:219], v230 offset:36864
  v_mfma_f32_16x16x32_bf16 v[180:183], a[244:247], a[40:43], v[180:183]
  ds_read_b64_tr_b16 a[220:221], v231 offset:32768
  v_mfma_f32_16x16x32_bf16 v[212:215], a[244:247], a[44:47], v[212:215]
  s_waitcnt lgkmcnt(5)
  ds_read_b64_tr_b16 a[222:223], v231 offset:36864
  v_mfma_f32_16x16x32_bf16 v[152:155], a[248:251], a[36:39], v[152:155]
  v_mfma_f32_16x16x32_bf16 v[184:187], a[248:251], a[40:43], v[184:187]
  v_mfma_f32_16x16x32_bf16 v[216:219], a[248:251], a[44:47], v[216:219]
  v_mfma_f32_16x16x32_bf16 v[156:159], a[252:255], a[36:39], v[156:159]
  v_mfma_f32_16x16x32_bf16 v[188:191], a[252:255], a[40:43], v[188:191]
  v_mfma_f32_16x16x32_bf16 v[220:223], a[252:255], a[44:47], v[220:223]
  s_waitcnt vmcnt(6)
  s_barrier
  v_mfma_f32_16x16x32_bf16 v[136:139], a[200:203], a[48:51], v[136:139]
  ds_read_b64_tr_b16 a[232:233], v226 offset:40960
  v_mfma_f32_16x16x32_bf16 v[168:171], a[200:203], a[52:55], v[168:171]
  ds_read_b64_tr_b16 a[234:235], v226 offset:45056
  v_mfma_f32_16x16x32_bf16 v[200:203], a[200:203], a[56:59], v[200:203]
  ds_read_b64_tr_b16 a[236:237], v227 offset:40960
  v_mfma_f32_16x16x32_bf16 v[140:143], a[204:207], a[48:51], v[140:143]
  ds_read_b64_tr_b16 a[238:239], v227 offset:45056
  v_mfma_f32_16x16x32_bf16 v[172:175], a[204:207], a[52:55], v[172:175]
  ds_read_b64_tr_b16 a[240:241], v228 offset:40960
  v_mfma_f32_16x16x32_bf16 v[204:207], a[204:207], a[56:59], v[204:207]
  ds_read_b64_tr_b16 a[242:243], v228 offset:45056
  v_mfma_f32_16x16x32_bf16 v[144:147], a[208:211], a[48:51], v[144:147]
  ds_read_b64_tr_b16 a[244:245], v229 offset:40960
  v_mfma_f32_16x16x32_bf16 v[176:179], a[208:211], a[52:55], v[176:179]
  ds_read_b64_tr_b16 a[246:247], v229 offset:45056
  v_mfma_f32_16x16x32_bf16 v[208:211], a[208:211], a[56:59], v[208:211]
  s_waitcnt lgkmcnt(5)
  ds_read_b64_tr_b16 a[248:249], v230 offset:40960
  v_mfma_f32_16x16x32_bf16 v[148:151], a[212:215], a[48:51], v[148:151]
  ds_read_b64_tr_b16 a[250:251], v230 offset:45056
  v_mfma_f32_16x16x32_bf16 v[180:183], a[212:215], a[52:55], v[180:183]
  ds_read_b64_tr_b16 a[252:253], v231 offset:40960
  v_mfma_f32_16x16x32_bf16 v[212:215], a[212:215], a[56:59], v[212:215]
  ds_read_b64_tr_b16 a[254:255], v231 offset:45056
  v_mfma_f32_16x16x32_bf16 v[152:155], a[216:219], a[48:51], v[152:155]
  v_mfma_f32_16x16x32_bf16 v[184:187], a[216:219], a[52:55], v[184:187]
  v_mfma_f32_16x16x32_bf16 v[216:219], a[216:219], a[56:59], v[216:219]
  v_mfma_f32_16x16x32_bf16 v[156:159], a[220:223], a[48:51], v[156:159]
  v_mfma_f32_16x16x32_bf16 v[188:191], a[220:223], a[52:55], v[188:191]
  v_mfma_f32_16x16x32_bf16 v[220:223], a[220:223], a[56:59], v[220:223]
  v_mfma_f32_16x16x32_bf16 v[136:139], a[232:235], a[60:63], v[136:139]
  ds_read_b64_tr_b16 a[200:201], v226 offset:49152
  v_mfma_f32_16x16x32_bf16 v[168:171], a[232:235], a[64:67], v[168:171]
  ds_read_b64_tr_b16 a[202:203], v226 offset:53248
  v_mfma_f32_16x16x32_bf16 v[200:203], a[232:235], a[68:71], v[200:203]
  s_waitcnt lgkmcnt(2)
  ds_read_b64_tr_b16 a[204:205], v227 offset:49152
  v_mfma_f32_16x16x32_bf16 v[140:143], a[236:239], a[60:63], v[140:143]
  ds_read_b64_tr_b16 a[206:207], v227 offset:53248
  v_mfma_f32_16x16x32_bf16 v[172:175], a[236:239], a[64:67], v[172:175]
  ds_read_b64_tr_b16 a[208:209], v228 offset:49152
  v_mfma_f32_16x16x32_bf16 v[204:207], a[236:239], a[68:71], v[204:207]
  ds_read_b64_tr_b16 a[210:211], v228 offset:53248
  v_mfma_f32_16x16x32_bf16 v[144:147], a[240:243], a[60:63], v[144:147]
  ds_read_b64_tr_b16 a[212:213], v229 offset:49152
  v_mfma_f32_16x16x32_bf16 v[176:179], a[240:243], a[64:67], v[176:179]
  ds_read_b64_tr_b16 a[214:215], v229 offset:53248
  v_mfma_f32_16x16x32_bf16 v[208:211], a[240:243], a[68:71], v[208:211]
  ds_read_b64_tr_b16 a[216:217], v230 offset:49152
  v_mfma_f32_16x16x32_bf16 v[148:151], a[244:247], a[60:63], v[148:151]
  ds_read_b64_tr_b16 a[218:219], v230 offset:53248
  v_mfma_f32_16x16x32_bf16 v[180:183], a[244:247], a[64:67], v[180:183]
  ds_read_b64_tr_b16 a[220:221], v231 offset:49152
  v_mfma_f32_16x16x32_bf16 v[212:215], a[244:247], a[68:71], v[212:215]
  ds_read_b64_tr_b16 a[222:223], v231 offset:53248
  v_mfma_f32_16x16x32_bf16 v[152:155], a[248:251], a[60:63], v[152:155]
  v_mfma_f32_16x16x32_bf16 v[184:187], a[248:251], a[64:67], v[184:187]
  v_mfma_f32_16x16x32_bf16 v[216:219], a[248:251], a[68:71], v[216:219]
  v_mfma_f32_16x16x32_bf16 v[156:159], a[252:255], a[60:63], v[156:159]
  v_mfma_f32_16x16x32_bf16 v[188:191], a[252:255], a[64:67], v[188:191]
  v_mfma_f32_16x16x32_bf16 v[220:223], a[252:255], a[68:71], v[220:223]
  s_waitcnt lgkmcnt(0)
  s_waitcnt vmcnt(0)
  v_mfma_f32_16x16x32_bf16 v[136:139], a[200:203], a[72:75], v[136:139]
  ds_read_b64_tr_b16 a[232:233], v226 offset:57344
  v_mfma_f32_16x16x32_bf16 v[168:171], a[200:203], a[76:79], v[168:171]
  ds_read_b64_tr_b16 a[234:235], v226 offset:61440
  v_mfma_f32_16x16x32_bf16 v[200:203], a[200:203], a[80:83], v[200:203]
  ds_read_b64_tr_b16 a[236:237], v227 offset:57344
  v_mfma_f32_16x16x32_bf16 v[140:143], a[204:207], a[72:75], v[140:143]
  ds_read_b64_tr_b16 a[238:239], v227 offset:61440
  v_mfma_f32_16x16x32_bf16 v[172:175], a[204:207], a[76:79], v[172:175]
  ds_read_b64_tr_b16 a[240:241], v228 offset:57344
  v_mfma_f32_16x16x32_bf16 v[204:207], a[204:207], a[80:83], v[204:207]
  ds_read_b64_tr_b16 a[242:243], v228 offset:61440
  v_mfma_f32_16x16x32_bf16 v[144:147], a[208:211], a[72:75], v[144:147]
  ds_read_b64_tr_b16 a[244:245], v229 offset:57344
  v_mfma_f32_16x16x32_bf16 v[176:179], a[208:211], a[76:79], v[176:179]
  ds_read_b64_tr_b16 a[246:247], v229 offset:61440
  v_mfma_f32_16x16x32_bf16 v[208:211], a[208:211], a[80:83], v[208:211]
  ds_read_b64_tr_b16 a[248:249], v230 offset:57344
  v_mfma_f32_16x16x32_bf16 v[148:151], a[212:215], a[72:75], v[148:151]
  ds_read_b64_tr_b16 a[250:251], v230 offset:61440
  v_mfma_f32_16x16x32_bf16 v[180:183], a[212:215], a[76:79], v[180:183]
  ds_read_b64_tr_b16 a[252:253], v231 offset:57344
  v_mfma_f32_16x16x32_bf16 v[212:215], a[212:215], a[80:83], v[212:215]
  ds_read_b64_tr_b16 a[254:255], v231 offset:61440
  v_mfma_f32_16x16x32_bf16 v[152:155], a[216:219], a[72:75], v[152:155]
  v_mfma_f32_16x16x32_bf16 v[184:187], a[216:219], a[76:79], v[184:187]
  v_mfma_f32_16x16x32_bf16 v[216:219], a[216:219], a[80:83], v[216:219]
  v_mfma_f32_16x16x32_bf16 v[156:159], a[220:223], a[72:75], v[156:159]
  v_mfma_f32_16x16x32_bf16 v[188:191], a[220:223], a[76:79], v[188:191]
  v_mfma_f32_16x16x32_bf16 v[220:223], a[220:223], a[80:83], v[220:223]
  s_waitcnt lgkmcnt(0)
  v_mfma_f32_16x16x32_bf16 v[136:139], a[232:235], a[84:87], v[136:139]
  ds_read_b64_tr_b16 a[192:193], v3
  v_mfma_f32_16x16x32_bf16 v[168:171], a[232:235], a[88:91], v[168:171]
  ds_read_b64_tr_b16 a[194:195], v3 offset:4096
  v_mfma_f32_16x16x32_bf16 v[200:203], a[232:235], a[92:95], v[200:203]
  ds_read_b64_tr_b16 a[196:197], v4
  v_mfma_f32_16x16x32_bf16 v[140:143], a[236:239], a[84:87], v[140:143]
  ds_read_b64_tr_b16 a[198:199], v4 offset:4096
  v_mfma_f32_16x16x32_bf16 v[172:175], a[236:239], a[88:91], v[172:175]
  ds_read_b64_tr_b16 a[224:225], v3 offset:8192
  v_mfma_f32_16x16x32_bf16 v[204:207], a[236:239], a[92:95], v[204:207]
  ds_read_b64_tr_b16 a[226:227], v3 offset:12288
  v_mfma_f32_16x16x32_bf16 v[144:147], a[240:243], a[84:87], v[144:147]
  ds_read_b64_tr_b16 a[228:229], v4 offset:8192
  v_mfma_f32_16x16x32_bf16 v[176:179], a[240:243], a[88:91], v[176:179]
  ds_read_b64_tr_b16 a[230:231], v4 offset:12288
  v_mfma_f32_16x16x32_bf16 v[208:211], a[240:243], a[92:95], v[208:211]
  v_mfma_f32_16x16x32_bf16 v[148:151], a[244:247], a[84:87], v[148:151]
  v_mfma_f32_16x16x32_bf16 v[180:183], a[244:247], a[88:91], v[180:183]
  v_mfma_f32_16x16x32_bf16 v[212:215], a[244:247], a[92:95], v[212:215]
  v_mfma_f32_16x16x32_bf16 v[152:155], a[248:251], a[84:87], v[152:155]
  v_mfma_f32_16x16x32_bf16 v[184:187], a[248:251], a[88:91], v[184:187]
  v_mfma_f32_16x16x32_bf16 v[216:219], a[248:251], a[92:95], v[216:219]
  v_mfma_f32_16x16x32_bf16 v[156:159], a[252:255], a[84:87], v[156:159]
  v_mfma_f32_16x16x32_bf16 v[188:191], a[252:255], a[88:91], v[188:191]
  v_mfma_f32_16x16x32_bf16 v[220:223], a[252:255], a[92:95], v[220:223]
  s_waitcnt lgkmcnt(0)
  v_mfma_f32_16x16x32_bf16 v[128:131], a[192:195], v[32:35], v[128:131]
  v_mfma_f32_16x16x32_bf16 v[160:163], a[192:195], v[36:39], v[160:163]
  s_mul_i32 s76, s36, 3072
  s_add_u32 s74, s44, 1
  v_mfma_f32_16x16x32_bf16 v[192:195], a[192:195], v[40:43], v[192:195]
  s_and_b32 s74, s74, 3
  s_mul_i32 s74, s74, 36864
  v_mfma_f32_16x16x32_bf16 v[132:135], a[196:199], v[32:35], v[132:135]
  ds_read_b64_tr_b16 a[192:193], v3 offset:16384
  ds_read_b64_tr_b16 a[194:195], v3 offset:20480
  s_add_u32 s74, s74, s76
  s_add_u32 s74, s74, 0
  v_mfma_f32_16x16x32_bf16 v[164:167], a[196:199], v[36:39], v[164:167]
  v_cvt_pk_bf16_f32 v232, v136, v137
  v_cvt_pk_bf16_f32 v233, v138, v139
  v_mfma_f32_16x16x32_bf16 v[196:199], a[196:199], v[40:43], v[196:199]
  v_cvt_pk_bf16_f32 v234, v140, v141
  v_cvt_pk_bf16_f32 v235, v142, v143
  v_mfma_f32_16x16x32_bf16 v[128:131], a[224:227], v[44:47], v[128:131]
  ds_read_b64_tr_b16 a[196:197], v4 offset:16384
  ds_read_b64_tr_b16 a[198:199], v4 offset:20480
  buffer_store_dwordx4 v[232:235], v0, s[28:31], s74 offen offset:0 sc1
  v_cvt_pk_bf16_f32 v236, v168, v169
  v_mfma_f32_16x16x32_bf16 v[160:163], a[224:227], v[48:51], v[160:163]
  v_cvt_pk_bf16_f32 v237, v170, v171
  v_cvt_pk_bf16_f32 v238, v172, v173
  v_mfma_f32_16x16x32_bf16 v[192:195], a[224:227], v[52:55], v[192:195]
  v_cvt_pk_bf16_f32 v239, v174, v175
  buffer_store_dwordx4 v[236:239], v0, s[28:31], s74 offen offset:1024 sc1
  v_mfma_f32_16x16x32_bf16 v[132:135], a[228:231], v[44:47], v[132:135]
  ds_read_b64_tr_b16 a[224:225], v3 offset:24576
  ds_read_b64_tr_b16 a[226:227], v3 offset:28672
  v_cvt_pk_bf16_f32 v240, v200, v201
  v_cvt_pk_bf16_f32 v241, v202, v203
  v_mfma_f32_16x16x32_bf16 v[164:167], a[228:231], v[48:51], v[164:167]
  v_cvt_pk_bf16_f32 v242, v204, v205
  v_cvt_pk_bf16_f32 v243, v206, v207
  v_mfma_f32_16x16x32_bf16 v[196:199], a[228:231], v[52:55], v[196:199]
  s_waitcnt lgkmcnt(4)
  buffer_store_dwordx4 v[240:243], v0, s[28:31], s74 offen offset:2048 sc1
  s_add_u32 s74, s44, 2
  v_mfma_f32_16x16x32_bf16 v[128:131], a[192:195], v[56:59], v[128:131]
  ds_read_b64_tr_b16 a[228:229], v4 offset:24576
  ds_read_b64_tr_b16 a[230:231], v4 offset:28672
  s_and_b32 s74, s74, 3
  s_mul_i32 s74, s74, 36864
  v_mfma_f32_16x16x32_bf16 v[160:163], a[192:195], v[60:63], v[160:163]
  s_add_u32 s74, s74, s76
  s_add_u32 s74, s74, 12288
  v_mfma_f32_16x16x32_bf16 v[192:195], a[192:195], v[64:67], v[192:195]
  s_waitcnt lgkmcnt(4)
  v_cvt_pk_bf16_f32 v232, v144, v145
  v_cvt_pk_bf16_f32 v233, v146, v147
  v_mfma_f32_16x16x32_bf16 v[132:135], a[196:199], v[56:59], v[132:135]
  ds_read_b64_tr_b16 a[192:193], v3 offset:32768
  ds_read_b64_tr_b16 a[194:195], v3 offset:36864
  v_cvt_pk_bf16_f32 v234, v148, v149
  v_cvt_pk_bf16_f32 v235, v150, v151
  v_mfma_f32_16x16x32_bf16 v[164:167], a[196:199], v[60:63], v[164:167]
  buffer_store_dwordx4 v[232:235], v0, s[28:31], s74 offen offset:0 sc1
  v_cvt_pk_bf16_f32 v236, v176, v177
  v_mfma_f32_16x16x32_bf16 v[196:199], a[196:199], v[64:67], v[196:199]
  s_waitcnt lgkmcnt(4)
  v_cvt_pk_bf16_f32 v237, v178, v179
  v_cvt_pk_bf16_f32 v238, v180, v181
  v_mfma_f32_16x16x32_bf16 v[128:131], a[224:227], v[68:71], v[128:131]
  ds_read_b64_tr_b16 a[196:197], v4 offset:32768
  ds_read_b64_tr_b16 a[198:199], v4 offset:36864
  v_cvt_pk_bf16_f32 v239, v182, v183
  buffer_store_dwordx4 v[236:239], v0, s[28:31], s74 offen offset:1024 sc1
  v_mfma_f32_16x16x32_bf16 v[160:163], a[224:227], v[72:75], v[160:163]
  v_cvt_pk_bf16_f32 v240, v208, v209
  v_cvt_pk_bf16_f32 v241, v210, v211
  v_mfma_f32_16x16x32_bf16 v[192:195], a[224:227], v[76:79], v[192:195]
  s_waitcnt lgkmcnt(4)
  v_cvt_pk_bf16_f32 v242, v212, v213
  v_cvt_pk_bf16_f32 v243, v214, v215
  v_mfma_f32_16x16x32_bf16 v[132:135], a[228:231], v[68:71], v[132:135]
  ds_read_b64_tr_b16 a[224:225], v3 offset:40960
  ds_read_b64_tr_b16 a[226:227], v3 offset:45056
  buffer_store_dwordx4 v[240:243], v0, s[28:31], s74 offen offset:2048 sc1
  s_add_u32 s74, s44, 3
  v_mfma_f32_16x16x32_bf16 v[164:167], a[228:231], v[72:75], v[164:167]
  s_and_b32 s74, s74, 3
  s_mul_i32 s74, s74, 36864
  v_mfma_f32_16x16x32_bf16 v[196:199], a[228:231], v[76:79], v[196:199]
  s_waitcnt lgkmcnt(4)
  s_add_u32 s74, s74, s76
  s_add_u32 s74, s74, 24576
  v_mfma_f32_16x16x32_bf16 v[128:131], a[192:195], v[80:83], v[128:131]
  ds_read_b64_tr_b16 a[228:229], v4 offset:40960
  ds_read_b64_tr_b16 a[230:231], v4 offset:45056
  v_cvt_pk_bf16_f32 v232, v152, v153
  v_cvt_pk_bf16_f32 v233, v154, v155
  v_mfma_f32_16x16x32_bf16 v[160:163], a[192:195], v[84:87], v[160:163]
  v_cvt_pk_bf16_f32 v234, v156, v157
  v_cvt_pk_bf16_f32 v235, v158, v159
  v_mfma_f32_16x16x32_bf16 v[192:195], a[192:195], v[88:91], v[192:195]
  s_waitcnt lgkmcnt(4)
  buffer_store_dwordx4 v[232:235], v0, s[28:31], s74 offen offset:0 sc1
  v_cvt_pk_bf16_f32 v236, v184, v185
  v_mfma_f32_16x16x32_bf16 v[132:135], a[196:199], v[80:83], v[132:135]
  ds_read_b64_tr_b16 a[192:193], v3 offset:49152
  ds_read_b64_tr_b16 a[194:195], v3 offset:53248
  v_cvt_pk_bf16_f32 v237, v186, v187
  v_cvt_pk_bf16_f32 v238, v188, v189
  v_mfma_f32_16x16x32_bf16 v[164:167], a[196:199], v[84:87], v[164:167]
  v_cvt_pk_bf16_f32 v239, v190, v191
  buffer_store_dwordx4 v[236:239], v0, s[28:31], s74 offen offset:1024 sc1
  v_mfma_f32_16x16x32_bf16 v[196:199], a[196:199], v[88:91], v[196:199]
  s_waitcnt lgkmcnt(4)
  v_cvt_pk_bf16_f32 v240, v216, v217
  v_cvt_pk_bf16_f32 v241, v218, v219
  v_mfma_f32_16x16x32_bf16 v[128:131], a[224:227], v[92:95], v[128:131]
  ds_read_b64_tr_b16 a[196:197], v4 offset:49152
  ds_read_b64_tr_b16 a[198:199], v4 offset:53248
  v_cvt_pk_bf16_f32 v242, v220, v221
  v_cvt_pk_bf16_f32 v243, v222, v223
  v_mfma_f32_16x16x32_bf16 v[160:163], a[224:227], v[96:99], v[160:163]
  buffer_store_dwordx4 v[240:243], v0, s[28:31], s74 offen offset:2048 sc1
  v_mfma_f32_16x16x32_bf16 v[192:195], a[224:227], v[100:103], v[192:195]
  s_waitcnt lgkmcnt(4)
  v_mfma_f32_16x16x32_bf16 v[132:135], a[228:231], v[92:95], v[132:135]
  ds_read_b64_tr_b16 a[224:225], v3 offset:57344
  ds_read_b64_tr_b16 a[226:227], v3 offset:61440
  v_mfma_f32_16x16x32_bf16 v[164:167], a[228:231], v[96:99], v[164:167]
  v_mfma_f32_16x16x32_bf16 v[196:199], a[228:231], v[100:103], v[196:199]
  s_waitcnt lgkmcnt(4)
  v_mfma_f32_16x16x32_bf16 v[128:131], a[192:195], v[104:107], v[128:131]
  ds_read_b64_tr_b16 a[228:229], v4 offset:57344
  ds_read_b64_tr_b16 a[230:231], v4 offset:61440
  v_mfma_f32_16x16x32_bf16 v[160:163], a[192:195], v[108:111], v[160:163]
  v_mfma_f32_16x16x32_bf16 v[192:195], a[192:195], v[112:115], v[192:195]
  s_waitcnt lgkmcnt(4)
  v_mfma_f32_16x16x32_bf16 v[132:135], a[196:199], v[104:107], v[132:135]
  ds_read_b64_tr_b16 a[192:193], v224
  ds_read_b64_tr_b16 a[194:195], v224 offset:4096
  v_mfma_f32_16x16x32_bf16 v[164:167], a[196:199], v[108:111], v[164:167]
  v_mfma_f32_16x16x32_bf16 v[196:199], a[196:199], v[112:115], v[196:199]
  s_waitcnt lgkmcnt(4)
  v_mfma_f32_16x16x32_bf16 v[128:131], a[224:227], v[116:119], v[128:131]
  ds_read_b64_tr_b16 a[196:197], v225
  ds_read_b64_tr_b16 a[198:199], v225 offset:4096
  v_mfma_f32_16x16x32_bf16 v[160:163], a[224:227], v[120:123], v[160:163]
  v_mfma_f32_16x16x32_bf16 v[192:195], a[224:227], v[124:127], v[192:195]
  s_waitcnt lgkmcnt(4)
  v_mfma_f32_16x16x32_bf16 v[132:135], a[228:231], v[116:119], v[132:135]
  ds_read_b64_tr_b16 a[224:225], v224 offset:8192
  ds_read_b64_tr_b16 a[226:227], v224 offset:12288
  v_mfma_f32_16x16x32_bf16 v[164:167], a[228:231], v[120:123], v[164:167]
  v_mfma_f32_16x16x32_bf16 v[196:199], a[228:231], v[124:127], v[196:199]
  s_waitcnt lgkmcnt(4)
  v_mfma_f32_16x16x32_bf16 v[128:131], a[192:195], a[0:3], v[128:131]
  ds_read_b64_tr_b16 a[228:229], v225 offset:8192
  ds_read_b64_tr_b16 a[230:231], v225 offset:12288
  v_mfma_f32_16x16x32_bf16 v[160:163], a[192:195], a[4:7], v[160:163]
  v_mfma_f32_16x16x32_bf16 v[192:195], a[192:195], a[8:11], v[192:195]
  s_waitcnt lgkmcnt(4)
  v_mfma_f32_16x16x32_bf16 v[132:135], a[196:199], a[0:3], v[132:135]
  ds_read_b64_tr_b16 a[192:193], v224 offset:16384
  ds_read_b64_tr_b16 a[194:195], v224 offset:20480
  v_mfma_f32_16x16x32_bf16 v[164:167], a[196:199], a[4:7], v[164:167]
  v_mfma_f32_16x16x32_bf16 v[196:199], a[196:199], a[8:11], v[196:199]
  s_waitcnt lgkmcnt(4)
  v_mfma_f32_16x16x32_bf16 v[128:131], a[224:227], a[12:15], v[128:131]
  ds_read_b64_tr_b16 a[196:197], v225 offset:16384
  ds_read_b64_tr_b16 a[198:199], v225 offset:20480
  v_mfma_f32_16x16x32_bf16 v[160:163], a[224:227], a[16:19], v[160:163]
  v_mfma_f32_16x16x32_bf16 v[192:195], a[224:227], a[20:23], v[192:195]
  s_waitcnt lgkmcnt(4)
  v_mfma_f32_16x16x32_bf16 v[132:135], a[228:231], a[12:15], v[132:135]
  ds_read_b64_tr_b16 a[224:225], v224 offset:24576
  ds_read_b64_tr_b16 a[226:227], v224 offset:28672
  v_mfma_f32_16x16x32_bf16 v[164:167], a[228:231], a[16:19], v[164:167]
  v_mfma_f32_16x16x32_bf16 v[196:199], a[228:231], a[20:23], v[196:199]
  s_waitcnt lgkmcnt(4)
  v_mfma_f32_16x16x32_bf16 v[128:131], a[192:195], a[24:27], v[128:131]
  ds_read_b64_tr_b16 a[228:229], v225 offset:24576
  ds_read_b64_tr_b16 a[230:231], v225 offset:28672
  v_mfma_f32_16x16x32_bf16 v[160:163], a[192:195], a[28:31], v[160:163]
  v_mfma_f32_16x16x32_bf16 v[192:195], a[192:195], a[32:35], v[192:195]
  s_waitcnt lgkmcnt(4)
  v_mfma_f32_16x16x32_bf16 v[132:135], a[196:199], a[24:27], v[132:135]
  ds_read_b64_tr_b16 a[192:193], v224 offset:32768
  ds_read_b64_tr_b16 a[194:195], v224 offset:36864
  v_mfma_f32_16x16x32_bf16 v[164:167], a[196:199], a[28:31], v[164:167]
  v_mfma_f32_16x16x32_bf16 v[196:199], a[196:199], a[32:35], v[196:199]
  s_waitcnt lgkmcnt(4)
  v_mfma_f32_16x16x32_bf16 v[128:131], a[224:227], a[36:39], v[128:131]
  ds_read_b64_tr_b16 a[196:197], v225 offset:32768
  ds_read_b64_tr_b16 a[198:199], v225 offset:36864
  v_mfma_f32_16x16x32_bf16 v[160:163], a[224:227], a[40:43], v[160:163]
  v_mfma_f32_16x16x32_bf16 v[192:195], a[224:227], a[44:47], v[192:195]
  s_waitcnt lgkmcnt(4)
  v_mfma_f32_16x16x32_bf16 v[132:135], a[228:231], a[36:39], v[132:135]
  ds_read_b64_tr_b16 a[224:225], v224 offset:40960
  ds_read_b64_tr_b16 a[226:227], v224 offset:45056
  v_mfma_f32_16x16x32_bf16 v[164:167], a[228:231], a[40:43], v[164:167]
  v_mfma_f32_16x16x32_bf16 v[196:199], a[228:231], a[44:47], v[196:199]
  s_waitcnt lgkmcnt(4)
  v_mfma_f32_16x16x32_bf16 v[128:131], a[192:195], a[48:51], v[128:131]
  ds_read_b64_tr_b16 a[228:229], v225 offset:40960
  ds_read_b64_tr_b16 a[230:231], v225 offset:45056
  v_mfma_f32_16x16x32_bf16 v[160:163], a[192:195], a[52:55], v[160:163]
  v_mfma_f32_16x16x32_bf16 v[192:195], a[192:195], a[56:59], v[192:195]
  s_waitcnt lgkmcnt(4)
  v_mfma_f32_16x16x32_bf16 v[132:135], a[196:199], a[48:51], v[132:135]
  ds_read_b64_tr_b16 a[192:193], v224 offset:49152
  ds_read_b64_tr_b16 a[194:195], v224 offset:53248
  v_mfma_f32_16x16x32_bf16 v[164:167], a[196:199], a[52:55], v[164:167]
  v_mfma_f32_16x16x32_bf16 v[196:199], a[196:199], a[56:59], v[196:199]
  s_waitcnt lgkmcnt(4)
  v_mfma_f32_16x16x32_bf16 v[128:131], a[224:227], a[60:63], v[128:131]
  ds_read_b64_tr_b16 a[196:197], v225 offset:49152
  ds_read_b64_tr_b16 a[198:199], v225 offset:53248
  v_mfma_f32_16x16x32_bf16 v[160:163], a[224:227], a[64:67], v[160:163]
  v_mfma_f32_16x16x32_bf16 v[192:195], a[224:227], a[68:71], v[192:195]
  s_waitcnt lgkmcnt(4)
  v_mfma_f32_16x16x32_bf16 v[132:135], a[228:231], a[60:63], v[132:135]
  ds_read_b64_tr_b16 a[224:225], v224 offset:57344
  ds_read_b64_tr_b16 a[226:227], v224 offset:61440
  v_mfma_f32_16x16x32_bf16 v[164:167], a[228:231], a[64:67], v[164:167]
  v_mfma_f32_16x16x32_bf16 v[196:199], a[228:231], a[68:71], v[196:199]
  s_waitcnt lgkmcnt(4)
  v_mfma_f32_16x16x32_bf16 v[128:131], a[192:195], a[72:75], v[128:131]
  ds_read_b64_tr_b16 a[228:229], v225 offset:57344
  ds_read_b64_tr_b16 a[230:231], v225 offset:61440
  v_mfma_f32_16x16x32_bf16 v[160:163], a[192:195], a[76:79], v[160:163]
  v_mfma_f32_16x16x32_bf16 v[192:195], a[192:195], a[80:83], v[192:195]
  s_waitcnt lgkmcnt(4)
  v_mfma_f32_16x16x32_bf16 v[132:135], a[196:199], a[72:75], v[132:135]
  v_mfma_f32_16x16x32_bf16 v[164:167], a[196:199], a[76:79], v[164:167]
  v_mfma_f32_16x16x32_bf16 v[196:199], a[196:199], a[80:83], v[196:199]
  s_waitcnt lgkmcnt(2)
  v_mfma_f32_16x16x32_bf16 v[128:131], a[224:227], a[84:87], v[128:131]
  v_mfma_f32_16x16x32_bf16 v[160:163], a[224:227], a[88:91], v[160:163]
  v_mfma_f32_16x16x32_bf16 v[192:195], a[224:227], a[92:95], v[192:195]
  s_waitcnt lgkmcnt(0)
  v_mfma_f32_16x16x32_bf16 v[132:135], a[228:231], a[84:87], v[132:135]
  v_mfma_f32_16x16x32_bf16 v[164:167], a[228:231], a[88:91], v[164:167]
  v_mfma_f32_16x16x32_bf16 v[196:199], a[228:231], a[92:95], v[196:199]
  s_lshl_b32 s74, s47, 7
  s_add_u32 s74, s74, 0x8000
  s_lshl_b32 s75, s44, 4
  s_add_u32 s75, s75, s74
  v_mov_b32_e32 v27, s75
  v_add_u32_e32 v26, s74, v0
  s_waitcnt vmcnt(0) lgkmcnt(0)
  s_barrier
  s_cmp_lg_u32 s36, 0
  s_cbranch_scc1 .Lk2sync%=
  s_mov_b64 exec, 1
  v_mov_b32_e32 v250, s82
  v_mov_b32_e32 v251, s83
  global_store_dwordx2 v27, v[250:251], s[92:93] sc1
  s_mov_b64 exec, -1
  s_nop 0
  s_mov_b64 exec, 0xf
  s_mov_b32 s73, 0x20000
.Lxwa%=:
  global_load_dwordx2 v[246:247], v26, s[92:93] sc1
  s_waitcnt vmcnt(0)
.Lxwc%=:
  v_cmp_ne_u64_e32 vcc, s[82:83], v[246:247]
  s_sub_u32 s73, s73, 1
  s_cbranch_scc1 .Lxwb%=
  s_nop 3
  s_cbranch_vccnz .Lxwa%=
.Lxwb%=:
  s_mov_b64 exec, -1
.Lk2sync%=:
  s_barrier
  s_mul_i32 s74, s44, 36864
  s_add_u32 s74, s74, s76
  s_add_u32 s75, s74, 12288
  s_add_u32 s76, s74, 24576
  buffer_load_dwordx4 v[32:35], v0, s[28:31], s74 offen offset:0 sc1
  buffer_load_dwordx4 v[36:39], v0, s[28:31], s75 offen offset:0 sc1
  buffer_load_dwordx4 v[40:43], v0, s[28:31], s76 offen offset:0 sc1
  buffer_load_dwordx4 v[44:47], v0, s[28:31], s74 offen offset:1024 sc1
  buffer_load_dwordx4 v[48:51], v0, s[28:31], s75 offen offset:1024 sc1
  buffer_load_dwordx4 v[52:55], v0, s[28:31], s76 offen offset:1024 sc1
  buffer_load_dwordx4 v[56:59], v0, s[28:31], s74 offen offset:2048 sc1
  buffer_load_dwordx4 v[60:63], v0, s[28:31], s75 offen offset:2048 sc1
  buffer_load_dwordx4 v[64:67], v0, s[28:31], s76 offen offset:2048 sc1
  s_waitcnt vmcnt(6)
  v_mfma_f32_16x16x32_bf16 v[128:131], v[22:25], v[32:35], v[128:131]
  v_mfma_f32_16x16x32_bf16 v[132:135], v[20:23], v[32:35], v[132:135]
  v_mfma_f32_16x16x32_bf16 v[128:131], v[22:25], v[36:39], v[128:131]
  v_mfma_f32_16x16x32_bf16 v[132:135], v[20:23], v[36:39], v[132:135]
  v_mfma_f32_16x16x32_bf16 v[128:131], v[22:25], v[40:43], v[128:131]
  v_mfma_f32_16x16x32_bf16 v[132:135], v[20:23], v[40:43], v[132:135]
  s_waitcnt vmcnt(3)
  v_mfma_f32_16x16x32_bf16 v[160:163], v[22:25], v[44:47], v[160:163]
  v_mfma_f32_16x16x32_bf16 v[164:167], v[20:23], v[44:47], v[164:167]
  v_mfma_f32_16x16x32_bf16 v[160:163], v[22:25], v[48:51], v[160:163]
  v_mfma_f32_16x16x32_bf16 v[164:167], v[20:23], v[48:51], v[164:167]
  v_mfma_f32_16x16x32_bf16 v[160:163], v[22:25], v[52:55], v[160:163]
  v_mfma_f32_16x16x32_bf16 v[164:167], v[20:23], v[52:55], v[164:167]
  s_waitcnt vmcnt(0)
  v_mfma_f32_16x16x32_bf16 v[192:195], v[22:25], v[56:59], v[192:195]
  v_mfma_f32_16x16x32_bf16 v[196:199], v[20:23], v[56:59], v[196:199]
  v_mfma_f32_16x16x32_bf16 v[192:195], v[22:25], v[60:63], v[192:195]
  v_mfma_f32_16x16x32_bf16 v[196:199], v[20:23], v[60:63], v[196:199]
  v_mfma_f32_16x16x32_bf16 v[192:195], v[22:25], v[64:67], v[192:195]
  v_mfma_f32_16x16x32_bf16 v[196:199], v[20:23], v[64:67], v[196:199]
  s_nop 1
  v_cvt_pk_bf16_f32 v232, v128, v129
  v_cvt_pk_bf16_f32 v233, v130, v131
  v_cvt_pk_bf16_f32 v234, v132, v133
  v_cvt_pk_bf16_f32 v235, v134, v135
  s_mov_b32 s77, 0
  s_nop 0
  v_permlane16_swap_b32_e32 v232, v234
  v_permlane16_swap_b32_e32 v233, v235
  buffer_store_dwordx4 v[232:235], v1, s[24:27], s77 offen
  v_cvt_pk_bf16_f32 v236, v160, v161
  v_cvt_pk_bf16_f32 v237, v162, v163
  v_cvt_pk_bf16_f32 v238, v164, v165
  v_cvt_pk_bf16_f32 v239, v166, v167
  s_mov_b32 s77, 40960
  s_nop 0
  v_permlane16_swap_b32_e32 v236, v238
  v_permlane16_swap_b32_e32 v237, v239
  buffer_store_dwordx4 v[236:239], v1, s[24:27], s77 offen
  v_cvt_pk_bf16_f32 v240, v192, v193
  v_cvt_pk_bf16_f32 v241, v194, v195
  v_cvt_pk_bf16_f32 v242, v196, v197
  v_cvt_pk_bf16_f32 v243, v198, v199
  s_mov_b32 s77, 81920
  s_nop 0
  v_permlane16_swap_b32_e32 v240, v242
  v_permlane16_swap_b32_e32 v241, v243
  buffer_store_dwordx4 v[240:243], v1, s[24:27], s77 offen
  s_waitcnt vmcnt(0) lgkmcnt(0)
.Ldone%=:
  s_waitcnt vmcnt(0) lgkmcnt(0)
  s_mov_b64 %[tag], s[82:83]
.Lend%=:
)ASM"
      : [bad] "=&s"(bad), [tag] "=&s"(tag)
      : [ka] "s"(ka), [wid] "s"(wid), [gid] "s"(g)
      : "v0", "v1", "v2", "v3", "v4", "v5", "v6", "v7", "v8", "v9", "v10", "v11", "v12", "v13", "v14", "v15", "v16", "v17", "v18", "v19", "v20", "v21", "v22", "v23",
      "v24", "v25", "v26", "v27", "v28", "v29", "v30", "v31", "v32", "v33", "v34", "v35", "v36", "v37", "v38", "v39", "v40", "v41", "v42", "v43", "v44", "v45", "v46", "v47",
      "v48", "v49", "v50", "v51", "v52", "v53", "v54", "v55", "v56", "v57", "v58", "v59", "v60", "v61", "v62", "v63", "v64", "v65", "v66", "v67", "v68", "v69", "v70", "v71",
      "v72", "v73", "v74", "v75", "v76", "v77", "v78", "v79", "v80", "v81", "v82", "v83", "v84", "v85", "v86", "v87", "v88", "v89", "v90", "v91", "v92", "v93", "v94", "v95",
      "v96", "v97", "v98", "v99", "v100", "v101", "v102", "v103", "v104", "v105", "v106", "v107", "v108", "v109", "v110", "v111", "v112", "v113", "v114", "v115", "v116", "v117", "v118", "v119",
      "v120", "v121", "v122", "v123", "v124", "v125", "v126", "v127", "v128", "v129", "v130", "v131", "v132", "v133", "v134", "v135", "v136", "v137", "v138", "v139", "v140", "v141", "v142", "v143",
      "v144", "v145", "v146", "v147", "v148", "v149", "v150", "v151", "v152", "v153", "v154", "v155", "v156", "v157", "v158", "v159", "v160", "v161", "v162", "v163", "v164", "v165", "v166", "v167",
      "v168", "v169", "v170", "v171", "v172", "v173", "v174", "v175", "v176", "v177", "v178", "v179", "v180", "v181", "v182", "v183", "v184", "v185", "v186", "v187", "v188", "v189", "v190", "v191",
      "v192", "v193", "v194", "v195", "v196", "v197", "v198", "v199", "v200", "v201", "v202", "v203", "v204", "v205", "v206", "v207", "v208", "v209", "v210", "v211", "v212", "v213", "v214", "v215",
      "v216", "v217", "v218", "v219", "v220", "v221", "v222", "v223", "v224", "v225", "v226", "v227", "v228", "v229", "v230", "v231", "v232", "v233", "v234", "v235", "v236", "v237", "v238", "v239",
      "v240", "v241", "v242", "v243", "v244", "v245", "v246", "v247", "v248", "v249", "v250", "v251", "v252", "v253", "v254", "v255", "a0", "a1", "a2", "a3", "a4", "a5", "a6", "a7",
      "a8", "a9", "a10", "a11", "a12", "a13", "a14", "a15", "a16", "a17", "a18", "a19", "a20", "a21", "a22", "a23", "a24", "a25", "a26", "a27", "a28", "a29", "a30", "a31",
      "a32", "a33", "a34", "a35", "a36", "a37", "a38", "a39", "a40", "a41", "a42", "a43", "a44", "a45", "a46", "a47", "a48", "a49", "a50", "a51", "a52", "a53", "a54", "a55",
      "a56", "a57", "a58", "a59", "a60", "a61", "a62", "a63", "a64", "a65", "a66", "a67", "a68", "a69", "a70", "a71", "a72", "a73", "a74", "a75", "a76", "a77", "a78", "a79",
      "a80", "a81", "a82", "a83", "a84", "a85", "a86", "a87", "a88", "a89", "a90", "a91", "a92", "a93", "a94", "a95", "a96", "a97", "a98", "a99", "a100", "a101", "a102", "a103",
      "a104", "a105", "a106", "a107", "a108", "a109", "a110", "a111", "a112", "a113", "a114", "a115", "a116", "a117", "a118", "a119", "a120", "a121", "a122", "a123", "a124", "a125", "a126", "a127",
      "a128", "a129", "a130", "a131", "a132", "a133", "a134", "a135", "a136", "a137", "a138", "a139", "a140", "a141", "a142", "a143", "a144", "a145", "a146", "a147", "a148", "a149", "a150", "a151",
      "a152", "a153", "a154", "a155", "a156", "a157", "a158", "a159", "a160", "a161", "a162", "a163", "a164", "a165", "a166", "a167", "a168", "a169", "a170", "a171", "a172", "a173", "a174", "a175",
      "a176", "a177", "a178", "a179", "a180", "a181", "a182", "a183", "a184", "a185", "a186", "a187", "a188", "a189", "a190", "a191", "a192", "a193", "a194", "a195", "a196", "a197", "a198", "a199",
      "a200", "a201", "a202", "a203", "a204", "a205", "a206", "a207", "a208", "a209", "a210", "a211", "a212", "a213", "a214", "a215", "a216", "a217", "a218", "a219", "a220", "a221", "a222", "a223",
      "a224", "a225", "a226", "a227", "a228", "a229", "a230", "a231", "a232", "a233", "a234", "a235", "a236", "a237", "a238", "a239", "a240", "a241", "a242", "a243", "a244", "a245", "a246", "a247",
      "a248", "a249", "a250", "a251", "a252", "a253", "a254", "a255", "s16", "s17", "s18", "s19", "s20", "s21", "s22", "s23", "s24", "s25", "s26", "s27", "s28", "s29", "s30", "s31",
      "s36", "s37", "s38", "s39", "s40", "s41", "s42", "s43", "s44", "s45", "s46", "s47", "s48", "s49", "s50", "s51", "s52", "s53", "s54", "s55", "s56", "s57", "s58", "s59",
      "s60", "s61", "s62", "s63", "s64", "s65", "s66", "s67", "s68", "s69", "s70", "s71", "s72", "s73", "s74", "s75", "s76", "s77", "s78", "s79", "s80", "s81", "s82", "s83",
      "s84", "s85", "s86", "s87", "s88", "s89", "s90", "s91", "s92", "s93", "s94", "s95", "s96", "s97", "s98", "s99", "vcc", "scc", "memory");
  if (bad) {
    int lane;
    asm volatile("v_mbcnt_lo_u32_b32 %0, -1, 0\n\tv_mbcnt_hi_u32_b32 %0, -1, %0" : "=v"(lane));
    asm volatile("" : "+s"(ka), "+s"(g));
    __syncthreads();
    const int xcd = g & 7, j = g >> 3, br = xcd >> 2, b = j >> 2;
    for (int it = 0; it < 2; ++it)
      if (bad >> it & 1) {
        const int h = (xcd & 3) * 8 + (j & 3) + 4 * it;
        slow_item(ka[4 * br] + (size_t)b * SQ * DM, ka[4 * br + 3] + h * DH, ka[4 * br + 1] + (size_t)(b * NH + h) * SK * DH, ka[4 * br + 2] + (size_t)(b * NH + h) * SK * DH,
                  (u16*)ka[10] + ((size_t)(br * 8 + b) * 9 * 128 + 4 * h) * 512, wid, lane, (float*)lds);
      }
    asm volatile(R"ASM(
  s_mov_b32 s36, %[wid]
  s_mov_b64 s[82:83], %[tag]
  s_bfe_u32 s50, %[gid], 0x10002
  s_lshl_b32 s52, s50, 3
  s_add_u32 s52, s52, 64
  s_load_dwordx2 s[80:81], %[ka], s52
  s_load_dwordx4 s[88:91], %[ka], 0x50
  v_mbcnt_lo_u32_b32 v27, -1, 0
  v_mbcnt_hi_u32_b32 v27, -1, v27
  v_lshlrev_b32_e32 v0, 4, v27
  s_waitcnt lgkmcnt(0)
  s_add_u32 s92, s88, 19922944
  s_addc_u32 s93, s89, 0
  v_bfe_u32 v28, v27, 2, 2
  v_lshrrev_b32_e32 v29, 4, v27
  v_and_b32_e32 v30, 1, v27
  v_lshlrev_b32_e32 v30, 4, v30
  v_mov_b32_e32 v31, 0x3f80
  v_lshlrev_b32_e32 v30, v30, v31
  v_cmp_eq_u32_e32 vcc, v28, v29
  v_and_b32_e32 v28, 2, v27
  v_mov_b32_e32 v20, 0
  v_cndmask_b32_e32 v30, 0, v30, vcc
  v_cmp_eq_u32_e32 vcc, 0, v28
  v_mov_b32_e32 v21, 0
  v_mov_b32_e32 v24, 0
  v_cndmask_b32_e32 v22, 0, v30, vcc
  v_sub_u32_e32 v23, v30, v22
  v_mov_b32_e32 v25, 0
  s_bfe_u32 s68, %[gid], 0x10002
  s_lshr_b32 s69, %[gid], 3
  s_mul_i32 s70, s69, 52
  s_lshr_b32 s70, s70, 8
  s_mul_i32 s71, s70, 5
  s_sub_u32 s71, s69, s71
  s_mul_i32 s72, s70, 43
  s_lshr_b32 s72, s72, 5
  v_lshrrev_b32_e32 v248, 6, v0
  v_add_u32_e32 v248, s72, v248
  v_and_b32_e32 v248, 7, v248
  v_bfe_u32 v249, v0, 4, 2
  v_lshl_add_u32 v248, v248, 2, v249
  s_and_b32 s73, %[gid], 7
  v_lshl_add_u32 v248, v248, 3, s73
  v_lshlrev_b32_e32 v26, 6, v248
  s_lshl_b32 s73, %[gid], 6
  v_mov_b32_e32 v27, s73
  s_cmp_lg_u32 s36, 0
  s_cbranch_scc1 .Lnosig%=
  s_mov_b64 exec, 1
  v_mov_b32_e32 v250, s82
  v_mov_b32_e32 v251, s83
  global_store_dwordx2 v27, v[250:251], s[92:93] sc1
  s_mov_b64 exec, -1
  s_nop 0
  s_mov_b64 exec, 1
  v_mov_b32_e32 v250, s82
  v_mov_b32_e32 v251, s83
  global_store_dwordx2 v27, v[250:251], s[92:93] offset:16 sc1
  s_mov_b64 exec, -1
  s_nop 0
.Lnosig%=:
  s_cmp_lt_u32 %[gid], 240
  s_cbranch_scc1 .Lwork%=
  s_cmp_lg_u32 %[gid], 255
  s_cbranch_scc1 .Ldone%=
  s_cmp_lg_u32 s36, 0
  s_cbranch_scc1 .Ldone%=
  v_lshlrev_b32_e32 v26, 2, v0
  s_mov_b64 exec, -1
  s_mov_b32 s73, 0x20000
.Lmon0a%=:
  global_load_dwordx2 v[246:247], v26, s[92:93] sc1
  s_waitcnt vmcnt(0)
.Lmon0c%=:
  v_cmp_ne_u64_e32 vcc, s[82:83], v[246:247]
  s_sub_u32 s73, s73, 1
  s_cbranch_scc1 .Lmon0b%=
  s_nop 3
  s_cbranch_vccnz .Lmon0a%=
.Lmon0b%=:
  s_mov_b64 exec, -1
  v_add_u32_e32 v26, 0x1000, v26
  s_mov_b64 exec, -1
  s_mov_b32 s73, 0x20000
.Lmon1a%=:
  global_load_dwordx2 v[246:247], v26, s[92:93] sc1
  s_waitcnt vmcnt(0)
.Lmon1c%=:
  v_cmp_ne_u64_e32 vcc, s[82:83], v[246:247]
  s_sub_u32 s73, s73, 1
  s_cbranch_scc1 .Lmon1b%=
  s_nop 3
  s_cbranch_vccnz .Lmon1a%=
.Lmon1b%=:
  s_mov_b64 exec, -1
  v_add_u32_e32 v26, 0x1000, v26
  s_mov_b64 exec, -1
  s_mov_b32 s73, 0x20000
.Lmon2a%=:
  global_load_dwordx2 v[246:247], v26, s[92:93] sc1
  s_waitcnt vmcnt(0)
.Lmon2c%=:
  v_cmp_ne_u64_e32 vcc, s[82:83], v[246:247]
  s_sub_u32 s73, s73, 1
  s_cbranch_scc1 .Lmon2b%=
  s_nop 3
  s_cbranch_vccnz .Lmon2a%=
.Lmon2b%=:
  s_mov_b64 exec, -1
  v_add_u32_e32 v26, 0x1000, v26
  s_mov_b64 exec, -1
  s_mov_b32 s73, 0x20000
.Lmon3a%=:
  global_load_dwordx2 v[246:247], v26, s[92:93] sc1
  s_waitcnt vmcnt(0)
.Lmon3c%=:
  v_cmp_ne_u64_e32 vcc, s[82:83], v[246:247]
  s_sub_u32 s73, s73, 1
  s_cbranch_scc1 .Lmon3b%=
  s_nop 3
  s_cbranch_vccnz .Lmon3a%=
.Lmon3b%=:
  s_mov_b64 exec, -1
  v_add_u32_e32 v26, 0x1000, v26
  v_mov_b32_e32 v26, 0xff00
  s_mov_b64 exec, 1
  v_mov_b32_e32 v250, s82
  v_mov_b32_e32 v251, s83
  global_store_dwordx2 v26, v[250:251], s[92:93] sc1
  s_mov_b64 exec, -1
  s_nop 0
  s_branch .Ldone%=
.Lwork%=:
  s_bfe_u32 s68, %[gid], 0x10002
  s_lshr_b32 s69, %[gid], 3
  s_mul_i32 s70, s69, 52
  s_lshr_b32 s70, s70, 8
  s_mul_i32 s71, s70, 5
  s_sub_u32 s71, s69, s71
  s_mul_i32 s72, s70, 43
  s_lshr_b32 s72, s72, 5
  s_and_b32 s44, %[gid], 3
  s_mul_i32 s47, s68, 30
  s_add_u32 s47, s47, s69
  s_mul_i32 s74, s44, 1310720
  s_lshl_b32 s75, s71, 8
  s_add_u32 s74, s74, s75
  s_add_u32 s20, s80, s74
  s_addc_u32 s21, s81, 0
  s_mul_i32 s74, s68, 72
  s_mul_i32 s75, s70, 12
  s_add_u32 s74, s74, s75
  s_mul_i32 s75, s36, 3
  s_add_u32 s74, s74, s75
  s_lshl_b32 s74, s74, 2
  s_add_u32 s74, s74, s44
  s_lshl_b32 s74, s74, 15
  s_add_u32 s16, s88, s74
  s_addc_u32 s17, s89, 0
  s_lshl_b32 s74, s70, 2
  s_add_u32 s74, s74, s36
  s_mul_i32 s74, s74, 122880
  s_mul_i32 s75, s68, 1280
  s_add_u32 s74, s74, s75
  s_lshl_b32 s75, s71, 8
  s_add_u32 s74, s74, s75
  s_lshl_b32 s75, s44, 6
  s_add_u32 s74, s74, s75
  s_add_u32 s24, s90, s74
  s_addc_u32 s25, s91, 0
  s_mul_i32 s74, s47, 147456
  s_add_u32 s74, s74, 19988480
  s_add_u32 s28, s88, s74
  s_addc_u32 s29, s89, 0
  s_mov_b32 s18, 294912
  s_mov_b32 s19, 0x20000
  s_mov_b32 s22, 1309696
  s_mov_b32 s23, 0x20000
  s_mov_b32 s26, 120384
  s_mov_b32 s27, 0x20000
  s_mov_b32 s30, 147456
  s_mov_b32 s31, 0x20000
  s_lshl_b32 s37, s36, 11
  s_mul_i32 s74, s36, 10240
  v_lshrrev_b32_e32 v15, 4, v0
  v_lshrrev_b32_e32 v16, 4, v15
  v_and_b32_e32 v17, 15, v15
  v_lshlrev_b32_e32 v18, 1, v16
  v_xor_b32_e32 v17, v17, v18
  v_mul_u32_u24_e32 v16, 0x500, v16
  v_add_u32_e32 v16, s74, v16
  v_lshl_add_u32 v11, v17, 4, v16
  v_xor_b32_e32 v17, 8, v17
  v_add_u32_e32 v16, 4096, v16
  v_lshl_add_u32 v12, v17, 4, v16
  v_add_u32_e32 v13, 0x20000, v0
  v_add_u32_e32 v14, 0x40000, v0
  v_lshrrev_b32_e32 v16, 2, v15
  v_lshlrev_b32_e32 v19, 8, v16
  v_and_b32_e32 v17, 1, v15
  v_lshl_add_u32 v19, v17, 3, v19
  v_and_b32_e32 v16, 7, v16
  v_lshlrev_b32_e32 v16, 1, v16
  v_bfe_u32 v17, v15, 1, 1
  v_xor_b32_e32 v16, v16, v17
  s_lshl_b32 s74, s44, 2
  s_add_u32 s75, s74, 0
  s_and_b32 s75, s75, 15
  v_xor_b32_e32 v3, s75, v16
  v_lshl_add_u32 v3, v3, 4, v19
  s_add_u32 s75, s74, 2
  s_and_b32 s75, s75, 15
  v_xor_b32_e32 v4, s75, v16
  v_lshl_add_u32 v4, v4, 4, v19
  s_add_u32 s75, s74, 4
  s_and_b32 s75, s75, 15
  v_xor_b32_e32 v5, s75, v16
  v_lshl_add_u32 v5, v5, 4, v19
  s_add_u32 s75, s74, 6
  s_and_b32 s75, s75, 15
  v_xor_b32_e32 v6, s75, v16
  v_lshl_add_u32 v6, v6, 4, v19
  s_add_u32 s75, s74, 8
  s_and_b32 s75, s75, 15
  v_xor_b32_e32 v7, s75, v16
  v_lshl_add_u32 v7, v7, 4, v19
  s_add_u32 s75, s74, 10
  s_and_b32 s75, s75, 15
  v_xor_b32_e32 v8, s75, v16
  v_lshl_add_u32 v8, v8, 4, v19
  s_add_u32 s75, s74, 12
  s_and_b32 s75, s75, 15
  v_xor_b32_e32 v9, s75, v16
  v_lshl_add_u32 v9, v9, 4, v19
  s_add_u32 s75, s74, 14
  s_and_b32 s75, s75, 15
  v_xor_b32_e32 v10, s75, v16
  v_lshl_add_u32 v10, v10, 4, v19
  v_and_b32_e32 v16, 15, v15
  v_mul_u32_u24_e32 v16, 0xa00, v16
  v_bfe_u32 v17, v15, 4, 1
  v_lshl_add_u32 v16, v17, 5, v16
  v_lshrrev_b32_e32 v17, 5, v15
  v_lshl_add_u32 v1, v17, 4, v16
  s_mov_b64 exec, 0xff
  s_mov_b32 s73, 0x20000
.Lp0a%=:
  global_load_dwordx2 v[246:247], v26, s[92:93] offset:16 sc1
  s_waitcnt vmcnt(0)
.Lp0c%=:
  v_cmp_ne_u64_e32 vcc, s[82:83], v[246:247]
  s_sub_u32 s73, s73, 1
  s_cbranch_scc1 .Lp0b%=
  s_nop 3
  s_cbranch_vccnz .Lp0a%=
.Lp0b%=:
  s_mov_b64 exec, -1
  s_mov_b64 exec, 0xff
  s_mov_b32 s73, 0x20000
.Lp1a%=:
  global_load_dwordx2 v[246:247], v26, s[92:93] sc1
  s_waitcnt vmcnt(0)
.Lp1c%=:
  v_cmp_ne_u64_e32 vcc, s[82:83], v[246:247]
  s_sub_u32 s73, s73, 1
  s_cbranch_scc1 .Lp1b%=
  s_nop 3
  s_cbranch_vccnz .Lp1a%=
.Lp1b%=:
  s_mov_b64 exec, -1
  s_mov_b32 s39, 0
  s_add_u32 m0, s37, 0
  s_nop 0
  buffer_load_dwordx4 v11, s[20:23], s39 offen lds
  buffer_load_dwordx4 v12, s[20:23], s39 offen offset:1024 lds
  s_mov_b32 s43, 40960
  s_add_u32 m0, s37, 8192
  s_nop 0
  buffer_load_dwordx4 v11, s[20:23], s43 offen lds
  buffer_load_dwordx4 v12, s[20:23], s43 offen offset:1024 lds
  s_mov_b32 s39, 81920
  s_add_u32 m0, s37, 16384
  s_nop 0
  buffer_load_dwordx4 v11, s[20:23], s39 offen lds
  buffer_load_dwordx4 v12, s[20:23], s39 offen offset:1024 lds
  s_mov_b32 s43, 122880
  s_add_u32 m0, s37, 24576
  s_nop 0
  buffer_load_dwordx4 v11, s[20:23], s43 offen lds
  buffer_load_dwordx4 v12, s[20:23], s43 offen offset:1024 lds
  s_mov_b32 s39, 163840
  s_add_u32 m0, s37, 32768
  s_nop 0
  buffer_load_dwordx4 v11, s[20:23], s39 offen lds
  buffer_load_dwordx4 v12, s[20:23], s39 offen offset:1024 lds
  s_mov_b32 s43, 204800
  s_add_u32 m0, s37, 40960
  s_nop 0
  buffer_load_dwordx4 v11, s[20:23], s43 offen lds
  buffer_load_dwordx4 v12, s[20:23], s43 offen offset:1024 lds
  s_mov_b32 s41, 0
  buffer_load_dwordx4 v[32:35], v0, s[16:19], s41 offen sc1
  buffer_load_dwordx4 v[36:39], v13, s[16:19], s41 offen sc1
  buffer_load_dwordx4 v[40:43], v14, s[16:19], s41 offen sc1
  s_mov_b32 s42, 1024
  buffer_load_dwordx4 v[44:47], v0, s[16:19], s42 offen sc1
  buffer_load_dwordx4 v[48:51], v13, s[16:19], s42 offen sc1
  buffer_load_dwordx4 v[52:55], v14, s[16:19], s42 offen sc1
  s_mov_b32 s41, 2048
  buffer_load_dwordx4 v[56:59], v0, s[16:19], s41 offen sc1
  buffer_load_dwordx4 v[60:63], v13, s[16:19], s41 offen sc1
  buffer_load_dwordx4 v[64:67], v14, s[16:19], s41 offen sc1
  s_mov_b32 s42, 3072
  buffer_load_dwordx4 v[68:71], v0, s[16:19], s42 offen sc1
  buffer_load_dwordx4 v[72:75], v13, s[16:19], s42 offen sc1
  buffer_load_dwordx4 v[76:79], v14, s[16:19], s42 offen sc1
  s_mov_b32 s41, 4096
  buffer_load_dwordx4 v[80:83], v0, s[16:19], s41 offen sc1
  buffer_load_dwordx4 v[84:87], v13, s[16:19], s41 offen sc1
  buffer_load_dwordx4 v[88:91], v14, s[16:19], s41 offen sc1
  s_mov_b32 s42, 5120
  buffer_load_dwordx4 v[92:95], v0, s[16:19], s42 offen sc1
  buffer_load_dwordx4 v[96:99], v13, s[16:19], s42 offen sc1
  buffer_load_dwordx4 v[100:103], v14, s[16:19], s42 offen sc1
  v_add_u32_e32 v224, 0x10000, v3
  v_add_u32_e32 v225, 0x10000, v4
  v_add_u32_e32 v226, 0x10000, v5
  v_add_u32_e32 v227, 0x10000, v6
  v_add_u32_e32 v228, 0x10000, v7
  v_add_u32_e32 v229, 0x10000, v8
  v_add_u32_e32 v230, 0x10000, v9
  v_add_u32_e32 v231, 0x10000, v10
  s_waitcnt vmcnt(0) lgkmcnt(0)
  s_barrier
  ds_read_b64_tr_b16 a[192:193], v3
  ds_read_b64_tr_b16 a[194:195], v3 offset:4096
  ds_read_b64_tr_b16 a[196:197], v4
  ds_read_b64_tr_b16 a[198:199], v4 offset:4096
  ds_read_b64_tr_b16 a[200:201], v5
  ds_read_b64_tr_b16 a[202:203], v5 offset:4096
  ds_read_b64_tr_b16 a[204:205], v6
  ds_read_b64_tr_b16 a[206:207], v6 offset:4096
  ds_read_b64_tr_b16 a[208:209], v7
  ds_read_b64_tr_b16 a[210:211], v7 offset:4096
  ds_read_b64_tr_b16 a[212:213], v8
  ds_read_b64_tr_b16 a[214:215], v8 offset:4096
  ds_read_b64_tr_b16 a[216:217], v9
  ds_read_b64_tr_b16 a[218:219], v9 offset:4096
  ds_read_b64_tr_b16 a[220:221], v10
  ds_read_b64_tr_b16 a[222:223], v10 offset:4096
  s_waitcnt lgkmcnt(0)
  v_mfma_f32_16x16x32_bf16 v[128:131], a[192:195], v[32:35], 0
  ds_read_b64_tr_b16 a[224:225], v3 offset:8192
  v_mfma_f32_16x16x32_bf16 v[160:163], a[192:195], v[36:39], 0
  s_mov_b32 s39, 245760
  s_add_u32 m0, s37, 49152
  ds_read_b64_tr_b16 a[226:227], v3 offset:12288
  v_mfma_f32_16x16x32_bf16 v[192:195], a[192:195], v[40:43], 0
  buffer_load_dwordx4 v11, s[20:23], s39 offen lds
  v_mfma_f32_16x16x32_bf16 v[132:135], a[196:199], v[32:35], 0
  ds_read_b64_tr_b16 a[228:229], v4 offset:8192
  v_mfma_f32_16x16x32_bf16 v[164:167], a[196:199], v[36:39], 0
  ds_read_b64_tr_b16 a[230:231], v4 offset:12288
  v_mfma_f32_16x16x32_bf16 v[196:199], a[196:199], v[40:43], 0
  ds_read_b64_tr_b16 a[232:233], v5 offset:8192
  v_mfma_f32_16x16x32_bf16 v[136:139], a[200:203], v[32:35], 0
  buffer_load_dwordx4 v12, s[20:23], s39 offen offset:1024 lds
  v_mfma_f32_16x16x32_bf16 v[168:171], a[200:203], v[36:39], 0
  ds_read_b64_tr_b16 a[234:235], v5 offset:12288
  v_mfma_f32_16x16x32_bf16 v[200:203], a[200:203], v[40:43], 0
  ds_read_b64_tr_b16 a[236:237], v6 offset:8192
  v_mfma_f32_16x16x32_bf16 v[140:143], a[204:207], v[32:35], 0
  s_mov_b32 s43, 286720
  s_add_u32 m0, s37, 57344
  ds_read_b64_tr_b16 a[238:239], v6 offset:12288
  v_mfma_f32_16x16x32_bf16 v[172:175], a[204:207], v[36:39], 0
  buffer_load_dwordx4 v11, s[20:23], s43 offen lds
  v_mfma_f32_16x16x32_bf16 v[204:207], a[204:207], v[40:43], 0
  ds_read_b64_tr_b16 a[240:241], v7 offset:8192
  v_mfma_f32_16x16x32_bf16 v[144:147], a[208:211], v[32:35], 0
  ds_read_b64_tr_b16 a[242:243], v7 offset:12288
  v_mfma_f32_16x16x32_bf16 v[176:179], a[208:211], v[36:39], 0
  ds_read_b64_tr_b16 a[244:245], v8 offset:8192
  v_mfma_f32_16x16x32_bf16 v[208:211], a[208:211], v[40:43], 0
  buffer_load_dwordx4 v12, s[20:23], s43 offen offset:1024 lds
  v_mfma_f32_16x16x32_bf16 v[148:151], a[212:215], v[32:35], 0
  ds_read_b64_tr_b16 a[246:247], v8 offset:12288
  v_mfma_f32_16x16x32_bf16 v[180:183], a[212:215], v[36:39], 0
  ds_read_b64_tr_b16 a[248:249], v9 offset:8192
  v_mfma_f32_16x16x32_bf16 v[212:215], a[212:215], v[40:43], 0
  ds_read_b64_tr_b16 a[250:251], v9 offset:12288
  v_mfma_f32_16x16x32_bf16 v[152:155], a[216:219], v[32:35], 0
  s_mov_b32 s41, 6144
  ds_read_b64_tr_b16 a[252:253], v10 offset:8192
  v_mfma_f32_16x16x32_bf16 v[184:187], a[216:219], v[36:39], 0
  buffer_load_dwordx4 v[104:107], v0, s[16:19], s41 offen sc1
  v_mfma_f32_16x16x32_bf16 v[216:219], a[216:219], v[40:43], 0
  ds_read_b64_tr_b16 a[254:255], v10 offset:12288
  v_mfma_f32_16x16x32_bf16 v[156:159], a[220:223], v[32:35], 0
  v_mfma_f32_16x16x32_bf16 v[188:191], a[220:223], v[36:39], 0
  v_mfma_f32_16x16x32_bf16 v[220:223], a[220:223], v[40:43], 0
  s_waitcnt lgkmcnt(2)
  buffer_load_dwordx4 v[108:111], v13, s[16:19], s41 offen sc1
  v_mfma_f32_16x16x32_bf16 v[128:131], a[224:227], v[44:47], v[128:131]
  ds_read_b64_tr_b16 a[192:193], v3 offset:16384
  v_mfma_f32_16x16x32_bf16 v[160:163], a[224:227], v[48:51], v[160:163]
  ds_read_b64_tr_b16 a[194:195], v3 offset:20480
  v_mfma_f32_16x16x32_bf16 v[192:195], a[224:227], v[52:55], v[192:195]
  ds_read_b64_tr_b16 a[196:197], v4 offset:16384
  v_mfma_f32_16x16x32_bf16 v[132:135], a[228:231], v[44:47], v[132:135]
  buffer_load_dwordx4 v[112:115], v14, s[16:19], s41 offen sc1
  v_mfma_f32_16x16x32_bf16 v[164:167], a[228:231], v[48:51], v[164:167]
  ds_read_b64_tr_b16 a[198:199], v4 offset:20480
  v_mfma_f32_16x16x32_bf16 v[196:199], a[228:231], v[52:55], v[196:199]
  ds_read_b64_tr_b16 a[200:201], v5 offset:16384
  v_mfma_f32_16x16x32_bf16 v[136:139], a[232:235], v[44:47], v[136:139]
  ds_read_b64_tr_b16 a[202:203], v5 offset:20480
  v_mfma_f32_16x16x32_bf16 v[168:171], a[232:235], v[48:51], v[168:171]
  s_mov_b32 s42, 7168
  ds_read_b64_tr_b16 a[204:205], v6 offset:16384
  v_mfma_f32_16x16x32_bf16 v[200:203], a[232:235], v[52:55], v[200:203]
  buffer_load_dwordx4 v[116:119], v0, s[16:19], s42 offen sc1
  v_mfma_f32_16x16x32_bf16 v[140:143], a[236:239], v[44:47], v[140:143]
  ds_read_b64_tr_b16 a[206:207], v6 offset:20480
  v_mfma_f32_16x16x32_bf16 v[172:175], a[236:239], v[48:51], v[172:175]
  ds_read_b64_tr_b16 a[208:209], v7 offset:16384
  v_mfma_f32_16x16x32_bf16 v[204:207], a[236:239], v[52:55], v[204:207]
  ds_read_b64_tr_b16 a[210:211], v7 offset:20480
  v_mfma_f32_16x16x32_bf16 v[144:147], a[240:243], v[44:47], v[144:147]
  buffer_load_dwordx4 v[120:123], v13, s[16:19], s42 offen sc1
  v_mfma_f32_16x16x32_bf16 v[176:179], a[240:243], v[48:51], v[176:179]
  ds_read_b64_tr_b16 a[212:213], v8 offset:16384
  v_mfma_f32_16x16x32_bf16 v[208:211], a[240:243], v[52:55], v[208:211]
  ds_read_b64_tr_b16 a[214:215], v8 offset:20480
  v_mfma_f32_16x16x32_bf16 v[148:151], a[244:247], v[44:47], v[148:151]
  ds_read_b64_tr_b16 a[216:217], v9 offset:16384
  v_mfma_f32_16x16x32_bf16 v[180:183], a[244:247], v[48:51], v[180:183]
  buffer_load_dwordx4 v[124:127], v14, s[16:19], s42 offen sc1
  v_mfma_f32_16x16x32_bf16 v[212:215], a[244:247], v[52:55], v[212:215]
  ds_read_b64_tr_b16 a[218:219], v9 offset:20480
  v_mfma_f32_16x16x32_bf16 v[152:155], a[248:251], v[44:47], v[152:155]
  ds_read_b64_tr_b16 a[220:221], v10 offset:16384
  v_mfma_f32_16x16x32_bf16 v[184:187], a[248:251], v[48:51], v[184:187]
  ds_read_b64_tr_b16 a[222:223], v10 offset:20480
  v_mfma_f32_16x16x32_bf16 v[216:219], a[248:251], v[52:55], v[216:219]
  s_waitcnt lgkmcnt(4)
  v_mfma_f32_16x16x32_bf16 v[156:159], a[252:255], v[44:47], v[156:159]
  v_mfma_f32_16x16x32_bf16 v[188:191], a[252:255], v[48:51], v[188:191]
  v_mfma_f32_16x16x32_bf16 v[220:223], a[252:255], v[52:55], v[220:223]
  v_mfma_f32_16x16x32_bf16 v[128:131], a[192:195], v[56:59], v[128:131]
  ds_read_b64_tr_b16 a[224:225], v3 offset:24576
  v_mfma_f32_16x16x32_bf16 v[160:163], a[192:195], v[60:63], v[160:163]
  s_mov_b32 s39, 327680
  s_add_u32 m0, s37, 65536
  ds_read_b64_tr_b16 a[226:227], v3 offset:28672
  v_mfma_f32_16x16x32_bf16 v[192:195], a[192:195], v[64:67], v[192:195]
  buffer_load_dwordx4 v11, s[20:23], s39 offen lds
  v_mfma_f32_16x16x32_bf16 v[132:135], a[196:199], v[56:59], v[132:135]
  ds_read_b64_tr_b16 a[228:229], v4 offset:24576
  v_mfma_f32_16x16x32_bf16 v[164:167], a[196:199], v[60:63], v[164:167]
  ds_read_b64_tr_b16 a[230:231], v4 offset:28672
  v_mfma_f32_16x16x32_bf16 v[196:199], a[196:199], v[64:67], v[196:199]
  ds_read_b64_tr_b16 a[232:233], v5 offset:24576
  v_mfma_f32_16x16x32_bf16 v[136:139], a[200:203], v[56:59], v[136:139]
  buffer_load_dwordx4 v12, s[20:23], s39 offen offset:1024 lds
  v_mfma_f32_16x16x32_bf16 v[168:171], a[200:203], v[60:63], v[168:171]
  ds_read_b64_tr_b16 a[234:235], v5 offset:28672
  v_mfma_f32_16x16x32_bf16 v[200:203], a[200:203], v[64:67], v[200:203]
  ds_read_b64_tr_b16 a[236:237], v6 offset:24576
  v_mfma_f32_16x16x32_bf16 v[140:143], a[204:207], v[56:59], v[140:143]
  s_mov_b32 s43, 368640
  s_add_u32 m0, s37, 73728
  ds_read_b64_tr_b16 a[238:239], v6 offset:28672
  v_mfma_f32_16x16x32_bf16 v[172:175], a[204:207], v[60:63], v[172:175]
  buffer_load_dwordx4 v11, s[20:23], s43 offen lds
  v_mfma_f32_16x16x32_bf16 v[204:207], a[204:207], v[64:67], v[204:207]
  ds_read_b64_tr_b16 a[240:241], v7 offset:24576
  v_mfma_f32_16x16x32_bf16 v[144:147], a[208:211], v[56:59], v[144:147]
  ds_read_b64_tr_b16 a[242:243], v7 offset:28672
  v_mfma_f32_16x16x32_bf16 v[176:179], a[208:211], v[60:63], v[176:179]
  ds_read_b64_tr_b16 a[244:245], v8 offset:24576
  v_mfma_f32_16x16x32_bf16 v[208:211], a[208:211], v[64:67], v[208:211]
  buffer_load_dwordx4 v12, s[20:23], s43 offen offset:1024 lds
  v_mfma_f32_16x16x32_bf16 v[148:151], a[212:215], v[56:59], v[148:151]
  ds_read_b64_tr_b16 a[246:247], v8 offset:28672
  v_mfma_f32_16x16x32_bf16 v[180:183], a[212:215], v[60:63], v[180:183]
  ds_read_b64_tr_b16 a[248:249], v9 offset:24576
  v_mfma_f32_16x16x32_bf16 v[212:215], a[212:215], v[64:67], v[212:215]
  s_waitcnt lgkmcnt(4)
  ds_read_b64_tr_b16 a[250:251], v9 offset:28672
  v_mfma_f32_16x16x32_bf16 v[152:155], a[216:219], v[56:59], v[152:155]
  s_mov_b32 s41, 8192
  ds_read_b64_tr_b16 a[252:253], v10 offset:24576
  v_mfma_f32_16x16x32_bf16 v[184:187], a[216:219], v[60:63], v[184:187]
  buffer_load_dwordx4 a[0:3], v0, s[16:19], s41 offen sc1
  v_mfma_f32_16x16x32_bf16 v[216:219], a[216:219], v[64:67], v[216:219]
  ds_read_b64_tr_b16 a[254:255], v10 offset:28672
  v_mfma_f32_16x16x32_bf16 v[156:159], a[220:223], v[56:59], v[156:159]
  v_mfma_f32_16x16x32_bf16 v[188:191], a[220:223], v[60:63], v[188:191]
  v_mfma_f32_16x16x32_bf16 v[220:223], a[220:223], v[64:67], v[220:223]
  buffer_load_dwordx4 a[4:7], v13, s[16:19], s41 offen sc1
  v_mfma_f32_16x16x32_bf16 v[128:131], a[224:227], v[68:71], v[128:131]
  ds_read_b64_tr_b16 a[192:193], v3 offset:32768
  v_mfma_f32_16x16x32_bf16 v[160:163], a[224:227], v[72:75], v[160:163]
  ds_read_b64_tr_b16 a[194:195], v3 offset:36864
  v_mfma_f32_16x16x32_bf16 v[192:195], a[224:227], v[76:79], v[192:195]
  ds_read_b64_tr_b16 a[196:197], v4 offset:32768
  v_mfma_f32_16x16x32_bf16 v[132:135], a[228:231], v[68:71], v[132:135]
  buffer_load_dwordx4 a[8:11], v14, s[16:19], s41 offen sc1
  v_mfma_f32_16x16x32_bf16 v[164:167], a[228:231], v[72:75], v[164:167]
  ds_read_b64_tr_b16 a[198:199], v4 offset:36864
  v_mfma_f32_16x16x32_bf16 v[196:199], a[228:231], v[76:79], v[196:199]
  ds_read_b64_tr_b16 a[200:201], v5 offset:32768
  v_mfma_f32_16x16x32_bf16 v[136:139], a[232:235], v[68:71], v[136:139]
  ds_read_b64_tr_b16 a[202:203], v5 offset:36864
  v_mfma_f32_16x16x32_bf16 v[168:171], a[232:235], v[72:75], v[168:171]
  s_mov_b32 s42, 9216
  ds_read_b64_tr_b16 a[204:205], v6 offset:32768
  v_mfma_f32_16x16x32_bf16 v[200:203], a[232:235], v[76:79], v[200:203]
  buffer_load_dwordx4 a[12:15], v0, s[16:19], s42 offen sc1
  v_mfma_f32_16x16x32_bf16 v[140:143], a[236:239], v[68:71], v[140:143]
  ds_read_b64_tr_b16 a[206:207], v6 offset:36864
  v_mfma_f32_16x16x32_bf16 v[172:175], a[236:239], v[72:75], v[172:175]
  ds_read_b64_tr_b16 a[208:209], v7 offset:32768
  v_mfma_f32_16x16x32_bf16 v[204:207], a[236:239], v[76:79], v[204:207]
  s_waitcnt lgkmcnt(4)
  ds_read_b64_tr_b16 a[210:211], v7 offset:36864
  v_mfma_f32_16x16x32_bf16 v[144:147], a[240:243], v[68:71], v[144:147]
  buffer_load_dwordx4 a[16:19], v13, s[16:19], s42 offen sc1
  v_mfma_f32_16x16x32_bf16 v[176:179], a[240:243], v[72:75], v[176:179]
  ds_read_b64_tr_b16 a[212:213], v8 offset:32768
  v_mfma_f32_16x16x32_bf16 v[208:211], a[240:243], v[76:79], v[208:211]
  ds_read_b64_tr_b16 a[214:215], v8 offset:36864
  v_mfma_f32_16x16x32_bf16 v[148:151], a[244:247], v[68:71], v[148:151]
  ds_read_b64_tr_b16 a[216:217], v9 offset:32768
  v_mfma_f32_16x16x32_bf16 v[180:183], a[244:247], v[72:75], v[180:183]
  buffer_load_dwordx4 a[20:23], v14, s[16:19], s42 offen sc1
  v_mfma_f32_16x16x32_bf16 v[212:215], a[244:247], v[76:79], v[212:215]
  ds_read_b64_tr_b16 a[218:219], v9 offset:36864
  v_mfma_f32_16x16x32_bf16 v[152:155], a[248:251], v[68:71], v[152:155]
  ds_read_b64_tr_b16 a[220:221], v10 offset:32768
  v_mfma_f32_16x16x32_bf16 v[184:187], a[248:251], v[72:75], v[184:187]
  ds_read_b64_tr_b16 a[222:223], v10 offset:36864
  v_mfma_f32_16x16x32_bf16 v[216:219], a[248:251], v[76:79], v[216:219]
  v_mfma_f32_16x16x32_bf16 v[156:159], a[252:255], v[68:71], v[156:159]
  v_mfma_f32_16x16x32_bf16 v[188:191], a[252:255], v[72:75], v[188:191]
  v_mfma_f32_16x16x32_bf16 v[220:223], a[252:255], v[76:79], v[220:223]
  s_waitcnt vmcnt(16)
  s_barrier
  v_mfma_f32_16x16x32_bf16 v[128:131], a[192:195], v[80:83], v[128:131]
  ds_read_b64_tr_b16 a[224:225], v3 offset:40960
  v_mfma_f32_16x16x32_bf16 v[160:163], a[192:195], v[84:87], v[160:163]
  s_mov_b32 s39, 409600
  s_add_u32 m0, s37, 81920
  ds_read_b64_tr_b16 a[226:227], v3 offset:45056
  v_mfma_f32_16x16x32_bf16 v[192:195], a[192:195], v[88:91], v[192:195]
  buffer_load_dwordx4 v11, s[20:23], s39 offen lds
  v_mfma_f32_16x16x32_bf16 v[132:135], a[196:199], v[80:83], v[132:135]
  ds_read_b64_tr_b16 a[228:229], v4 offset:40960
  v_mfma_f32_16x16x32_bf16 v[164:167], a[196:199], v[84:87], v[164:167]
  ds_read_b64_tr_b16 a[230:231], v4 offset:45056
  v_mfma_f32_16x16x32_bf16 v[196:199], a[196:199], v[88:91], v[196:199]
  s_waitcnt lgkmcnt(4)
  ds_read_b64_tr_b16 a[232:233], v5 offset:40960
  v_mfma_f32_16x16x32_bf16 v[136:139], a[200:203], v[80:83], v[136:139]
  buffer_load_dwordx4 v12, s[20:23], s39 offen offset:1024 lds
  v_mfma_f32_16x16x32_bf16 v[168:171], a[200:203], v[84:87], v[168:171]
  ds_read_b64_tr_b16 a[234:235], v5 offset:45056
  v_mfma_f32_16x16x32_bf16 v[200:203], a[200:203], v[88:91], v[200:203]
  ds_read_b64_tr_b16 a[236:237], v6 offset:40960
  v_mfma_f32_16x16x32_bf16 v[140:143], a[204:207], v[80:83], v[140:143]
  s_mov_b32 s43, 450560
  s_add_u32 m0, s37, 90112
  ds_read_b64_tr_b16 a[238:239], v6 offset:45056
  v_mfma_f32_16x16x32_bf16 v[172:175], a[204:207], v[84:87], v[172:175]
  buffer_load_dwordx4 v11, s[20:23], s43 offen lds
  v_mfma_f32_16x16x32_bf16 v[204:207], a[204:207], v[88:91], v[204:207]
  ds_read_b64_tr_b16 a[240:241], v7 offset:40960
  v_mfma_f32_16x16x32_bf16 v[144:147], a[208:211], v[80:83], v[144:147]
  ds_read_b64_tr_b16 a[242:243], v7 offset:45056
  v_mfma_f32_16x16x32_bf16 v[176:179], a[208:211], v[84:87], v[176:179]
  ds_read_b64_tr_b16 a[244:245], v8 offset:40960
  v_mfma_f32_16x16x32_bf16 v[208:211], a[208:211], v[88:91], v[208:211]
  buffer_load_dwordx4 v12, s[20:23], s43 offen offset:1024 lds
  v_mfma_f32_16x16x32_bf16 v[148:151], a[212:215], v[80:83], v[148:151]
  ds_read_b64_tr_b16 a[246:247], v8 offset:45056
  v_mfma_f32_16x16x32_bf16 v[180:183], a[212:215], v[84:87], v[180:183]
  ds_read_b64_tr_b16 a[248:249], v9 offset:40960
  v_mfma_f32_16x16x32_bf16 v[212:215], a[212:215], v[88:91], v[212:215]
  ds_read_b64_tr_b16 a[250:251], v9 offset:45056
  v_mfma_f32_16x16x32_bf16 v[152:155], a[216:219], v[80:83], v[152:155]
  s_mov_b32 s41, 10240
  ds_read_b64_tr_b16 a[252:253], v10 offset:40960
  v_mfma_f32_16x16x32_bf16 v[184:187], a[216:219], v[84:87], v[184:187]
  buffer_load_dwordx4 a[24:27], v0, s[16:19], s41 offen sc1
  v_mfma_f32_16x16x32_bf16 v[216:219], a[216:219], v[88:91], v[216:219]
  ds_read_b64_tr_b16 a[254:255], v10 offset:45056
  v_mfma_f32_16x16x32_bf16 v[156:159], a[220:223], v[80:83], v[156:159]
  v_mfma_f32_16x16x32_bf16 v[188:191], a[220:223], v[84:87], v[188:191]
  v_mfma_f32_16x16x32_bf16 v[220:223], a[220:223], v[88:91], v[220:223]
  s_waitcnt lgkmcnt(2)
  buffer_load_dwordx4 a[28:31], v13, s[16:19], s41 offen sc1
  v_mfma_f32_16x16x32_bf16 v[128:131], a[224:227], v[92:95], v[128:131]
  ds_read_b64_tr_b16 a[192:193], v3 offset:49152
  v_mfma_f32_16x16x32_bf16 v[160:163], a[224:227], v[96:99], v[160:163]
  ds_read_b64_tr_b16 a[194:195], v3 offset:53248
  v_mfma_f32_16x16x32_bf16 v[192:195], a[224:227], v[100:103], v[192:195]
  ds_read_b64_tr_b16 a[196:197], v4 offset:49152
  v_mfma_f32_16x16x32_bf16 v[132:135], a[228:231], v[92:95], v[132:135]
  buffer_load_dwordx4 a[32:35], v14, s[16:19], s41 offen sc1
  v_mfma_f32_16x16x32_bf16 v[164:167], a[228:231], v[96:99], v[164:167]
  ds_read_b64_tr_b16 a[198:199], v4 offset:53248
  v_mfma_f32_16x16x32_bf16 v[196:199], a[228:231], v[100:103], v[196:199]
  ds_read_b64_tr_b16 a[200:201], v5 offset:49152
  v_mfma_f32_16x16x32_bf16 v[136:139], a[232:235], v[92:95], v[136:139]
  ds_read_b64_tr_b16 a[202:203], v5 offset:53248
  v_mfma_f32_16x16x32_bf16 v[168:171], a[232:235], v[96:99], v[168:171]
  s_mov_b32 s42, 11264
  ds_read_b64_tr_b16 a[204:205], v6 offset:49152
  v_mfma_f32_16x16x32_bf16 v[200:203], a[232:235], v[100:103], v[200:203]
  buffer_load_dwordx4 a[36:39], v0, s[16:19], s42 offen sc1
  v_mfma_f32_16x16x32_bf16 v[140:143], a[236:239], v[92:95], v[140:143]
  ds_read_b64_tr_b16 a[206:207], v6 offset:53248
  v_mfma_f32_16x16x32_bf16 v[172:175], a[236:239], v[96:99], v[172:175]
  ds_read_b64_tr_b16 a[208:209], v7 offset:49152
  v_mfma_f32_16x16x32_bf16 v[204:207], a[236:239], v[100:103], v[204:207]
  ds_read_b64_tr_b16 a[210:211], v7 offset:53248
  v_mfma_f32_16x16x32_bf16 v[144:147], a[240:243], v[92:95], v[144:147]
  buffer_load_dwordx4 a[40:43], v13, s[16:19], s42 offen sc1
  v_mfma_f32_16x16x32_bf16 v[176:179], a[240:243], v[96:99], v[176:179]
  ds_read_b64_tr_b16 a[212:213], v8 offset:49152
  v_mfma_f32_16x16x32_bf16 v[208:211], a[240:243], v[100:103], v[208:211]
  ds_read_b64_tr_b16 a[214:215], v8 offset:53248
  v_mfma_f32_16x16x32_bf16 v[148:151], a[244:247], v[92:95], v[148:151]
  ds_read_b64_tr_b16 a[216:217], v9 offset:49152
  v_mfma_f32_16x16x32_bf16 v[180:183], a[244:247], v[96:99], v[180:183]
  buffer_load_dwordx4 a[44:47], v14, s[16:19], s42 offen sc1
  v_mfma_f32_16x16x32_bf16 v[212:215], a[244:247], v[100:103], v[212:215]
  ds_read_b64_tr_b16 a[218:219], v9 offset:53248
  v_mfma_f32_16x16x32_bf16 v[152:155], a[248:251], v[92:95], v[152:155]
  ds_read_b64_tr_b16 a[220:221], v10 offset:49152
  v_mfma_f32_16x16x32_bf16 v[184:187], a[248:251], v[96:99], v[184:187]
  ds_read_b64_tr_b16 a[222:223], v10 offset:53248
  v_mfma_f32_16x16x32_bf16 v[216:219], a[248:251], v[100:103], v[216:219]
  s_waitcnt lgkmcnt(4)
  v_mfma_f32_16x16x32_bf16 v[156:159], a[252:255], v[92:95], v[156:159]
  v_mfma_f32_16x16x32_bf16 v[188:191], a[252:255], v[96:99], v[188:191]
  v_mfma_f32_16x16x32_bf16 v[220:223], a[252:255], v[100:103], v[220:223]
  s_waitcnt vmcnt(16)
  s_barrier
  v_mfma_f32_16x16x32_bf16 v[128:131], a[192:195], v[104:107], v[128:131]
  ds_read_b64_tr_b16 a[224:225], v3 offset:57344
  v_mfma_f32_16x16x32_bf16 v[160:163], a[192:195], v[108:111], v[160:163]
  s_mov_b32 s39, 491520
  s_add_u32 m0, s37, 98304
  ds_read_b64_tr_b16 a[226:227], v3 offset:61440
  v_mfma_f32_16x16x32_bf16 v[192:195], a[192:195], v[112:115], v[192:195]
  buffer_load_dwordx4 v11, s[20:23], s39 offen lds
  v_mfma_f32_16x16x32_bf16 v[132:135], a[196:199], v[104:107], v[132:135]
  ds_read_b64_tr_b16 a[228:229], v4 offset:57344
  v_mfma_f32_16x16x32_bf16 v[164:167], a[196:199], v[108:111], v[164:167]
  ds_read_b64_tr_b16 a[230:231], v4 offset:61440
  v_mfma_f32_16x16x32_bf16 v[196:199], a[196:199], v[112:115], v[196:199]
  ds_read_b64_tr_b16 a[232:233], v5 offset:57344
  v_mfma_f32_16x16x32_bf16 v[136:139], a[200:203], v[104:107], v[136:139]
  buffer_load_dwordx4 v12, s[20:23], s39 offen offset:1024 lds
  v_mfma_f32_16x16x32_bf16 v[168:171], a[200:203], v[108:111], v[168:171]
  ds_read_b64_tr_b16 a[234:235], v5 offset:61440
  v_mfma_f32_16x16x32_bf16 v[200:203], a[200:203], v[112:115], v[200:203]
  ds_read_b64_tr_b16 a[236:237], v6 offset:57344
  v_mfma_f32_16x16x32_bf16 v[140:143], a[204:207], v[104:107], v[140:143]
  s_mov_b32 s43, 532480
  s_add_u32 m0, s37, 106496
  ds_read_b64_tr_b16 a[238:239], v6 offset:61440
  v_mfma_f32_16x16x32_bf16 v[172:175], a[204:207], v[108:111], v[172:175]
  buffer_load_dwordx4 v11, s[20:23], s43 offen lds
  v_mfma_f32_16x16x32_bf16 v[204:207], a[204:207], v[112:115], v[204:207]
  ds_read_b64_tr_b16 a[240:241], v7 offset:57344
  v_mfma_f32_16x16x32_bf16 v[144:147], a[208:211], v[104:107], v[144:147]
  ds_read_b64_tr_b16 a[242:243], v7 offset:61440
  v_mfma_f32_16x16x32_bf16 v[176:179], a[208:211], v[108:111], v[176:179]
  ds_read_b64_tr_b16 a[244:245], v8 offset:57344
  v_mfma_f32_16x16x32_bf16 v[208:211], a[208:211], v[112:115], v[208:211]
  buffer_load_dwordx4 v12, s[20:23], s43 offen offset:1024 lds
  v_mfma_f32_16x16x32_bf16 v[148:151], a[212:215], v[104:107], v[148:151]
  ds_read_b64_tr_b16 a[246:247], v8 offset:61440
  v_mfma_f32_16x16x32_bf16 v[180:183], a[212:215], v[108:111], v[180:183]
  ds_read_b64_tr_b16 a[248:249], v9 offset:57344
  v_mfma_f32_16x16x32_bf16 v[212:215], a[212:215], v[112:115], v[212:215]
  s_waitcnt lgkmcnt(4)
  ds_read_b64_tr_b16 a[250:251], v9 offset:61440
  v_mfma_f32_16x16x32_bf16 v[152:155], a[216:219], v[104:107], v[152:155]
  s_mov_b32 s41, 12288
  ds_read_b64_tr_b16 a[252:253], v10 offset:57344
  v_mfma_f32_16x16x32_bf16 v[184:187], a[216:219], v[108:111], v[184:187]
  buffer_load_dwordx4 a[48:51], v0, s[16:19], s41 offen sc1
  v_mfma_f32_16x16x32_bf16 v[216:219], a[216:219], v[112:115], v[216:219]
  ds_read_b64_tr_b16 a[254:255], v10 offset:61440
  v_mfma_f32_16x16x32_bf16 v[156:159], a[220:223], v[104:107], v[156:159]
  v_mfma_f32_16x16x32_bf16 v[188:191], a[220:223], v[108:111], v[188:191]
  v_mfma_f32_16x16x32_bf16 v[220:223], a[220:223], v[112:115], v[220:223]
  buffer_load_dwordx4 a[52:55], v13, s[16:19], s41 offen sc1
  v_mfma_f32_16x16x32_bf16 v[128:131], a[224:227], v[116:119], v[128:131]
  ds_read_b64_tr_b16 a[192:193], v224
  v_mfma_f32_16x16x32_bf16 v[160:163], a[224:227], v[120:123], v[160:163]
  ds_read_b64_tr_b16 a[194:195], v224 offset:4096
  v_mfma_f32_16x16x32_bf16 v[192:195], a[224:227], v[124:127], v[192:195]
  ds_read_b64_tr_b16 a[196:197], v225
  v_mfma_f32_16x16x32_bf16 v[132:135], a[228:231], v[116:119], v[132:135]
  buffer_load_dwordx4 a[56:59], v14, s[16:19], s41 offen sc1
  v_mfma_f32_16x16x32_bf16 v[164:167], a[228:231], v[120:123], v[164:167]
  ds_read_b64_tr_b16 a[198:199], v225 offset:4096
  v_mfma_f32_16x16x32_bf16 v[196:199], a[228:231], v[124:127], v[196:199]
  ds_read_b64_tr_b16 a[200:201], v226
  v_mfma_f32_16x16x32_bf16 v[136:139], a[232:235], v[116:119], v[136:139]
  ds_read_b64_tr_b16 a[202:203], v226 offset:4096
  v_mfma_f32_16x16x32_bf16 v[168:171], a[232:235], v[120:123], v[168:171]
  s_mov_b32 s42, 13312
  ds_read_b64_tr_b16 a[204:205], v227
  v_mfma_f32_16x16x32_bf16 v[200:203], a[232:235], v[124:127], v[200:203]
  buffer_load_dwordx4 a[60:63], v0, s[16:19], s42 offen sc1
  v_mfma_f32_16x16x32_bf16 v[140:143], a[236:239], v[116:119], v[140:143]
  ds_read_b64_tr_b16 a[206:207], v227 offset:4096
  v_mfma_f32_16x16x32_bf16 v[172:175], a[236:239], v[120:123], v[172:175]
  ds_read_b64_tr_b16 a[208:209], v228
  v_mfma_f32_16x16x32_bf16 v[204:207], a[236:239], v[124:127], v[204:207]
  s_waitcnt lgkmcnt(4)
  ds_read_b64_tr_b16 a[210:211], v228 offset:4096
  v_mfma_f32_16x16x32_bf16 v[144:147], a[240:243], v[116:119], v[144:147]
  buffer_load_dwordx4 a[64:67], v13, s[16:19], s42 offen sc1
  v_mfma_f32_16x16x32_bf16 v[176:179], a[240:243], v[120:123], v[176:179]
  ds_read_b64_tr_b16 a[212:213], v229
  v_mfma_f32_16x16x32_bf16 v[208:211], a[240:243], v[124:127], v[208:211]
  ds_read_b64_tr_b16 a[214:215], v229 offset:4096
  v_mfma_f32_16x16x32_bf16 v[148:151], a[244:247], v[116:119], v[148:151]
  ds_read_b64_tr_b16 a[216:217], v230
  v_mfma_f32_16x16x32_bf16 v[180:183], a[244:247], v[120:123], v[180:183]
  buffer_load_dwordx4 a[68:71], v14, s[16:19], s42 offen sc1
  v_mfma_f32_16x16x32_bf16 v[212:215], a[244:247], v[124:127], v[212:215]
  ds_read_b64_tr_b16 a[218:219], v230 offset:4096
  v_mfma_f32_16x16x32_bf16 v[152:155], a[248:251], v[116:119], v[152:155]
  ds_read_b64_tr_b16 a[220:221], v231
  v_mfma_f32_16x16x32_bf16 v[184:187], a[248:251], v[120:123], v[184:187]
  ds_read_b64_tr_b16 a[222:223], v231 offset:4096
  v_mfma_f32_16x16x32_bf16 v[216:219], a[248:251], v[124:127], v[216:219]
  v_mfma_f32_16x16x32_bf16 v[156:159], a[252:255], v[116:119], v[156:159]
  v_mfma_f32_16x16x32_bf16 v[188:191], a[252:255], v[120:123], v[188:191]
  v_mfma_f32_16x16x32_bf16 v[220:223], a[252:255], v[124:127], v[220:223]
  s_waitcnt vmcnt(16)
  s_barrier
  v_mfma_f32_16x16x32_bf16 v[128:131], a[192:195], a[0:3], v[128:131]
  ds_read_b64_tr_b16 a[224:225], v224 offset:8192
  v_mfma_f32_16x16x32_bf16 v[160:163], a[192:195], a[4:7], v[160:163]
  s_mov_b32 s39, 573440
  s_add_u32 m0, s37, 114688
  ds_read_b64_tr_b16 a[226:227], v224 offset:12288
  v_mfma_f32_16x16x32_bf16 v[192:195], a[192:195], a[8:11], v[192:195]
  buffer_load_dwordx4 v11, s[20:23], s39 offen lds
  v_mfma_f32_16x16x32_bf16 v[132:135], a[196:199], a[0:3], v[132:135]
  ds_read_b64_tr_b16 a[228:229], v225 offset:8192
  v_mfma_f32_16x16x32_bf16 v[164:167], a[196:199], a[4:7], v[164:167]
  ds_read_b64_tr_b16 a[230:231], v225 offset:12288
  v_mfma_f32_16x16x32_bf16 v[196:199], a[196:199], a[8:11], v[196:199]
  s_waitcnt lgkmcnt(4)
  ds_read_b64_tr_b16 a[232:233], v226 offset:8192
  v_mfma_f32_16x16x32_bf16 v[136:139], a[200:203], a[0:3], v[136:139]
  buffer_load_dwordx4 v12, s[20:23], s39 offen offset:1024 lds
  v_mfma_f32_16x16x32_bf16 v[168:171], a[200:203], a[4:7], v[168:171]
  ds_read_b64_tr_b16 a[234:235], v226 offset:12288
  v_mfma_f32_16x16x32_bf16 v[200:203], a[200:203], a[8:11], v[200:203]
  ds_read_b64_tr_b16 a[236:237], v227 offset:8192
  v_mfma_f32_16x16x32_bf16 v[140:143], a[204:207], a[0:3], v[140:143]
  s_mov_b32 s43, 614400
  s_add_u32 m0, s37, 122880
  ds_read_b64_tr_b16 a[238:239], v227 offset:12288
  v_mfma_f32_16x16x32_bf16 v[172:175], a[204:207], a[4:7], v[172:175]
  buffer_load_dwordx4 v11, s[20:23], s43 offen lds
  v_mfma_f32_16x16x32_bf16 v[204:207], a[204:207], a[8:11], v[204:207]
  ds_read_b64_tr_b16 a[240:241], v228 offset:8192
  v_mfma_f32_16x16x32_bf16 v[144:147], a[208:211], a[0:3], v[144:147]
  ds_read_b64_tr_b16 a[242:243], v228 offset:12288
  v_mfma_f32_16x16x32_bf16 v[176:179], a[208:211], a[4:7], v[176:179]
  ds_read_b64_tr_b16 a[244:245], v229 offset:8192
  v_mfma_f32_16x16x32_bf16 v[208:211], a[208:211], a[8:11], v[208:211]
  buffer_load_dwordx4 v12, s[20:23], s43 offen offset:1024 lds
  v_mfma_f32_16x16x32_bf16 v[148:151], a[212:215], a[0:3], v[148:151]
  ds_read_b64_tr_b16 a[246:247], v229 offset:12288
  v_mfma_f32_16x16x32_bf16 v[180:183], a[212:215], a[4:7], v[180:183]
  ds_read_b64_tr_b16 a[248:249], v230 offset:8192
  v_mfma_f32_16x16x32_bf16 v[212:215], a[212:215], a[8:11], v[212:215]
  ds_read_b64_tr_b16 a[250:251], v230 offset:12288
  v_mfma_f32_16x16x32_bf16 v[152:155], a[216:219], a[0:3], v[152:155]
  s_mov_b32 s41, 14336
  ds_read_b64_tr_b16 a[252:253], v231 offset:8192
  v_mfma_f32_16x16x32_bf16 v[184:187], a[216:219], a[4:7], v[184:187]
  buffer_load_dwordx4 a[72:75], v0, s[16:19], s41 offen sc1
  v_mfma_f32_16x16x32_bf16 v[216:219], a[216:219], a[8:11], v[216:219]
  ds_read_b64_tr_b16 a[254:255], v231 offset:12288
  v_mfma_f32_16x16x32_bf16 v[156:159], a[220:223], a[0:3], v[156:159]
  v_mfma_f32_16x16x32_bf16 v[188:191], a[220:223], a[4:7], v[188:191]
  v_mfma_f32_16x16x32_bf16 v[220:223], a[220:223], a[8:11], v[220:223]
  s_waitcnt lgkmcnt(2)
  buffer_load_dwordx4 a[76:79], v13, s[16:19], s41 offen sc1
  v_mfma_f32_16x16x32_bf16 v[128:131], a[224:227], a[12:15], v[128:131]
  ds_read_b64_tr_b16 a[192:193], v224 offset:16384
  v_mfma_f32_16x16x32_bf16 v[160:163], a[224:227], a[16:19], v[160:163]
  ds_read_b64_tr_b16 a[194:195], v224 offset:20480
  v_mfma_f32_16x16x32_bf16 v[192:195], a[224:227], a[20:23], v[192:195]
  ds_read_b64_tr_b16 a[196:197], v225 offset:16384
  v_mfma_f32_16x16x32_bf16 v[132:135], a[228:231], a[12:15], v[132:135]
  buffer_load_dwordx4 a[80:83], v14, s[16:19], s41 offen sc1
  v_mfma_f32_16x16x32_bf16 v[164:167], a[228:231], a[16:19], v[164:167]
  ds_read_b64_tr_b16 a[198:199], v225 offset:20480
  v_mfma_f32_16x16x32_bf16 v[196:199], a[228:231], a[20:23], v[196:199]
  ds_read_b64_tr_b16 a[200:201], v226 offset:16384
  v_mfma_f32_16x16x32_bf16 v[136:139], a[232:235], a[12:15], v[136:139]
  ds_read_b64_tr_b16 a[202:203], v226 offset:20480
  v_mfma_f32_16x16x32_bf16 v[168:171], a[232:235], a[16:19], v[168:171]
  s_mov_b32 s42, 15360
  ds_read_b64_tr_b16 a[204:205], v227 offset:16384
  v_mfma_f32_16x16x32_bf16 v[200:203], a[232:235], a[20:23], v[200:203]
  buffer_load_dwordx4 a[84:87], v0, s[16:19], s42 offen sc1
  v_mfma_f32_16x16x32_bf16 v[140:143], a[236:239], a[12:15], v[140:143]
  ds_read_b64_tr_b16 a[206:207], v227 offset:20480
  v_mfma_f32_16x16x32_bf16 v[172:175], a[236:239], a[16:19], v[172:175]
  ds_read_b64_tr_b16 a[208:209], v228 offset:16384
  v_mfma_f32_16x16x32_bf16 v[204:207], a[236:239], a[20:23], v[204:207]
  ds_read_b64_tr_b16 a[210:211], v228 offset:20480
  v_mfma_f32_16x16x32_bf16 v[144:147], a[240:243], a[12:15], v[144:147]
  buffer_load_dwordx4 a[88:91], v13, s[16:19], s42 offen sc1
  v_mfma_f32_16x16x32_bf16 v[176:179], a[240:243], a[16:19], v[176:179]
  ds_read_b64_tr_b16 a[212:213], v229 offset:16384
  v_mfma_f32_16x16x32_bf16 v[208:211], a[240:243], a[20:23], v[208:211]
  ds_read_b64_tr_b16 a[214:215], v229 offset:20480
  v_mfma_f32_16x16x32_bf16 v[148:151], a[244:247], a[12:15], v[148:151]
  ds_read_b64_tr_b16 a[216:217], v230 offset:16384
  v_mfma_f32_16x16x32_bf16 v[180:183], a[244:247], a[16:19], v[180:183]
  buffer_load_dwordx4 a[92:95], v14, s[16:19], s42 offen sc1
  v_mfma_f32_16x16x32_bf16 v[212:215], a[244:247], a[20:23], v[212:215]
  ds_read_b64_tr_b16 a[218:219], v230 offset:20480
  v_mfma_f32_16x16x32_bf16 v[152:155], a[248:251], a[12:15], v[152:155]
  ds_read_b64_tr_b16 a[220:221], v231 offset:16384
  v_mfma_f32_16x16x32_bf16 v[184:187], a[248:251], a[16:19], v[184:187]
  ds_read_b64_tr_b16 a[222:223], v231 offset:20480
  v_mfma_f32_16x16x32_bf16 v[216:219], a[248:251], a[20:23], v[216:219]
  s_waitcnt lgkmcnt(4)
  v_mfma_f32_16x16x32_bf16 v[156:159], a[252:255], a[12:15], v[156:159]
  v_mfma_f32_16x16x32_bf16 v[188:191], a[252:255], a[16:19], v[188:191]
  v_mfma_f32_16x16x32_bf16 v[220:223], a[252:255], a[20:23], v[220:223]
  s_waitcnt vmcnt(16)
  s_barrier
  v_mfma_f32_16x16x32_bf16 v[128:131], a[192:195], a[24:27], v[128:131]
  ds_read_b64_tr_b16 a[224:225], v224 offset:24576
  v_mfma_f32_16x16x32_bf16 v[160:163], a[192:195], a[28:31], v[160:163]
  s_mov_b32 s39, 655360
  s_add_u32 m0, s37, 0
  ds_read_b64_tr_b16 a[226:227], v224 offset:28672
  v_mfma_f32_16x16x32_bf16 v[192:195], a[192:195], a[32:35], v[192:195]
  buffer_load_dwordx4 v11, s[20:23], s39 offen lds
  v_mfma_f32_16x16x32_bf16 v[132:135], a[196:199], a[24:27], v[132:135]
  ds_read_b64_tr_b16 a[228:229], v225 offset:24576
  v_mfma_f32_16x16x32_bf16 v[164:167], a[196:199], a[28:31], v[164:167]
  ds_read_b64_tr_b16 a[230:231], v225 offset:28672
  v_mfma_f32_16x16x32_bf16 v[196:199], a[196:199], a[32:35], v[196:199]
  ds_read_b64_tr_b16 a[232:233], v226 offset:24576
  v_mfma_f32_16x16x32_bf16 v[136:139], a[200:203], a[24:27], v[136:139]
  buffer_load_dwordx4 v12, s[20:23], s39 offen offset:1024 lds
  v_mfma_f32_16x16x32_bf16 v[168:171], a[200:203], a[28:31], v[168:171]
  ds_read_b64_tr_b16 a[234:235], v226 offset:28672
  v_mfma_f32_16x16x32_bf16 v[200:203], a[200:203], a[32:35], v[200:203]
  ds_read_b64_tr_b16 a[236:237], v227 offset:24576
  v_mfma_f32_16x16x32_bf16 v[140:143], a[204:207], a[24:27], v[140:143]
  s_mov_b32 s43, 696320
  s_add_u32 m0, s37, 8192
  ds_read_b64_tr_b16 a[238:239], v227 offset:28672
  v_mfma_f32_16x16x32_bf16 v[172:175], a[204:207], a[28:31], v[172:175]
  buffer_load_dwordx4 v11, s[20:23], s43 offen lds
  v_mfma_f32_16x16x32_bf16 v[204:207], a[204:207], a[32:35], v[204:207]
  ds_read_b64_tr_b16 a[240:241], v228 offset:24576
  v_mfma_f32_16x16x32_bf16 v[144:147], a[208:211], a[24:27], v[144:147]
  ds_read_b64_tr_b16 a[242:243], v228 offset:28672
  v_mfma_f32_16x16x32_bf16 v[176:179], a[208:211], a[28:31], v[176:179]
  ds_read_b64_tr_b16 a[244:245], v229 offset:24576
  v_mfma_f32_16x16x32_bf16 v[208:211], a[208:211], a[32:35], v[208:211]
  buffer_load_dwordx4 v12, s[20:23], s43 offen offset:1024 lds
  v_mfma_f32_16x16x32_bf16 v[148:151], a[212:215], a[24:27], v[148:151]
  ds_read_b64_tr_b16 a[246:247], v229 offset:28672
  v_mfma_f32_16x16x32_bf16 v[180:183], a[212:215], a[28:31], v[180:183]
  ds_read_b64_tr_b16 a[248:249], v230 offset:24576
  v_mfma_f32_16x16x32_bf16 v[212:215], a[212:215], a[32:35], v[212:215]
  s_waitcnt lgkmcnt(4)
  ds_read_b64_tr_b16 a[250:251], v230 offset:28672
  v_mfma_f32_16x16x32_bf16 v[152:155], a[216:219], a[24:27], v[152:155]
  s_mov_b32 s41, 16384
  ds_read_b64_tr_b16 a[252:253], v231 offset:24576
  v_mfma_f32_16x16x32_bf16 v[184:187], a[216:219], a[28:31], v[184:187]
  buffer_load_dwordx4 v[32:35], v0, s[16:19], s41 offen sc1
  v_mfma_f32_16x16x32_bf16 v[216:219], a[216:219], a[32:35], v[216:219]
  ds_read_b64_tr_b16 a[254:255], v231 offset:28672
  v_mfma_f32_16x16x32_bf16 v[156:159], a[220:223], a[24:27], v[156:159]
  v_mfma_f32_16x16x32_bf16 v[188:191], a[220:223], a[28:31], v[188:191]
  v_mfma_f32_16x16x32_bf16 v[220:223], a[220:223], a[32:35], v[220:223]
  buffer_load_dwordx4 v[36:39], v13, s[16:19], s41 offen sc1
  v_mfma_f32_16x16x32_bf16 v[128:131], a[224:227], a[36:39], v[128:131]
  ds_read_b64_tr_b16 a[192:193], v224 offset:32768
  v_mfma_f32_16x16x32_bf16 v[160:163], a[224:227], a[40:43], v[160:163]
  ds_read_b64_tr_b16 a[194:195], v224 offset:36864
  v_mfma_f32_16x16x32_bf16 v[192:195], a[224:227], a[44:47], v[192:195]
  ds_read_b64_tr_b16 a[196:197], v225 offset:32768
  v_mfma_f32_16x16x32_bf16 v[132:135], a[228:231], a[36:39], v[132:135]
  buffer_load_dwordx4 v[40:43], v14, s[16:19], s41 offen sc1
  v_mfma_f32_16x16x32_bf16 v[164:167], a[228:231], a[40:43], v[164:167]
  ds_read_b64_tr_b16 a[198:199], v225 offset:36864
  v_mfma_f32_16x16x32_bf16 v[196:199], a[228:231], a[44:47], v[196:199]
  ds_read_b64_tr_b16 a[200:201], v226 offset:32768
  v_mfma_f32_16x16x32_bf16 v[136:139], a[232:235], a[36:39], v[136:139]
  ds_read_b64_tr_b16 a[202:203], v226 offset:36864
  v_mfma_f32_16x16x32_bf16 v[168:171], a[232:235], a[40:43], v[168:171]
  s_mov_b32 s42, 17408
  ds_read_b64_tr_b16 a[204:205], v227 offset:32768
  v_mfma_f32_16x16x32_bf16 v[200:203], a[232:235], a[44:47], v[200:203]
  buffer_load_dwordx4 v[44:47], v0, s[16:19], s42 offen sc1
  v_mfma_f32_16x16x32_bf16 v[140:143], a[236:239], a[36:39], v[140:143]
  ds_read_b64_tr_b16 a[206:207], v227 offset:36864
  v_mfma_f32_16x16x32_bf16 v[172:175], a[236:239], a[40:43], v[172:175]
  ds_read_b64_tr_b16 a[208:209], v228 offset:32768
  v_mfma_f32_16x16x32_bf16 v[204:207], a[236:239], a[44:47], v[204:207]
  s_waitcnt lgkmcnt(4)
  ds_read_b64_tr_b16 a[210:211], v228 offset:36864
  v_mfma_f32_16x16x32_bf16 v[144:147], a[240:243], a[36:39], v[144:147]
  buffer_load_dwordx4 v[48:51], v13, s[16:19], s42 offen sc1
  v_mfma_f32_16x16x32_bf16 v[176:179], a[240:243], a[40:43], v[176:179]
  ds_read_b64_tr_b16 a[212:213], v229 offset:32768
  v_mfma_f32_16x16x32_bf16 v[208:211], a[240:243], a[44:47], v[208:211]
  ds_read_b64_tr_b16 a[214:215], v229 offset:36864
  v_mfma_f32_16x16x32_bf16 v[148:151], a[244:247], a[36:39], v[148:151]
  ds_read_b64_tr_b16 a[216:217], v230 offset:32768
  v_mfma_f32_16x16x32_bf16 v[180:183], a[244:247], a[40:43], v[180:183]
  buffer_load_dwordx4 v[52:55], v14, s[16:19], s42 offen sc1
  v_mfma_f32_16x16x32_bf16 v[212:215], a[244:247], a[44:47], v[212:215]
  ds_read_b64_tr_b16 a[218:219], v230 offset:36864
  v_mfma_f32_16x16x32_bf16 v[152:155], a[248:251], a[36:39], v[152:155]
  ds_read_b64_tr_b16 a[220:221], v231 offset:32768
  v_mfma_f32_16x16x32_bf16 v[184:187], a[248:251], a[40:43], v[184:187]
  ds_read_b64_tr_b16 a[222:223], v231 offset:36864
  v_mfma_f32_16x16x32_bf16 v[216:219], a[248:251], a[44:47], v[216:219]
  v_mfma_f32_16x16x32_bf16 v[156:159], a[252:255], a[36:39], v[156:159]
  v_mfma_f32_16x16x32_bf16 v[188:191], a[252:255], a[40:43], v[188:191]
  v_mfma_f32_16x16x32_bf16 v[220:223], a[252:255], a[44:47], v[220:223]
  s_waitcnt vmcnt(16)
  s_barrier
  v_mfma_f32_16x16x32_bf16 v[128:131], a[192:195], a[48:51], v[128:131]
  ds_read_b64_tr_b16 a[224:225], v224 offset:40960
  v_mfma_f32_16x16x32_bf16 v[160:163], a[192:195], a[52:55], v[160:163]
  s_mov_b32 s39, 737280
  s_add_u32 m0, s37, 16384
  ds_read_b64_tr_b16 a[226:227], v224 offset:45056
  v_mfma_f32_16x16x32_bf16 v[192:195], a[192:195], a[56:59], v[192:195]
  buffer_load_dwordx4 v11, s[20:23], s39 offen lds
  v_mfma_f32_16x16x32_bf16 v[132:135], a[196:199], a[48:51], v[132:135]
  ds_read_b64_tr_b16 a[228:229], v225 offset:40960
  v_mfma_f32_16x16x32_bf16 v[164:167], a[196:199], a[52:55], v[164:167]
  ds_read_b64_tr_b16 a[230:231], v225 offset:45056
  v_mfma_f32_16x16x32_bf16 v[196:199], a[196:199], a[56:59], v[196:199]
  s_waitcnt lgkmcnt(4)
  ds_read_b64_tr_b16 a[232:233], v226 offset:40960
  v_mfma_f32_16x16x32_bf16 v[136:139], a[200:203], a[48:51], v[136:139]
  buffer_load_dwordx4 v12, s[20:23], s39 offen offset:1024 lds
  v_mfma_f32_16x16x32_bf16 v[168:171], a[200:203], a[52:55], v[168:171]
  ds_read_b64_tr_b16 a[234:235], v226 offset:45056
  v_mfma_f32_16x16x32_bf16 v[200:203], a[200:203], a[56:59], v[200:203]
  ds_read_b64_tr_b16 a[236:237], v227 offset:40960
  v_mfma_f32_16x16x32_bf16 v[140:143], a[204:207], a[48:51], v[140:143]
  s_mov_b32 s43, 778240
  s_add_u32 m0, s37, 24576
  ds_read_b64_tr_b16 a[238:239], v227 offset:45056
  v_mfma_f32_16x16x32_bf16 v[172:175], a[204:207], a[52:55], v[172:175]
  buffer_load_dwordx4 v11, s[20:23], s43 offen lds
  v_mfma_f32_16x16x32_bf16 v[204:207], a[204:207], a[56:59], v[204:207]
  ds_read_b64_tr_b16 a[240:241], v228 offset:40960
  v_mfma_f32_16x16x32_bf16 v[144:147], a[208:211], a[48:51], v[144:147]
  ds_read_b64_tr_b16 a[242:243], v228 offset:45056
  v_mfma_f32_16x16x32_bf16 v[176:179], a[208:211], a[52:55], v[176:179]
  ds_read_b64_tr_b16 a[244:245], v229 offset:40960
  v_mfma_f32_16x16x32_bf16 v[208:211], a[208:211], a[56:59], v[208:211]
  buffer_load_dwordx4 v12, s[20:23], s43 offen offset:1024 lds
  v_mfma_f32_16x16x32_bf16 v[148:151], a[212:215], a[48:51], v[148:151]
  ds_read_b64_tr_b16 a[246:247], v229 offset:45056
  v_mfma_f32_16x16x32_bf16 v[180:183], a[212:215], a[52:55], v[180:183]
  ds_read_b64_tr_b16 a[248:249], v230 offset:40960
  v_mfma_f32_16x16x32_bf16 v[212:215], a[212:215], a[56:59], v[212:215]
  ds_read_b64_tr_b16 a[250:251], v230 offset:45056
  v_mfma_f32_16x16x32_bf16 v[152:155], a[216:219], a[48:51], v[152:155]
  s_mov_b32 s41, 18432
  ds_read_b64_tr_b16 a[252:253], v231 offset:40960
  v_mfma_f32_16x16x32_bf16 v[184:187], a[216:219], a[52:55], v[184:187]
  buffer_load_dwordx4 v[56:59], v0, s[16:19], s41 offen sc1
  v_mfma_f32_16x16x32_bf16 v[216:219], a[216:219], a[56:59], v[216:219]
  ds_read_b64_tr_b16 a[254:255], v231 offset:45056
  v_mfma_f32_16x16x32_bf16 v[156:159], a[220:223], a[48:51], v[156:159]
  v_mfma_f32_16x16x32_bf16 v[188:191], a[220:223], a[52:55], v[188:191]
  v_mfma_f32_16x16x32_bf16 v[220:223], a[220:223], a[56:59], v[220:223]
  s_waitcnt lgkmcnt(2)
  buffer_load_dwordx4 v[60:63], v13, s[16:19], s41 offen sc1
  v_mfma_f32_16x16x32_bf16 v[128:131], a[224:227], a[60:63], v[128:131]
  ds_read_b64_tr_b16 a[192:193], v224 offset:49152
  v_mfma_f32_16x16x32_bf16 v[160:163], a[224:227], a[64:67], v[160:163]
  ds_read_b64_tr_b16 a[194:195], v224 offset:53248
  v_mfma_f32_16x16x32_bf16 v[192:195], a[224:227], a[68:71], v[192:195]
  ds_read_b64_tr_b16 a[196:197], v225 offset:49152
  v_mfma_f32_16x16x32_bf16 v[132:135], a[228:231], a[60:63], v[132:135]
  buffer_load_dwordx4 v[64:67], v14, s[16:19], s41 offen sc1
  v_mfma_f32_16x16x32_bf16 v[164:167], a[228:231], a[64:67], v[164:167]
  ds_read_b64_tr_b16 a[198:199], v225 offset:53248
  v_mfma_f32_16x16x32_bf16 v[196:199], a[228:231], a[68:71], v[196:199]
  ds_read_b64_tr_b16 a[200:201], v226 offset:49152
  v_mfma_f32_16x16x32_bf16 v[136:139], a[232:235], a[60:63], v[136:139]
  ds_read_b64_tr_b16 a[202:203], v226 offset:53248
  v_mfma_f32_16x16x32_bf16 v[168:171], a[232:235], a[64:67], v[168:171]
  s_mov_b32 s42, 19456
  ds_read_b64_tr_b16 a[204:205], v227 offset:49152
  v_mfma_f32_16x16x32_bf16 v[200:203], a[232:235], a[68:71], v[200:203]
  buffer_load_dwordx4 v[68:71], v0, s[16:19], s42 offen sc1
  v_mfma_f32_16x16x32_bf16 v[140:143], a[236:239], a[60:63], v[140:143]
  ds_read_b64_tr_b16 a[206:207], v227 offset:53248
  v_mfma_f32_16x16x32_bf16 v[172:175], a[236:239], a[64:67], v[172:175]
  ds_read_b64_tr_b16 a[208:209], v228 offset:49152
  v_mfma_f32_16x16x32_bf16 v[204:207], a[236:239], a[68:71], v[204:207]
  ds_read_b64_tr_b16 a[210:211], v228 offset:53248
  v_mfma_f32_16x16x32_bf16 v[144:147], a[240:243], a[60:63], v[144:147]
  buffer_load_dwordx4 v[72:75], v13, s[16:19], s42 offen sc1
  v_mfma_f32_16x16x32_bf16 v[176:179], a[240:243], a[64:67], v[176:179]
  ds_read_b64_tr_b16 a[212:213], v229 offset:49152
  v_mfma_f32_16x16x32_bf16 v[208:211], a[240:243], a[68:71], v[208:211]
  ds_read_b64_tr_b16 a[214:215], v229 offset:53248
  v_mfma_f32_16x16x32_bf16 v[148:151], a[244:247], a[60:63], v[148:151]
  ds_read_b64_tr_b16 a[216:217], v230 offset:49152
  v_mfma_f32_16x16x32_bf16 v[180:183], a[244:247], a[64:67], v[180:183]
  buffer_load_dwordx4 v[76:79], v14, s[16:19], s42 offen sc1
  v_mfma_f32_16x16x32_bf16 v[212:215], a[244:247], a[68:71], v[212:215]
  ds_read_b64_tr_b16 a[218:219], v230 offset:53248
  v_mfma_f32_16x16x32_bf16 v[152:155], a[248:251], a[60:63], v[152:155]
  ds_read_b64_tr_b16 a[220:221], v231 offset:49152
  v_mfma_f32_16x16x32_bf16 v[184:187], a[248:251], a[64:67], v[184:187]
  ds_read_b64_tr_b16 a[222:223], v231 offset:53248
  v_mfma_f32_16x16x32_bf16 v[216:219], a[248:251], a[68:71], v[216:219]
  s_waitcnt lgkmcnt(4)
  v_mfma_f32_16x16x32_bf16 v[156:159], a[252:255], a[60:63], v[156:159]
  v_mfma_f32_16x16x32_bf16 v[188:191], a[252:255], a[64:67], v[188:191]
  v_mfma_f32_16x16x32_bf16 v[220:223], a[252:255], a[68:71], v[220:223]
  s_waitcnt vmcnt(16)
  s_barrier
  v_mfma_f32_16x16x32_bf16 v[128:131], a[192:195], a[72:75], v[128:131]
  ds_read_b64_tr_b16 a[224:225], v224 offset:57344
  v_mfma_f32_16x16x32_bf16 v[160:163], a[192:195], a[76:79], v[160:163]
  s_mov_b32 s39, 819200
  s_add_u32 m0, s37, 32768
  ds_read_b64_tr_b16 a[226:227], v224 offset:61440
  v_mfma_f32_16x16x32_bf16 v[192:195], a[192:195], a[80:83], v[192:195]
  buffer_load_dwordx4 v11, s[20:23], s39 offen lds
  v_mfma_f32_16x16x32_bf16 v[132:135], a[196:199], a[72:75], v[132:135]
  ds_read_b64_tr_b16 a[228:229], v225 offset:57344
  v_mfma_f32_16x16x32_bf16 v[164:167], a[196:199], a[76:79], v[164:167]
  ds_read_b64_tr_b16 a[230:231], v225 offset:61440
  v_mfma_f32_16x16x32_bf16 v[196:199], a[196:199], a[80:83], v[196:199]
  ds_read_b64_tr_b16 a[232:233], v226 offset:57344
  v_mfma_f32_16x16x32_bf16 v[136:139], a[200:203], a[72:75], v[136:139]
  buffer_load_dwordx4 v12, s[20:23], s39 offen offset:1024 lds
  v_mfma_f32_16x16x32_bf16 v[168:171], a[200:203], a[76:79], v[168:171]
  ds_read_b64_tr_b16 a[234:235], v226 offset:61440
  v_mfma_f32_16x16x32_bf16 v[200:203], a[200:203], a[80:83], v[200:203]
  ds_read_b64_tr_b16 a[236:237], v227 offset:57344
  v_mfma_f32_16x16x32_bf16 v[140:143], a[204:207], a[72:75], v[140:143]
  s_mov_b32 s43, 860160
  s_add_u32 m0, s37, 40960
  ds_read_b64_tr_b16 a[238:239], v227 offset:61440
  v_mfma_f32_16x16x32_bf16 v[172:175], a[204:207], a[76:79], v[172:175]
  buffer_load_dwordx4 v11, s[20:23], s43 offen lds
  v_mfma_f32_16x16x32_bf16 v[204:207], a[204:207], a[80:83], v[204:207]
  ds_read_b64_tr_b16 a[240:241], v228 offset:57344
  v_mfma_f32_16x16x32_bf16 v[144:147], a[208:211], a[72:75], v[144:147]
  ds_read_b64_tr_b16 a[242:243], v228 offset:61440
  v_mfma_f32_16x16x32_bf16 v[176:179], a[208:211], a[76:79], v[176:179]
  ds_read_b64_tr_b16 a[244:245], v229 offset:57344
  v_mfma_f32_16x16x32_bf16 v[208:211], a[208:211], a[80:83], v[208:211]
  buffer_load_dwordx4 v12, s[20:23], s43 offen offset:1024 lds
  v_mfma_f32_16x16x32_bf16 v[148:151], a[212:215], a[72:75], v[148:151]
  ds_read_b64_tr_b16 a[246:247], v229 offset:61440
  v_mfma_f32_16x16x32_bf16 v[180:183], a[212:215], a[76:79], v[180:183]
  ds_read_b64_tr_b16 a[248:249], v230 offset:57344
  v_mfma_f32_16x16x32_bf16 v[212:215], a[212:215], a[80:83], v[212:215]
  s_waitcnt lgkmcnt(4)
  ds_read_b64_tr_b16 a[250:251], v230 offset:61440
  v_mfma_f32_16x16x32_bf16 v[152:155], a[216:219], a[72:75], v[152:155]
  s_mov_b32 s41, 20480
  ds_read_b64_tr_b16 a[252:253], v231 offset:57344
  v_mfma_f32_16x16x32_bf16 v[184:187], a[216:219], a[76:79], v[184:187]
  buffer_load_dwordx4 v[80:83], v0, s[16:19], s41 offen sc1
  v_mfma_f32_16x16x32_bf16 v[216:219], a[216:219], a[80:83], v[216:219]
  ds_read_b64_tr_b16 a[254:255], v231 offset:61440
  v_mfma_f32_16x16x32_bf16 v[156:159], a[220:223], a[72:75], v[156:159]
  v_mfma_f32_16x16x32_bf16 v[188:191], a[220:223], a[76:79], v[188:191]
  v_mfma_f32_16x16x32_bf16 v[220:223], a[220:223], a[80:83], v[220:223]
  buffer_load_dwordx4 v[84:87], v13, s[16:19], s41 offen sc1
  v_mfma_f32_16x16x32_bf16 v[128:131], a[224:227], a[84:87], v[128:131]
  ds_read_b64_tr_b16 a[200:201], v5
  v_mfma_f32_16x16x32_bf16 v[160:163], a[224:227], a[88:91], v[160:163]
  ds_read_b64_tr_b16 a[202:203], v5 offset:4096
  v_mfma_f32_16x16x32_bf16 v[192:195], a[224:227], a[92:95], v[192:195]
  ds_read_b64_tr_b16 a[204:205], v6
  v_mfma_f32_16x16x32_bf16 v[132:135], a[228:231], a[84:87], v[132:135]
  buffer_load_dwordx4 v[88:91], v14, s[16:19], s41 offen sc1
  v_mfma_f32_16x16x32_bf16 v[164:167], a[228:231], a[88:91], v[164:167]
  ds_read_b64_tr_b16 a[206:207], v6 offset:4096
  v_mfma_f32_16x16x32_bf16 v[196:199], a[228:231], a[92:95], v[196:199]
  ds_read_b64_tr_b16 a[208:209], v7
  v_mfma_f32_16x16x32_bf16 v[136:139], a[232:235], a[84:87], v[136:139]
  ds_read_b64_tr_b16 a[210:211], v7 offset:4096
  v_mfma_f32_16x16x32_bf16 v[168:171], a[232:235], a[88:91], v[168:171]
  s_mov_b32 s42, 21504
  ds_read_b64_tr_b16 a[212:213], v8
  v_mfma_f32_16x16x32_bf16 v[200:203], a[232:235], a[92:95], v[200:203]
  buffer_load_dwordx4 v[92:95], v0, s[16:19], s42 offen sc1
  v_mfma_f32_16x16x32_bf16 v[140:143], a[236:239], a[84:87], v[140:143]
  ds_read_b64_tr_b16 a[214:215], v8 offset:4096
  v_mfma_f32_16x16x32_bf16 v[172:175], a[236:239], a[88:91], v[172:175]
  ds_read_b64_tr_b16 a[216:217], v9
  v_mfma_f32_16x16x32_bf16 v[204:207], a[236:239], a[92:95], v[204:207]
  s_waitcnt lgkmcnt(4)
  ds_read_b64_tr_b16 a[218:219], v9 offset:4096
  v_mfma_f32_16x16x32_bf16 v[144:147], a[240:243], a[84:87], v[144:147]
  buffer_load_dwordx4 v[96:99], v13, s[16:19], s42 offen sc1
  v_mfma_f32_16x16x32_bf16 v[176:179], a[240:243], a[88:91], v[176:179]
  ds_read_b64_tr_b16 a[220:221], v10
  v_mfma_f32_16x16x32_bf16 v[208:211], a[240:243], a[92:95], v[208:211]
  ds_read_b64_tr_b16 a[222:223], v10 offset:4096
  v_mfma_f32_16x16x32_bf16 v[148:151], a[244:247], a[84:87], v[148:151]
  v_mfma_f32_16x16x32_bf16 v[180:183], a[244:247], a[88:91], v[180:183]
  buffer_load_dwordx4 v[100:103], v14, s[16:19], s42 offen sc1
  v_mfma_f32_16x16x32_bf16 v[212:215], a[244:247], a[92:95], v[212:215]
  v_mfma_f32_16x16x32_bf16 v[152:155], a[248:251], a[84:87], v[152:155]
  v_mfma_f32_16x16x32_bf16 v[184:187], a[248:251], a[88:91], v[184:187]
  v_mfma_f32_16x16x32_bf16 v[216:219], a[248:251], a[92:95], v[216:219]
  v_mfma_f32_16x16x32_bf16 v[156:159], a[252:255], a[84:87], v[156:159]
  v_mfma_f32_16x16x32_bf16 v[188:191], a[252:255], a[88:91], v[188:191]
  v_mfma_f32_16x16x32_bf16 v[220:223], a[252:255], a[92:95], v[220:223]
  s_waitcnt vmcnt(16)
  s_barrier
  v_mfma_f32_16x16x32_bf16 v[136:139], a[200:203], v[32:35], v[136:139]
  ds_read_b64_tr_b16 a[232:233], v5 offset:8192
  v_mfma_f32_16x16x32_bf16 v[168:171], a[200:203], v[36:39], v[168:171]
  s_mov_b32 s39, 901120
  s_add_u32 m0, s37, 49152
  ds_read_b64_tr_b16 a[234:235], v5 offset:12288
  v_mfma_f32_16x16x32_bf16 v[200:203], a[200:203], v[40:43], v[200:203]
  ds_read_b64_tr_b16 a[236:237], v6 offset:8192
  buffer_load_dwordx4 v11, s[20:23], s39 offen lds
  v_mfma_f32_16x16x32_bf16 v[140:143], a[204:207], v[32:35], v[140:143]
  ds_read_b64_tr_b16 a[238:239], v6 offset:12288
  v_mfma_f32_16x16x32_bf16 v[172:175], a[204:207], v[36:39], v[172:175]
  ds_read_b64_tr_b16 a[240:241], v7 offset:8192
  v_mfma_f32_16x16x32_bf16 v[204:207], a[204:207], v[40:43], v[204:207]
  s_waitcnt lgkmcnt(5)
  ds_read_b64_tr_b16 a[242:243], v7 offset:12288
  buffer_load_dwordx4 v12, s[20:23], s39 offen offset:1024 lds
  v_mfma_f32_16x16x32_bf16 v[144:147], a[208:211], v[32:35], v[144:147]
  ds_read_b64_tr_b16 a[244:245], v8 offset:8192
  v_mfma_f32_16x16x32_bf16 v[176:179], a[208:211], v[36:39], v[176:179]
  s_mov_b32 s43, 942080
  s_add_u32 m0, s37, 57344
  ds_read_b64_tr_b16 a[246:247], v8 offset:12288
  v_mfma_f32_16x16x32_bf16 v[208:211], a[208:211], v[40:43], v[208:211]
  ds_read_b64_tr_b16 a[248:249], v9 offset:8192
  buffer_load_dwordx4 v11, s[20:23], s43 offen lds
  v_mfma_f32_16x16x32_bf16 v[148:151], a[212:215], v[32:35], v[148:151]
  ds_read_b64_tr_b16 a[250:251], v9 offset:12288
  v_mfma_f32_16x16x32_bf16 v[180:183], a[212:215], v[36:39], v[180:183]
  ds_read_b64_tr_b16 a[252:253], v10 offset:8192
  v_mfma_f32_16x16x32_bf16 v[212:215], a[212:215], v[40:43], v[212:215]
  ds_read_b64_tr_b16 a[254:255], v10 offset:12288
  buffer_load_dwordx4 v12, s[20:23], s43 offen offset:1024 lds
  v_mfma_f32_16x16x32_bf16 v[152:155], a[216:219], v[32:35], v[152:155]
  v_mfma_f32_16x16x32_bf16 v[184:187], a[216:219], v[36:39], v[184:187]
  s_mov_b32 s41, 22528
  v_mfma_f32_16x16x32_bf16 v[216:219], a[216:219], v[40:43], v[216:219]
  buffer_load_dwordx4 v[104:107], v0, s[16:19], s41 offen sc1
  v_mfma_f32_16x16x32_bf16 v[156:159], a[220:223], v[32:35], v[156:159]
  v_mfma_f32_16x16x32_bf16 v[188:191], a[220:223], v[36:39], v[188:191]
  v_mfma_f32_16x16x32_bf16 v[220:223], a[220:223], v[40:43], v[220:223]
  s_waitcnt lgkmcnt(0)
  buffer_load_dwordx4 v[108:111], v13, s[16:19], s41 offen sc1
  v_mfma_f32_16x16x32_bf16 v[136:139], a[232:235], v[44:47], v[136:139]
  ds_read_b64_tr_b16 a[200:201], v5 offset:16384
  v_mfma_f32_16x16x32_bf16 v[168:171], a[232:235], v[48:51], v[168:171]
  ds_read_b64_tr_b16 a[202:203], v5 offset:20480
  v_mfma_f32_16x16x32_bf16 v[200:203], a[232:235], v[52:55], v[200:203]
  buffer_load_dwordx4 v[112:115], v14, s[16:19], s41 offen sc1
  v_mfma_f32_16x16x32_bf16 v[140:143], a[236:239], v[44:47], v[140:143]
  ds_read_b64_tr_b16 a[204:205], v6 offset:16384
  v_mfma_f32_16x16x32_bf16 v[172:175], a[236:239], v[48:51], v[172:175]
  s_mov_b32 s42, 23552
  ds_read_b64_tr_b16 a[206:207], v6 offset:20480
  v_mfma_f32_16x16x32_bf16 v[204:207], a[236:239], v[52:55], v[204:207]
  buffer_load_dwordx4 v[116:119], v0, s[16:19], s42 offen sc1
  v_mfma_f32_16x16x32_bf16 v[144:147], a[240:243], v[44:47], v[144:147]
  ds_read_b64_tr_b16 a[208:209], v7 offset:16384
  v_mfma_f32_16x16x32_bf16 v[176:179], a[240:243], v[48:51], v[176:179]
  ds_read_b64_tr_b16 a[210:211], v7 offset:20480
  v_mfma_f32_16x16x32_bf16 v[208:211], a[240:243], v[52:55], v[208:211]
  buffer_load_dwordx4 v[120:123], v13, s[16:19], s42 offen sc1
  v_mfma_f32_16x16x32_bf16 v[148:151], a[244:247], v[44:47], v[148:151]
  ds_read_b64_tr_b16 a[212:213], v8 offset:16384
  v_mfma_f32_16x16x32_bf16 v[180:183], a[244:247], v[48:51], v[180:183]
  ds_read_b64_tr_b16 a[214:215], v8 offset:20480
  v_mfma_f32_16x16x32_bf16 v[212:215], a[244:247], v[52:55], v[212:215]
  buffer_load_dwordx4 v[124:127], v14, s[16:19], s42 offen sc1
  v_mfma_f32_16x16x32_bf16 v[152:155], a[248:251], v[44:47], v[152:155]
  ds_read_b64_tr_b16 a[216:217], v9 offset:16384
  v_mfma_f32_16x16x32_bf16 v[184:187], a[248:251], v[48:51], v[184:187]
  ds_read_b64_tr_b16 a[218:219], v9 offset:20480
  v_mfma_f32_16x16x32_bf16 v[216:219], a[248:251], v[52:55], v[216:219]
  ds_read_b64_tr_b16 a[220:221], v10 offset:16384
  v_mfma_f32_16x16x32_bf16 v[156:159], a[252:255], v[44:47], v[156:159]
  ds_read_b64_tr_b16 a[222:223], v10 offset:20480
  v_mfma_f32_16x16x32_bf16 v[188:191], a[252:255], v[48:51], v[188:191]
  v_mfma_f32_16x16x32_bf16 v[220:223], a[252:255], v[52:55], v[220:223]
  s_waitcnt lgkmcnt(4)
  s_waitcnt vmcnt(16)
  s_barrier
  v_mfma_f32_16x16x32_bf16 v[136:139], a[200:203], v[56:59], v[136:139]
  ds_read_b64_tr_b16 a[232:233], v5 offset:24576
  v_mfma_f32_16x16x32_bf16 v[168:171], a[200:203], v[60:63], v[168:171]
  s_mov_b32 s39, 983040
  s_add_u32 m0, s37, 65536
  ds_read_b64_tr_b16 a[234:235], v5 offset:28672
  v_mfma_f32_16x16x32_bf16 v[200:203], a[200:203], v[64:67], v[200:203]
  ds_read_b64_tr_b16 a[236:237], v6 offset:24576
  buffer_load_dwordx4 v11, s[20:23], s39 offen lds
  v_mfma_f32_16x16x32_bf16 v[140:143], a[204:207], v[56:59], v[140:143]
  ds_read_b64_tr_b16 a[238:239], v6 offset:28672
  v_mfma_f32_16x16x32_bf16 v[172:175], a[204:207], v[60:63], v[172:175]
  ds_read_b64_tr_b16 a[240:241], v7 offset:24576
  v_mfma_f32_16x16x32_bf16 v[204:207], a[204:207], v[64:67], v[204:207]
  ds_read_b64_tr_b16 a[242:243], v7 offset:28672
  buffer_load_dwordx4 v12, s[20:23], s39 offen offset:1024 lds
  v_mfma_f32_16x16x32_bf16 v[144:147], a[208:211], v[56:59], v[144:147]
  ds_read_b64_tr_b16 a[244:245], v8 offset:24576
  v_mfma_f32_16x16x32_bf16 v[176:179], a[208:211], v[60:63], v[176:179]
  s_mov_b32 s43, 1024000
  s_add_u32 m0, s37, 73728
  ds_read_b64_tr_b16 a[246:247], v8 offset:28672
  v_mfma_f32_16x16x32_bf16 v[208:211], a[208:211], v[64:67], v[208:211]
  ds_read_b64_tr_b16 a[248:249], v9 offset:24576
  buffer_load_dwordx4 v11, s[20:23], s43 offen lds
  v_mfma_f32_16x16x32_bf16 v[148:151], a[212:215], v[56:59], v[148:151]
  ds_read_b64_tr_b16 a[250:251], v9 offset:28672
  v_mfma_f32_16x16x32_bf16 v[180:183], a[212:215], v[60:63], v[180:183]
  ds_read_b64_tr_b16 a[252:253], v10 offset:24576
  v_mfma_f32_16x16x32_bf16 v[212:215], a[212:215], v[64:67], v[212:215]
  s_waitcnt lgkmcnt(5)
  ds_read_b64_tr_b16 a[254:255], v10 offset:28672
  buffer_load_dwordx4 v12, s[20:23], s43 offen offset:1024 lds
  v_mfma_f32_16x16x32_bf16 v[152:155], a[216:219], v[56:59], v[152:155]
  v_mfma_f32_16x16x32_bf16 v[184:187], a[216:219], v[60:63], v[184:187]
  s_mov_b32 s41, 24576
  v_mfma_f32_16x16x32_bf16 v[216:219], a[216:219], v[64:67], v[216:219]
  buffer_load_dwordx4 a[0:3], v0, s[16:19], s41 offen sc1
  v_mfma_f32_16x16x32_bf16 v[156:159], a[220:223], v[56:59], v[156:159]
  v_mfma_f32_16x16x32_bf16 v[188:191], a[220:223], v[60:63], v[188:191]
  v_mfma_f32_16x16x32_bf16 v[220:223], a[220:223], v[64:67], v[220:223]
  buffer_load_dwordx4 a[4:7], v13, s[16:19], s41 offen sc1
  v_mfma_f32_16x16x32_bf16 v[136:139], a[232:235], v[68:71], v[136:139]
  ds_read_b64_tr_b16 a[200:201], v5 offset:32768
  v_mfma_f32_16x16x32_bf16 v[168:171], a[232:235], v[72:75], v[168:171]
  ds_read_b64_tr_b16 a[202:203], v5 offset:36864
  v_mfma_f32_16x16x32_bf16 v[200:203], a[232:235], v[76:79], v[200:203]
  buffer_load_dwordx4 a[8:11], v14, s[16:19], s41 offen sc1
  v_mfma_f32_16x16x32_bf16 v[140:143], a[236:239], v[68:71], v[140:143]
  ds_read_b64_tr_b16 a[204:205], v6 offset:32768
  v_mfma_f32_16x16x32_bf16 v[172:175], a[236:239], v[72:75], v[172:175]
  s_mov_b32 s42, 25600
  ds_read_b64_tr_b16 a[206:207], v6 offset:36864
  v_mfma_f32_16x16x32_bf16 v[204:207], a[236:239], v[76:79], v[204:207]
  buffer_load_dwordx4 a[12:15], v0, s[16:19], s42 offen sc1
  v_mfma_f32_16x16x32_bf16 v[144:147], a[240:243], v[68:71], v[144:147]
  ds_read_b64_tr_b16 a[208:209], v7 offset:32768
  v_mfma_f32_16x16x32_bf16 v[176:179], a[240:243], v[72:75], v[176:179]
  ds_read_b64_tr_b16 a[210:211], v7 offset:36864
  v_mfma_f32_16x16x32_bf16 v[208:211], a[240:243], v[76:79], v[208:211]
  s_waitcnt lgkmcnt(4)
  buffer_load_dwordx4 a[16:19], v13, s[16:19], s42 offen sc1
  v_mfma_f32_16x16x32_bf16 v[148:151], a[244:247], v[68:71], v[148:151]
  ds_read_b64_tr_b16 a[212:213], v8 offset:32768
  v_mfma_f32_16x16x32_bf16 v[180:183], a[244:247], v[72:75], v[180:183]
  ds_read_b64_tr_b16 a[214:215], v8 offset:36864
  v_mfma_f32_16x16x32_bf16 v[212:215], a[244:247], v[76:79], v[212:215]
  buffer_load_dwordx4 a[20:23], v14, s[16:19], s42 offen sc1
  v_mfma_f32_16x16x32_bf16 v[152:155], a[248:251], v[68:71], v[152:155]
  ds_read_b64_tr_b16 a[216:217], v9 offset:32768
  v_mfma_f32_16x16x32_bf16 v[184:187], a[248:251], v[72:75], v[184:187]
  ds_read_b64_tr_b16 a[218:219], v9 offset:36864
  v_mfma_f32_16x16x32_bf16 v[216:219], a[248:251], v[76:79], v[216:219]
  ds_read_b64_tr_b16 a[220:221], v10 offset:32768
  v_mfma_f32_16x16x32_bf16 v[156:159], a[252:255], v[68:71], v[156:159]
  ds_read_b64_tr_b16 a[222:223], v10 offset:36864
  v_mfma_f32_16x16x32_bf16 v[188:191], a[252:255], v[72:75], v[188:191]
  v_mfma_f32_16x16x32_bf16 v[220:223], a[252:255], v[76:79], v[220:223]
  s_waitcnt vmcnt(16)
  s_barrier
  v_mfma_f32_16x16x32_bf16 v[136:139], a[200:203], v[80:83], v[136:139]
  ds_read_b64_tr_b16 a[232:233], v5 offset:40960
  v_mfma_f32_16x16x32_bf16 v[168:171], a[200:203], v[84:87], v[168:171]
  s_mov_b32 s39, 1064960
  s_add_u32 m0, s37, 81920
  ds_read_b64_tr_b16 a[234:235], v5 offset:45056
  v_mfma_f32_16x16x32_bf16 v[200:203], a[200:203], v[88:91], v[200:203]
  s_waitcnt lgkmcnt(3)
  ds_read_b64_tr_b16 a[236:237], v6 offset:40960
  buffer_load_dwordx4 v11, s[20:23], s39 offen lds
  v_mfma_f32_16x16x32_bf16 v[140:143], a[204:207], v[80:83], v[140:143]
  ds_read_b64_tr_b16 a[238:239], v6 offset:45056
  v_mfma_f32_16x16x32_bf16 v[172:175], a[204:207], v[84:87], v[172:175]
  ds_read_b64_tr_b16 a[240:241], v7 offset:40960
  v_mfma_f32_16x16x32_bf16 v[204:207], a[204:207], v[88:91], v[204:207]
  ds_read_b64_tr_b16 a[242:243], v7 offset:45056
  buffer_load_dwordx4 v12, s[20:23], s39 offen offset:1024 lds
  v_mfma_f32_16x16x32_bf16 v[144:147], a[208:211], v[80:83], v[144:147]
  ds_read_b64_tr_b16 a[244:245], v8 offset:40960
  v_mfma_f32_16x16x32_bf16 v[176:179], a[208:211], v[84:87], v[176:179]
  s_mov_b32 s43, 1105920
  s_add_u32 m0, s37, 90112
  ds_read_b64_tr_b16 a[246:247], v8 offset:45056
  v_mfma_f32_16x16x32_bf16 v[208:211], a[208:211], v[88:91], v[208:211]
  ds_read_b64_tr_b16 a[248:249], v9 offset:40960
  buffer_load_dwordx4 v11, s[20:23], s43 offen lds
  v_mfma_f32_16x16x32_bf16 v[148:151], a[212:215], v[80:83], v[148:151]
  ds_read_b64_tr_b16 a[250:251], v9 offset:45056
  v_mfma_f32_16x16x32_bf16 v[180:183], a[212:215], v[84:87], v[180:183]
  ds_read_b64_tr_b16 a[252:253], v10 offset:40960
  v_mfma_f32_16x16x32_bf16 v[212:215], a[212:215], v[88:91], v[212:215]
  ds_read_b64_tr_b16 a[254:255], v10 offset:45056
  buffer_load_dwordx4 v12, s[20:23], s43 offen offset:1024 lds
  v_mfma_f32_16x16x32_bf16 v[152:155], a[216:219], v[80:83], v[152:155]
  v_mfma_f32_16x16x32_bf16 v[184:187], a[216:219], v[84:87], v[184:187]
  s_mov_b32 s41, 26624
  v_mfma_f32_16x16x32_bf16 v[216:219], a[216:219], v[88:91], v[216:219]
  s_waitcnt lgkmcnt(3)
  buffer_load_dwordx4 a[24:27], v0, s[16:19], s41 offen sc1
  v_mfma_f32_16x16x32_bf16 v[156:159], a[220:223], v[80:83], v[156:159]
  v_mfma_f32_16x16x32_bf16 v[188:191], a[220:223], v[84:87], v[188:191]
  v_mfma_f32_16x16x32_bf16 v[220:223], a[220:223], v[88:91], v[220:223]
  buffer_load_dwordx4 a[28:31], v13, s[16:19], s41 offen sc1
  v_mfma_f32_16x16x32_bf16 v[136:139], a[232:235], v[92:95], v[136:139]
  ds_read_b64_tr_b16 a[200:201], v5 offset:49152
  v_mfma_f32_16x16x32_bf16 v[168:171], a[232:235], v[96:99], v[168:171]
  ds_read_b64_tr_b16 a[202:203], v5 offset:53248
  v_mfma_f32_16x16x32_bf16 v[200:203], a[232:235], v[100:103], v[200:203]
  buffer_load_dwordx4 a[32:35], v14, s[16:19], s41 offen sc1
  v_mfma_f32_16x16x32_bf16 v[140:143], a[236:239], v[92:95], v[140:143]
  ds_read_b64_tr_b16 a[204:205], v6 offset:49152
  v_mfma_f32_16x16x32_bf16 v[172:175], a[236:239], v[96:99], v[172:175]
  s_mov_b32 s42, 27648
  ds_read_b64_tr_b16 a[206:207], v6 offset:53248
  v_mfma_f32_16x16x32_bf16 v[204:207], a[236:239], v[100:103], v[204:207]
  buffer_load_dwordx4 a[36:39], v0, s[16:19], s42 offen sc1
  v_mfma_f32_16x16x32_bf16 v[144:147], a[240:243], v[92:95], v[144:147]
  ds_read_b64_tr_b16 a[208:209], v7 offset:49152
  v_mfma_f32_16x16x32_bf16 v[176:179], a[240:243], v[96:99], v[176:179]
  ds_read_b64_tr_b16 a[210:211], v7 offset:53248
  v_mfma_f32_16x16x32_bf16 v[208:211], a[240:243], v[100:103], v[208:211]
  buffer_load_dwordx4 a[40:43], v13, s[16:19], s42 offen sc1
  v_mfma_f32_16x16x32_bf16 v[148:151], a[244:247], v[92:95], v[148:151]
  ds_read_b64_tr_b16 a[212:213], v8 offset:49152
  v_mfma_f32_16x16x32_bf16 v[180:183], a[244:247], v[96:99], v[180:183]
  ds_read_b64_tr_b16 a[214:215], v8 offset:53248
  v_mfma_f32_16x16x32_bf16 v[212:215], a[244:247], v[100:103], v[212:215]
  s_waitcnt lgkmcnt(4)
  buffer_load_dwordx4 a[44:47], v14, s[16:19], s42 offen sc1
  v_mfma_f32_16x16x32_bf16 v[152:155], a[248:251], v[92:95], v[152:155]
  ds_read_b64_tr_b16 a[216:217], v9 offset:49152
  v_mfma_f32_16x16x32_bf16 v[184:187], a[248:251], v[96:99], v[184:187]
  ds_read_b64_tr_b16 a[218:219], v9 offset:53248
  v_mfma_f32_16x16x32_bf16 v[216:219], a[248:251], v[100:103], v[216:219]
  ds_read_b64_tr_b16 a[220:221], v10 offset:49152
  v_mfma_f32_16x16x32_bf16 v[156:159], a[252:255], v[92:95], v[156:159]
  ds_read_b64_tr_b16 a[222:223], v10 offset:53248
  v_mfma_f32_16x16x32_bf16 v[188:191], a[252:255], v[96:99], v[188:191]
  v_mfma_f32_16x16x32_bf16 v[220:223], a[252:255], v[100:103], v[220:223]
  s_waitcnt vmcnt(16)
  s_barrier
  v_mfma_f32_16x16x32_bf16 v[136:139], a[200:203], v[104:107], v[136:139]
  ds_read_b64_tr_b16 a[232:233], v5 offset:57344
  v_mfma_f32_16x16x32_bf16 v[168:171], a[200:203], v[108:111], v[168:171]
  s_mov_b32 s39, 1146880
  s_add_u32 m0, s37, 98304
  ds_read_b64_tr_b16 a[234:235], v5 offset:61440
  v_mfma_f32_16x16x32_bf16 v[200:203], a[200:203], v[112:115], v[200:203]
  ds_read_b64_tr_b16 a[236:237], v6 offset:57344
  buffer_load_dwordx4 v11, s[20:23], s39 offen lds
  v_mfma_f32_16x16x32_bf16 v[140:143], a[204:207], v[104:107], v[140:143]
  ds_read_b64_tr_b16 a[238:239], v6 offset:61440
  v_mfma_f32_16x16x32_bf16 v[172:175], a[204:207], v[108:111], v[172:175]
  ds_read_b64_tr_b16 a[240:241], v7 offset:57344
  v_mfma_f32_16x16x32_bf16 v[204:207], a[204:207], v[112:115], v[204:207]
  s_waitcnt lgkmcnt(5)
  ds_read_b64_tr_b16 a[242:243], v7 offset:61440
  buffer_load_dwordx4 v12, s[20:23], s39 offen offset:1024 lds
  v_mfma_f32_16x16x32_bf16 v[144:147], a[208:211], v[104:107], v[144:147]
  ds_read_b64_tr_b16 a[244:245], v8 offset:57344
  v_mfma_f32_16x16x32_bf16 v[176:179], a[208:211], v[108:111], v[176:179]
  s_mov_b32 s43, 1187840
  s_add_u32 m0, s37, 106496
  ds_read_b64_tr_b16 a[246:247], v8 offset:61440
  v_mfma_f32_16x16x32_bf16 v[208:211], a[208:211], v[112:115], v[208:211]
  ds_read_b64_tr_b16 a[248:249], v9 offset:57344
  buffer_load_dwordx4 v11, s[20:23], s43 offen lds
  v_mfma_f32_16x16x32_bf16 v[148:151], a[212:215], v[104:107], v[148:151]
  ds_read_b64_tr_b16 a[250:251], v9 offset:61440
  v_mfma_f32_16x16x32_bf16 v[180:183], a[212:215], v[108:111], v[180:183]
  ds_read_b64_tr_b16 a[252:253], v10 offset:57344
  v_mfma_f32_16x16x32_bf16 v[212:215], a[212:215], v[112:115], v[212:215]
  ds_read_b64_tr_b16 a[254:255], v10 offset:61440
  buffer_load_dwordx4 v12, s[20:23], s43 offen offset:1024 lds
  v_mfma_f32_16x16x32_bf16 v[152:155], a[216:219], v[104:107], v[152:155]
  v_mfma_f32_16x16x32_bf16 v[184:187], a[216:219], v[108:111], v[184:187]
  s_mov_b32 s41, 28672
  v_mfma_f32_16x16x32_bf16 v[216:219], a[216:219], v[112:115], v[216:219]
  buffer_load_dwordx4 a[48:51], v0, s[16:19], s41 offen sc1
  v_mfma_f32_16x16x32_bf16 v[156:159], a[220:223], v[104:107], v[156:159]
  v_mfma_f32_16x16x32_bf16 v[188:191], a[220:223], v[108:111], v[188:191]
  v_mfma_f32_16x16x32_bf16 v[220:223], a[220:223], v[112:115], v[220:223]
  s_waitcnt lgkmcnt(0)
  buffer_load_dwordx4 a[52:55], v13, s[16:19], s41 offen sc1
  v_mfma_f32_16x16x32_bf16 v[136:139], a[232:235], v[116:119], v[136:139]
  ds_read_b64_tr_b16 a[200:201], v226
  v_mfma_f32_16x16x32_bf16 v[168:171], a[232:235], v[120:123], v[168:171]
  ds_read_b64_tr_b16 a[202:203], v226 offset:4096
  v_mfma_f32_16x16x32_bf16 v[200:203], a[232:235], v[124:127], v[200:203]
  buffer_load_dwordx4 a[56:59], v14, s[16:19], s41 offen sc1
  v_mfma_f32_16x16x32_bf16 v[140:143], a[236:239], v[116:119], v[140:143]
  ds_read_b64_tr_b16 a[204:205], v227
  v_mfma_f32_16x16x32_bf16 v[172:175], a[236:239], v[120:123], v[172:175]
  s_mov_b32 s42, 29696
  ds_read_b64_tr_b16 a[206:207], v227 offset:4096
  v_mfma_f32_16x16x32_bf16 v[204:207], a[236:239], v[124:127], v[204:207]
  buffer_load_dwordx4 a[60:63], v0, s[16:19], s42 offen sc1
  v_mfma_f32_16x16x32_bf16 v[144:147], a[240:243], v[116:119], v[144:147]
  ds_read_b64_tr_b16 a[208:209], v228
  v_mfma_f32_16x16x32_bf16 v[176:179], a[240:243], v[120:123], v[176:179]
  ds_read_b64_tr_b16 a[210:211], v228 offset:4096
  v_mfma_f32_16x16x32_bf16 v[208:211], a[240:243], v[124:127], v[208:211]
  buffer_load_dwordx4 a[64:67], v13, s[16:19], s42 offen sc1
  v_mfma_f32_16x16x32_bf16 v[148:151], a[244:247], v[116:119], v[148:151]
  ds_read_b64_tr_b16 a[212:213], v229
  v_mfma_f32_16x16x32_bf16 v[180:183], a[244:247], v[120:123], v[180:183]
  ds_read_b64_tr_b16 a[214:215], v229 offset:4096
  v_mfma_f32_16x16x32_bf16 v[212:215], a[244:247], v[124:127], v[212:215]
  buffer_load_dwordx4 a[68:71], v14, s[16:19], s42 offen sc1
  v_mfma_f32_16x16x32_bf16 v[152:155], a[248:251], v[116:119], v[152:155]
  ds_read_b64_tr_b16 a[216:217], v230
  v_mfma_f32_16x16x32_bf16 v[184:187], a[248:251], v[120:123], v[184:187]
  ds_read_b64_tr_b16 a[218:219], v230 offset:4096
  v_mfma_f32_16x16x32_bf16 v[216:219], a[248:251], v[124:127], v[216:219]
  ds_read_b64_tr_b16 a[220:221], v231
  v_mfma_f32_16x16x32_bf16 v[156:159], a[252:255], v[116:119], v[156:159]
  ds_read_b64_tr_b16 a[222:223], v231 offset:4096
  v_mfma_f32_16x16x32_bf16 v[188:191], a[252:255], v[120:123], v[188:191]
  v_mfma_f32_16x16x32_bf16 v[220:223], a[252:255], v[124:127], v[220:223]
  s_waitcnt lgkmcnt(4)
  s_waitcnt vmcnt(16)
  s_barrier
  v_mfma_f32_16x16x32_bf16 v[136:139], a[200:203], a[0:3], v[136:139]
  ds_read_b64_tr_b16 a[232:233], v226 offset:8192
  v_mfma_f32_16x16x32_bf16 v[168:171], a[200:203], a[4:7], v[168:171]
  s_mov_b32 s39, 1228800
  s_add_u32 m0, s37, 114688
  ds_read_b64_tr_b16 a[234:235], v226 offset:12288
  v_mfma_f32_16x16x32_bf16 v[200:203], a[200:203], a[8:11], v[200:203]
  ds_read_b64_tr_b16 a[236:237], v227 offset:8192
  buffer_load_dwordx4 v11, s[20:23], s39 offen lds
  v_mfma_f32_16x16x32_bf16 v[140:143], a[204:207], a[0:3], v[140:143]
  ds_read_b64_tr_b16 a[238:239], v227 offset:12288
  v_mfma_f32_16x16x32_bf16 v[172:175], a[204:207], a[4:7], v[172:175]
  ds_read_b64_tr_b16 a[240:241], v228 offset:8192
  v_mfma_f32_16x16x32_bf16 v[204:207], a[204:207], a[8:11], v[204:207]
  ds_read_b64_tr_b16 a[242:243], v228 offset:12288
  buffer_load_dwordx4 v12, s[20:23], s39 offen offset:1024 lds
  v_mfma_f32_16x16x32_bf16 v[144:147], a[208:211], a[0:3], v[144:147]
  ds_read_b64_tr_b16 a[244:245], v229 offset:8192
  v_mfma_f32_16x16x32_bf16 v[176:179], a[208:211], a[4:7], v[176:179]
  s_mov_b32 s43, 1269760
  s_add_u32 m0, s37, 122880
  ds_read_b64_tr_b16 a[246:247], v229 offset:12288
  v_mfma_f32_16x16x32_bf16 v[208:211], a[208:211], a[8:11], v[208:211]
  ds_read_b64_tr_b16 a[248:249], v230 offset:8192
  buffer_load_dwordx4 v11, s[20:23], s43 offen lds
  v_mfma_f32_16x16x32_bf16 v[148:151], a[212:215], a[0:3], v[148:151]
  ds_read_b64_tr_b16 a[250:251], v230 offset:12288
  v_mfma_f32_16x16x32_bf16 v[180:183], a[212:215], a[4:7], v[180:183]
  ds_read_b64_tr_b16 a[252:253], v231 offset:8192
  v_mfma_f32_16x16x32_bf16 v[212:215], a[212:215], a[8:11], v[212:215]
  s_waitcnt lgkmcnt(5)
  ds_read_b64_tr_b16 a[254:255], v231 offset:12288
  buffer_load_dwordx4 v12, s[20:23], s43 offen offset:1024 lds
  v_mfma_f32_16x16x32_bf16 v[152:155], a[216:219], a[0:3], v[152:155]
  v_mfma_f32_16x16x32_bf16 v[184:187], a[216:219], a[4:7], v[184:187]
  s_mov_b32 s41, 30720
  v_mfma_f32_16x16x32_bf16 v[216:219], a[216:219], a[8:11], v[216:219]
  buffer_load_dwordx4 a[72:75], v0, s[16:19], s41 offen sc1
  v_mfma_f32_16x16x32_bf16 v[156:159], a[220:223], a[0:3], v[156:159]
  v_mfma_f32_16x16x32_bf16 v[188:191], a[220:223], a[4:7], v[188:191]
  v_mfma_f32_16x16x32_bf16 v[220:223], a[220:223], a[8:11], v[220:223]
  buffer_load_dwordx4 a[76:79], v13, s[16:19], s41 offen sc1
  v_mfma_f32_16x16x32_bf16 v[136:139], a[232:235], a[12:15], v[136:139]
  ds_read_b64_tr_b16 a[200:201], v226 offset:16384
  v_mfma_f32_16x16x32_bf16 v[168:171], a[232:235], a[16:19], v[168:171]
  ds_read_b64_tr_b16 a[202:203], v226 offset:20480
  v_mfma_f32_16x16x32_bf16 v[200:203], a[232:235], a[20:23], v[200:203]
  buffer_load_dwordx4 a[80:83], v14, s[16:19], s41 offen sc1
  v_mfma_f32_16x16x32_bf16 v[140:143], a[236:239], a[12:15], v[140:143]
  ds_read_b64_tr_b16 a[204:205], v227 offset:16384
  v_mfma_f32_16x16x32_bf16 v[172:175], a[236:239], a[16:19], v[172:175]
  s_mov_b32 s42, 31744
  ds_read_b64_tr_b16 a[206:207], v227 offset:20480
  v_mfma_f32_16x16x32_bf16 v[204:207], a[236:239], a[20:23], v[204:207]
  buffer_load_dwordx4 a[84:87], v0, s[16:19], s42 offen sc1
  v_mfma_f32_16x16x32_bf16 v[144:147], a[240:243], a[12:15], v[144:147]
  ds_read_b64_tr_b16 a[208:209], v228 offset:16384
  v_mfma_f32_16x16x32_bf16 v[176:179], a[240:243], a[16:19], v[176:179]
  ds_read_b64_tr_b16 a[210:211], v228 offset:20480
  v_mfma_f32_16x16x32_bf16 v[208:211], a[240:243], a[20:23], v[208:211]
  s_waitcnt lgkmcnt(4)
  buffer_load_dwordx4 a[88:91], v13, s[16:19], s42 offen sc1
  v_mfma_f32_16x16x32_bf16 v[148:151], a[244:247], a[12:15], v[148:151]
  ds_read_b64_tr_b16 a[212:213], v229 offset:16384
  v_mfma_f32_16x16x32_bf16 v[180:183], a[244:247], a[16:19], v[180:183]
  ds_read_b64_tr_b16 a[214:215], v229 offset:20480
  v_mfma_f32_16x16x32_bf16 v[212:215], a[244:247], a[20:23], v[212:215]
  buffer_load_dwordx4 a[92:95], v14, s[16:19], s42 offen sc1
  v_mfma_f32_16x16x32_bf16 v[152:155], a[248:251], a[12:15], v[152:155]
  ds_read_b64_tr_b16 a[216:217], v230 offset:16384
  v_mfma_f32_16x16x32_bf16 v[184:187], a[248:251], a[16:19], v[184:187]
  ds_read_b64_tr_b16 a[218:219], v230 offset:20480
  v_mfma_f32_16x16x32_bf16 v[216:219], a[248:251], a[20:23], v[216:219]
  ds_read_b64_tr_b16 a[220:221], v231 offset:16384
  v_mfma_f32_16x16x32_bf16 v[156:159], a[252:255], a[12:15], v[156:159]
  ds_read_b64_tr_b16 a[222:223], v231 offset:20480
  v_mfma_f32_16x16x32_bf16 v[188:191], a[252:255], a[16:19], v[188:191]
  v_mfma_f32_16x16x32_bf16 v[220:223], a[252:255], a[20:23], v[220:223]
  s_waitcnt vmcnt(16)
  s_barrier
  v_mfma_f32_16x16x32_bf16 v[136:139], a[200:203], a[24:27], v[136:139]
  ds_read_b64_tr_b16 a[232:233], v226 offset:24576
  v_mfma_f32_16x16x32_bf16 v[168:171], a[200:203], a[28:31], v[168:171]
  ds_read_b64_tr_b16 a[234:235], v226 offset:28672
  v_mfma_f32_16x16x32_bf16 v[200:203], a[200:203], a[32:35], v[200:203]
  s_waitcnt lgkmcnt(3)
  ds_read_b64_tr_b16 a[236:237], v227 offset:24576
  v_mfma_f32_16x16x32_bf16 v[140:143], a[204:207], a[24:27], v[140:143]
  ds_read_b64_tr_b16 a[238:239], v227 offset:28672
  v_mfma_f32_16x16x32_bf16 v[172:175], a[204:207], a[28:31], v[172:175]
  ds_read_b64_tr_b16 a[240:241], v228 offset:24576
  v_mfma_f32_16x16x32_bf16 v[204:207], a[204:207], a[32:35], v[204:207]
  ds_read_b64_tr_b16 a[242:243], v228 offset:28672
  v_mfma_f32_16x16x32_bf16 v[144:147], a[208:211], a[24:27], v[144:147]
  ds_read_b64_tr_b16 a[244:245], v229 offset:24576
  v_mfma_f32_16x16x32_bf16 v[176:179], a[208:211], a[28:31], v[176:179]
  ds_read_b64_tr_b16 a[246:247], v229 offset:28672
  v_mfma_f32_16x16x32_bf16 v[208:211], a[208:211], a[32:35], v[208:211]
  ds_read_b64_tr_b16 a[248:249], v230 offset:24576
  v_mfma_f32_16x16x32_bf16 v[148:151], a[212:215], a[24:27], v[148:151]
  ds_read_b64_tr_b16 a[250:251], v230 offset:28672
  v_mfma_f32_16x16x32_bf16 v[180:183], a[212:215], a[28:31], v[180:183]
  ds_read_b64_tr_b16 a[252:253], v231 offset:24576
  v_mfma_f32_16x16x32_bf16 v[212:215], a[212:215], a[32:35], v[212:215]
  ds_read_b64_tr_b16 a[254:255], v231 offset:28672
  v_mfma_f32_16x16x32_bf16 v[152:155], a[216:219], a[24:27], v[152:155]
  v_mfma_f32_16x16x32_bf16 v[184:187], a[216:219], a[28:31], v[184:187]
  v_mfma_f32_16x16x32_bf16 v[216:219], a[216:219], a[32:35], v[216:219]
  s_waitcnt lgkmcnt(3)
  v_mfma_f32_16x16x32_bf16 v[156:159], a[220:223], a[24:27], v[156:159]
  v_mfma_f32_16x16x32_bf16 v[188:191], a[220:223], a[28:31], v[188:191]
  v_mfma_f32_16x16x32_bf16 v[220:223], a[220:223], a[32:35], v[220:223]
  v_mfma_f32_16x16x32_bf16 v[136:139], a[232:235], a[36:39], v[136:139]
  ds_read_b64_tr_b16 a[200:201], v226 offset:32768
  v_mfma_f32_16x16x32_bf16 v[168:171], a[232:235], a[40:43], v[168:171]
  ds_read_b64_tr_b16 a[202:203], v226 offset:36864
  v_mfma_f32_16x16x32_bf16 v[200:203], a[232:235], a[44:47], v[200:203]
  ds_read_b64_tr_b16 a[204:205], v227 offset:32768
  v_mfma_f32_16x16x32_bf16 v[140:143], a[236:239], a[36:39], v[140:143]
  ds_read_b64_tr_b16 a[206:207], v227 offset:36864
  v_mfma_f32_16x16x32_bf16 v[172:175], a[236:239], a[40:43], v[172:175]
  ds_read_b64_tr_b16 a[208:209], v228 offset:32768
  v_mfma_f32_16x16x32_bf16 v[204:207], a[236:239], a[44:47], v[204:207]
  ds_read_b64_tr_b16 a[210:211], v228 offset:36864
  v_mfma_f32_16x16x32_bf16 v[144:147], a[240:243], a[36:39], v[144:147]
  ds_read_b64_tr_b16 a[212:213], v229 offset:32768
  v_mfma_f32_16x16x32_bf16 v[176:179], a[240:243], a[40:43], v[176:179]
  ds_read_b64_tr_b16 a[214:215], v229 offset:36864
  v_mfma_f32_16x16x32_bf16 v[208:211], a[240:243], a[44:47], v[208:211]
  ds_read_b64_tr_b16 a[216:217], v230 offset:32768
  v_mfma_f32_16x16x32_bf16 v[148:151], a[244:247], a[36:39], v[148:151]
  ds_read_b64_tr_b16 a[218:219], v230 offset:36864
  v_mfma_f32_16x16x32_bf16 v[180:183], a[244:247], a[40:43], v[180:183]
  ds_read_b64_tr_b16 a[220:221], v231 offset:32768
  v_mfma_f32_16x16x32_bf16 v[212:215], a[244:247], a[44:47], v[212:215]
  s_waitcnt lgkmcnt(5)
  ds_read_b64_tr_b16 a[222:223], v231 offset:36864
  v_mfma_f32_16x16x32_bf16 v[152:155], a[248:251], a[36:39], v[152:155]
  v_mfma_f32_16x16x32_bf16 v[184:187], a[248:251], a[40:43], v[184:187]
  v_mfma_f32_16x16x32_bf16 v[216:219], a[248:251], a[44:47], v[216:219]
  v_mfma_f32_16x16x32_bf16 v[156:159], a[252:255], a[36:39], v[156:159]
  v_mfma_f32_16x16x32_bf16 v[188:191], a[252:255], a[40:43], v[188:191]
  v_mfma_f32_16x16x32_bf16 v[220:223], a[252:255], a[44:47], v[220:223]
  s_waitcnt vmcnt(6)
  s_barrier
  v_mfma_f32_16x16x32_bf16 v[136:139], a[200:203], a[48:51], v[136:139]
  ds_read_b64_tr_b16 a[232:233], v226 offset:40960
  v_mfma_f32_16x16x32_bf16 v[168:171], a[200:203], a[52:55], v[168:171]
  ds_read_b64_tr_b16 a[234:235], v226 offset:45056
  v_mfma_f32_16x16x32_bf16 v[200:203], a[200:203], a[56:59], v[200:203]
  ds_read_b64_tr_b16 a[236:237], v227 offset:40960
  v_mfma_f32_16x16x32_bf16 v[140:143], a[204:207], a[48:51], v[140:143]
  ds_read_b64_tr_b16 a[238:239], v227 offset:45056
  v_mfma_f32_16x16x32_bf16 v[172:175], a[204:207], a[52:55], v[172:175]
  ds_read_b64_tr_b16 a[240:241], v228 offset:40960
  v_mfma_f32_16x16x32_bf16 v[204:207], a[204:207], a[56:59], v[204:207]
  ds_read_b64_tr_b16 a[242:243], v228 offset:45056
  v_mfma_f32_16x16x32_bf16 v[144:147], a[208:211], a[48:51], v[144:147]
  ds_read_b64_tr_b16 a[244:245], v229 offset:40960
  v_mfma_f32_16x16x32_bf16 v[176:179], a[208:211], a[52:55], v[176:179]
  ds_read_b64_tr_b16 a[246:247], v229 offset:45056
  v_mfma_f32_16x16x32_bf16 v[208:211], a[208:211], a[56:59], v[208:211]
  s_waitcnt lgkmcnt(5)
  ds_read_b64_tr_b16 a[248:249], v230 offset:40960
  v_mfma_f32_16x16x32_bf16 v[148:151], a[212:215], a[48:51], v[148:151]
  ds_read_b64_tr_b16 a[250:251], v230 offset:45056
  v_mfma_f32_16x16x32_bf16 v[180:183], a[212:215], a[52:55], v[180:183]
  ds_read_b64_tr_b16 a[252:253], v231 offset:40960
  v_mfma_f32_16x16x32_bf16 v[212:215], a[212:215], a[56:59], v[212:215]
  ds_read_b64_tr_b16 a[254:255], v231 offset:45056
  v_mfma_f32_16x16x32_bf16 v[152:155], a[216:219], a[48:51], v[152:155]
  v_mfma_f32_16x16x32_bf16 v[184:187], a[216:219], a[52:55], v[184:187]
  v_mfma_f32_16x16x32_bf16 v[216:219], a[216:219], a[56:59], v[216:219]
  v_mfma_f32_16x16x32_bf16 v[156:159], a[220:223], a[48:51], v[156:159]
  v_mfma_f32_16x16x32_bf16 v[188:191], a[220:223], a[52:55], v[188:191]
  v_mfma_f32_16x16x32_bf16 v[220:223], a[220:223], a[56:59], v[220:223]
  v_mfma_f32_16x16x32_bf16 v[136:139], a[232:235], a[60:63], v[136:139]
  ds_read_b64_tr_b16 a[200:201], v226 offset:49152
  v_mfma_f32_16x16x32_bf16 v[168:171], a[232:235], a[64:67], v[168:171]
  ds_read_b64_tr_b16 a[202:203], v226 offset:53248
  v_mfma_f32_16x16x32_bf16 v[200:203], a[232:235], a[68:71], v[200:203]
  s_waitcnt lgkmcnt(2)
  ds_read_b64_tr_b16 a[204:205], v227 offset:49152
  v_mfma_f32_16x16x32_bf16 v[140:143], a[236:239], a[60:63], v[140:143]
  ds_read_b64_tr_b16 a[206:207], v227 offset:53248
  v_mfma_f32_16x16x32_bf16 v[172:175], a[236:239], a[64:67], v[172:175]
  ds_read_b64_tr_b16 a[208:209], v228 offset:49152
  v_mfma_f32_16x16x32_bf16 v[204:207], a[236:239], a[68:71], v[204:207]
  ds_read_b64_tr_b16 a[210:211], v228 offset:53248
  v_mfma_f32_16x16x32_bf16 v[144:147], a[240:243], a[60:63], v[144:147]
  ds_read_b64_tr_b16 a[212:213], v229 offset:49152
  v_mfma_f32_16x16x32_bf16 v[176:179], a[240:243], a[64:67], v[176:179]
  ds_read_b64_tr_b16 a[214:215], v229 offset:53248
  v_mfma_f32_16x16x32_bf16 v[208:211], a[240:243], a[68:71], v[208:211]
  ds_read_b64_tr_b16 a[216:217], v230 offset:49152
  v_mfma_f32_16x16x32_bf16 v[148:151], a[244:247], a[60:63], v[148:151]
  ds_read_b64_tr_b16 a[218:219], v230 offset:53248
  v_mfma_f32_16x16x32_bf16 v[180:183], a[244:247], a[64:67], v[180:183]
  ds_read_b64_tr_b16 a[220:221], v231 offset:49152
  v_mfma_f32_16x16x32_bf16 v[212:215], a[244:247], a[68:71], v[212:215]
  ds_read_b64_tr_b16 a[222:223], v231 offset:53248
  v_mfma_f32_16x16x32_bf16 v[152:155], a[248:251], a[60:63], v[152:155]
  v_mfma_f32_16x16x32_bf16 v[184:187], a[248:251], a[64:67], v[184:187]
  v_mfma_f32_16x16x32_bf16 v[216:219], a[248:251], a[68:71], v[216:219]
  v_mfma_f32_16x16x32_bf16 v[156:159], a[252:255], a[60:63], v[156:159]
  v_mfma_f32_16x16x32_bf16 v[188:191], a[252:255], a[64:67], v[188:191]
  v_mfma_f32_16x16x32_bf16 v[220:223], a[252:255], a[68:71], v[220:223]
  s_waitcnt lgkmcnt(0)
  s_waitcnt vmcnt(0)
  v_mfma_f32_16x16x32_bf16 v[136:139], a[200:203], a[72:75], v[136:139]
  ds_read_b64_tr_b16 a[232:233], v226 offset:57344
  v_mfma_f32_16x16x32_bf16 v[168:171], a[200:203], a[76:79], v[168:171]
  ds_read_b64_tr_b16 a[234:235], v226 offset:61440
  v_mfma_f32_16x16x32_bf16 v[200:203], a[200:203], a[80:83], v[200:203]
  ds_read_b64_tr_b16 a[236:237], v227 offset:57344
  v_mfma_f32_16x16x32_bf16 v[140:143], a[204:207], a[72:75], v[140:143]
  ds_read_b64_tr_b16 a[238:239], v227 offset:61440
  v_mfma_f32_16x16x32_bf16 v[172:175], a[204:207], a[76:79], v[172:175]
  ds_read_b64_tr_b16 a[240:241], v228 offset:57344
  v_mfma_f32_16x16x32_bf16 v[204:207], a[204:207], a[80:83], v[204:207]
  ds_read_b64_tr_b16 a[242:243], v228 offset:61440
  v_mfma_f32_16x16x32_bf16 v[144:147], a[208:211], a[72:75], v[144:147]
  ds_read_b64_tr_b16 a[244:245], v229 offset:57344
  v_mfma_f32_16x16x32_bf16 v[176:179], a[208:211], a[76:79], v[176:179]
  ds_read_b64_tr_b16 a[246:247], v229 offset:61440
  v_mfma_f32_16x16x32_bf16 v[208:211], a[208:211], a[80:83], v[208:211]
  ds_read_b64_tr_b16 a[248:249], v230 offset:57344
  v_mfma_f32_16x16x32_bf16 v[148:151], a[212:215], a[72:75], v[148:151]
  ds_read_b64_tr_b16 a[250:251], v230 offset:61440
  v_mfma_f32_16x16x32_bf16 v[180:183], a[212:215], a[76:79], v[180:183]
  ds_read_b64_tr_b16 a[252:253], v231 offset:57344
  v_mfma_f32_16x16x32_bf16 v[212:215], a[212:215], a[80:83], v[212:215]
  ds_read_b64_tr_b16 a[254:255], v231 offset:61440
  v_mfma_f32_16x16x32_bf16 v[152:155], a[216:219], a[72:75], v[152:155]
  v_mfma_f32_16x16x32_bf16 v[184:187], a[216:219], a[76:79], v[184:187]
  v_mfma_f32_16x16x32_bf16 v[216:219], a[216:219], a[80:83], v[216:219]
  v_mfma_f32_16x16x32_bf16 v[156:159], a[220:223], a[72:75], v[156:159]
  v_mfma_f32_16x16x32_bf16 v[188:191], a[220:223], a[76:79], v[188:191]
  v_mfma_f32_16x16x32_bf16 v[220:223], a[220:223], a[80:83], v[220:223]
  s_waitcnt lgkmcnt(0)
  v_mfma_f32_16x16x32_bf16 v[136:139], a[232:235], a[84:87], v[136:139]
  ds_read_b64_tr_b16 a[192:193], v3
  v_mfma_f32_16x16x32_bf16 v[168:171], a[232:235], a[88:91], v[168:171]
  ds_read_b64_tr_b16 a[194:195], v3 offset:4096
  v_mfma_f32_16x16x32_bf16 v[200:203], a[232:235], a[92:95], v[200:203]
  ds_read_b64_tr_b16 a[196:197], v4
  v_mfma_f32_16x16x32_bf16 v[140:143], a[236:239], a[84:87], v[140:143]
  ds_read_b64_tr_b16 a[198:199], v4 offset:4096
  v_mfma_f32_16x16x32_bf16 v[172:175], a[236:239], a[88:91], v[172:175]
  ds_read_b64_tr_b16 a[224:225], v3 offset:8192
  v_mfma_f32_16x16x32_bf16 v[204:207], a[236:239], a[92:95], v[204:207]
  ds_read_b64_tr_b16 a[226:227], v3 offset:12288
  v_mfma_f32_16x16x32_bf16 v[144:147], a[240:243], a[84:87], v[144:147]
  ds_read_b64_tr_b16 a[228:229], v4 offset:8192
  v_mfma_f32_16x16x32_bf16 v[176:179], a[240:243], a[88:91], v[176:179]
  ds_read_b64_tr_b16 a[230:231], v4 offset:12288
  v_mfma_f32_16x16x32_bf16 v[208:211], a[240:243], a[92:95], v[208:211]
  v_mfma_f32_16x16x32_bf16 v[148:151], a[244:247], a[84:87], v[148:151]
  v_mfma_f32_16x16x32_bf16 v[180:183], a[244:247], a[88:91], v[180:183]
  v_mfma_f32_16x16x32_bf16 v[212:215], a[244:247], a[92:95], v[212:215]
  v_mfma_f32_16x16x32_bf16 v[152:155], a[248:251], a[84:87], v[152:155]
  v_mfma_f32_16x16x32_bf16 v[184:187], a[248:251], a[88:91], v[184:187]
  v_mfma_f32_16x16x32_bf16 v[216:219], a[248:251], a[92:95], v[216:219]
  v_mfma_f32_16x16x32_bf16 v[156:159], a[252:255], a[84:87], v[156:159]
  v_mfma_f32_16x16x32_bf16 v[188:191], a[252:255], a[88:91], v[188:191]
  v_mfma_f32_16x16x32_bf16 v[220:223], a[252:255], a[92:95], v[220:223]
  s_waitcnt lgkmcnt(0)
  v_mfma_f32_16x16x32_bf16 v[128:131], a[192:195], v[32:35], v[128:131]
  v_mfma_f32_16x16x32_bf16 v[160:163], a[192:195], v[36:39], v[160:163]
  s_mul_i32 s76, s36, 3072
  s_add_u32 s74, s44, 1
  v_mfma_f32_16x16x32_bf16 v[192:195], a[192:195], v[40:43], v[192:195]
  s_and_b32 s74, s74, 3
  s_mul_i32 s74, s74, 36864
  v_mfma_f32_16x16x32_bf16 v[132:135], a[196:199], v[32:35], v[132:135]
  ds_read_b64_tr_b16 a[192:193], v3 offset:16384
  ds_read_b64_tr_b16 a[194:195], v3 offset:20480
  s_add_u32 s74, s74, s76
  s_add_u32 s74, s74, 0
  v_mfma_f32_16x16x32_bf16 v[164:167], a[196:199], v[36:39], v[164:167]
  v_cvt_pk_bf16_f32 v232, v136, v137
  v_cvt_pk_bf16_f32 v233, v138, v139
  v_mfma_f32_16x16x32_bf16 v[196:199], a[196:199], v[40:43], v[196:199]
  v_cvt_pk_bf16_f32 v234, v140, v141
  v_cvt_pk_bf16_f32 v235, v142, v143
  v_mfma_f32_16x16x32_bf16 v[128:131], a[224:227], v[44:47], v[128:131]
  ds_read_b64_tr_b16 a[196:197], v4 offset:16384
  ds_read_b64_tr_b16 a[198:199], v4 offset:20480
  buffer_store_dwordx4 v[232:235], v0, s[28:31], s74 offen offset:0 sc1
  v_cvt_pk_bf16_f32 v236, v168, v169
  v_mfma_f32_16x16x32_bf16 v[160:163], a[224:227], v[48:51], v[160:163]
  v_cvt_pk_bf16_f32 v237, v170, v171
  v_cvt_pk_bf16_f32 v238, v172, v173
  v_mfma_f32_16x16x32_bf16 v[192:195], a[224:227], v[52:55], v[192:195]
  v_cvt_pk_bf16_f32 v239, v174, v175
  buffer_store_dwordx4 v[236:239], v0, s[28:31], s74 offen offset:1024 sc1
  v_mfma_f32_16x16x32_bf16 v[132:135], a[228:231], v[44:47], v[132:135]
  ds_read_b64_tr_b16 a[224:225], v3 offset:24576
  ds_read_b64_tr_b16 a[226:227], v3 offset:28672
  v_cvt_pk_bf16_f32 v240, v200, v201
  v_cvt_pk_bf16_f32 v241, v202, v203
  v_mfma_f32_16x16x32_bf16 v[164:167], a[228:231], v[48:51], v[164:167]
  v_cvt_pk_bf16_f32 v242, v204, v205
  v_cvt_pk_bf16_f32 v243, v206, v207
  v_mfma_f32_16x16x32_bf16 v[196:199], a[228:231], v[52:55], v[196:199]
  s_waitcnt lgkmcnt(4)
  buffer_store_dwordx4 v[240:243], v0, s[28:31], s74 offen offset:2048 sc1
  s_add_u32 s74, s44, 2
  v_mfma_f32_16x16x32_bf16 v[128:131], a[192:195], v[56:59], v[128:131]
  ds_read_b64_tr_b16 a[228:229], v4 offset:24576
  ds_read_b64_tr_b16 a[230:231], v4 offset:28672
  s_and_b32 s74, s74, 3
  s_mul_i32 s74, s74, 36864
  v_mfma_f32_16x16x32_bf16 v[160:163], a[192:195], v[60:63], v[160:163]
  s_add_u32 s74, s74, s76
  s_add_u32 s74, s74, 12288
  v_mfma_f32_16x16x32_bf16 v[192:195], a[192:195], v[64:67], v[192:195]
  s_waitcnt lgkmcnt(4)
  v_cvt_pk_bf16_f32 v232, v144, v145
  v_cvt_pk_bf16_f32 v233, v146, v147
  v_mfma_f32_16x16x32_bf16 v[132:135], a[196:199], v[56:59], v[132:135]
  ds_read_b64_tr_b16 a[192:193], v3 offset:32768
  ds_read_b64_tr_b16 a[194:195], v3 offset:36864
  v_cvt_pk_bf16_f32 v234, v148, v149
  v_cvt_pk_bf16_f32 v235, v150, v151
  v_mfma_f32_16x16x32_bf16 v[164:167], a[196:199], v[60:63], v[164:167]
  buffer_store_dwordx4 v[232:235], v0, s[28:31], s74 offen offset:0 sc1
  v_cvt_pk_bf16_f32 v236, v176, v177
  v_mfma_f32_16x16x32_bf16 v[196:199], a[196:199], v[64:67], v[196:199]
  s_waitcnt lgkmcnt(4)
  v_cvt_pk_bf16_f32 v237, v178, v179
  v_cvt_pk_bf16_f32 v238, v180, v181
  v_mfma_f32_16x16x32_bf16 v[128:131], a[224:227], v[68:71], v[128:131]
  ds_read_b64_tr_b16 a[196:197], v4 offset:32768
  ds_read_b64_tr_b16 a[198:199], v4 offset:36864
  v_cvt_pk_bf16_f32 v239, v182, v183
  buffer_store_dwordx4 v[236:239], v0, s[28:31], s74 offen offset:1024 sc1
  v_mfma_f32_16x16x32_bf16 v[160:163], a[224:227], v[72:75], v[160:163]
  v_cvt_pk_bf16_f32 v240, v208, v209
  v_cvt_pk_bf16_f32 v241, v210, v211
  v_mfma_f32_16x16x32_bf16 v[192:195], a[224:227], v[76:79], v[192:195]
  s_waitcnt lgkmcnt(4)
  v_cvt_pk_bf16_f32 v242, v212, v213
  v_cvt_pk_bf16_f32 v243, v214, v215
  v_mfma_f32_16x16x32_bf16 v[132:135], a[228:231], v[68:71], v[132:135]
  ds_read_b64_tr_b16 a[224:225], v3 offset:40960
  ds_read_b64_tr_b16 a[226:227], v3 offset:45056
  buffer_store_dwordx4 v[240:243], v0, s[28:31], s74 offen offset:2048 sc1
  s_add_u32 s74, s44, 3
  v_mfma_f32_16x16x32_bf16 v[164:167], a[228:231], v[72:75], v[164:167]
  s_and_b32 s74, s74, 3
  s_mul_i32 s74, s74, 36864
  v_mfma_f32_16x16x32_bf16 v[196:199], a[228:231], v[76:79], v[196:199]
  s_waitcnt lgkmcnt(4)
  s_add_u32 s74, s74, s76
  s_add_u32 s74, s74, 24576
  v_mfma_f32_16x16x32_bf16 v[128:131], a[192:195], v[80:83], v[128:131]
  ds_read_b64_tr_b16 a[228:229], v4 offset:40960
  ds_read_b64_tr_b16 a[230:231], v4 offset:45056
  v_cvt_pk_bf16_f32 v232, v152, v153
  v_cvt_pk_bf16_f32 v233, v154, v155
  v_mfma_f32_16x16x32_bf16 v[160:163], a[192:195], v[84:87], v[160:163]
  v_cvt_pk_bf16_f32 v234, v156, v157
  v_cvt_pk_bf16_f32 v235, v158, v159
  v_mfma_f32_16x16x32_bf16 v[192:195], a[192:195], v[88:91], v[192:195]
  s_waitcnt lgkmcnt(4)
  buffer_store_dwordx4 v[232:235], v0, s[28:31], s74 offen offset:0 sc1
  v_cvt_pk_bf16_f32 v236, v184, v185
  v_mfma_f32_16x16x32_bf16 v[132:135], a[196:199], v[80:83], v[132:135]
  ds_read_b64_tr_b16 a[192:193], v3 offset:49152
  ds_read_b64_tr_b16 a[194:195], v3 offset:53248
  v_cvt_pk_bf16_f32 v237, v186, v187
  v_cvt_pk_bf16_f32 v238, v188, v189
  v_mfma_f32_16x16x32_bf16 v[164:167], a[196:199], v[84:87], v[164:167]
  v_cvt_pk_bf16_f32 v239, v190, v191
  buffer_store_dwordx4 v[236:239], v0, s[28:31], s74 offen offset:1024 sc1
  v_mfma_f32_16x16x32_bf16 v[196:199], a[196:199], v[88:91], v[196:199]
  s_waitcnt lgkmcnt(4)
  v_cvt_pk_bf16_f32 v240, v216, v217
  v_cvt_pk_bf16_f32 v241, v218, v219
  v_mfma_f32_16x16x32_bf16 v[128:131], a[224:227], v[92:95], v[128:131]
  ds_read_b64_tr_b16 a[196:197], v4 offset:49152
  ds_read_b64_tr_b16 a[198:199], v4 offset:53248
  v_cvt_pk_bf16_f32 v242, v220, v221
  v_cvt_pk_bf16_f32 v243, v222, v223
  v_mfma_f32_16x16x32_bf16 v[160:163], a[224:227], v[96:99], v[160:163]
  buffer_store_dwordx4 v[240:243], v0, s[28:31], s74 offen offset:2048 sc1
  v_mfma_f32_16x16x32_bf16 v[192:195], a[224:227], v[100:103], v[192:195]
  s_waitcnt lgkmcnt(4)
  v_mfma_f32_16x16x32_bf16 v[132:135], a[228:231], v[92:95], v[132:135]
  ds_read_b64_tr_b16 a[224:225], v3 offset:57344
  ds_read_b64_tr_b16 a[226:227], v3 offset:61440
  v_mfma_f32_16x16x32_bf16 v[164:167], a[228:231], v[96:99], v[164:167]
  v_mfma_f32_16x16x32_bf16 v[196:199], a[228:231], v[100:103], v[196:199]
  s_waitcnt lgkmcnt(4)
  v_mfma_f32_16x16x32_bf16 v[128:131], a[192:195], v[104:107], v[128:131]
  ds_read_b64_tr_b16 a[228:229], v4 offset:57344
  ds_read_b64_tr_b16 a[230:231], v4 offset:61440
  v_mfma_f32_16x16x32_bf16 v[160:163], a[192:195], v[108:111], v[160:163]
  v_mfma_f32_16x16x32_bf16 v[192:195], a[192:195], v[112:115], v[192:195]
  s_waitcnt lgkmcnt(4)
  v_mfma_f32_16x16x32_bf16 v[132:135], a[196:199], v[104:107], v[132:135]
  ds_read_b64_tr_b16 a[192:193], v224
  ds_read_b64_tr_b16 a[194:195], v224 offset:4096
  v_mfma_f32_16x16x32_bf16 v[164:167], a[196:199], v[108:111], v[164:167]
  v_mfma_f32_16x16x32_bf16 v[196:199], a[196:199], v[112:115], v[196:199]
  s_waitcnt lgkmcnt(4)
  v_mfma_f32_16x16x32_bf16 v[128:131], a[224:227], v[116:119], v[128:131]
  ds_read_b64_tr_b16 a[196:197], v225
  ds_read_b64_tr_b16 a[198:199], v225 offset:4096
  v_mfma_f32_16x16x32_bf16 v[160:163], a[224:227], v[120:123], v[160:163]
  v_mfma_f32_16x16x32_bf16 v[192:195], a[224:227], v[124:127], v[192:195]
  s_waitcnt lgkmcnt(4)
  v_mfma_f32_16x16x32_bf16 v[132:135], a[228:231], v[116:119], v[132:135]
  ds_read_b64_tr_b16 a[224:225], v224 offset:8192
  ds_read_b64_tr_b16 a[226:227], v224 offset:12288
  v_mfma_f32_16x16x32_bf16 v[164:167], a[228:231], v[120:123], v[164:167]
  v_mfma_f32_16x16x32_bf16 v[196:199], a[228:231], v[124:127], v[196:199]
  s_waitcnt lgkmcnt(4)
  v_mfma_f32_16x16x32_bf16 v[128:131], a[192:195], a[0:3], v[128:131]
  ds_read_b64_tr_b16 a[228:229], v225 offset:8192
  ds_read_b64_tr_b16 a[230:231], v225 offset:12288
  v_mfma_f32_16x16x32_bf16 v[160:163], a[192:195], a[4:7], v[160:163]
  v_mfma_f32_16x16x32_bf16 v[192:195], a[192:195], a[8:11], v[192:195]
  s_waitcnt lgkmcnt(4)
  v_mfma_f32_16x16x32_bf16 v[132:135], a[196:199], a[0:3], v[132:135]
  ds_read_b64_tr_b16 a[192:193], v224 offset:16384
  ds_read_b64_tr_b16 a[194:195], v224 offset:20480
  v_mfma_f32_16x16x32_bf16 v[164:167], a[196:199], a[4:7], v[164:167]
  v_mfma_f32_16x16x32_bf16 v[196:199], a[196:199], a[8:11], v[196:199]
  s_waitcnt lgkmcnt(4)
  v_mfma_f32_16x16x32_bf16 v[128:131], a[224:227], a[12:15], v[128:131]
  ds_read_b64_tr_b16 a[196:197], v225 offset:16384
  ds_read_b64_tr_b16 a[198:199], v225 offset:20480
  v_mfma_f32_16x16x32_bf16 v[160:163], a[224:227], a[16:19], v[160:163]
  v_mfma_f32_16x16x32_bf16 v[192:195], a[224:227], a[20:23], v[192:195]
  s_waitcnt lgkmcnt(4)
  v_mfma_f32_16x16x32_bf16 v[132:135], a[228:231], a[12:15], v[132:135]
  ds_read_b64_tr_b16 a[224:225], v224 offset:24576
  ds_read_b64_tr_b16 a[226:227], v224 offset:28672
  v_mfma_f32_16x16x32_bf16 v[164:167], a[228:231], a[16:19], v[164:167]
  v_mfma_f32_16x16x32_bf16 v[196:199], a[228:231], a[20:23], v[196:199]
  s_waitcnt lgkmcnt(4)
  v_mfma_f32_16x16x32_bf16 v[128:131], a[192:195], a[24:27], v[128:131]
  ds_read_b64_tr_b16 a[228:229], v225 offset:24576
  ds_read_b64_tr_b16 a[230:231], v225 offset:28672
  v_mfma_f32_16x16x32_bf16 v[160:163], a[192:195], a[28:31], v[160:163]
  v_mfma_f32_16x16x32_bf16 v[192:195], a[192:195], a[32:35], v[192:195]
  s_waitcnt lgkmcnt(4)
  v_mfma_f32_16x16x32_bf16 v[132:135], a[196:199], a[24:27], v[132:135]
  ds_read_b64_tr_b16 a[192:193], v224 offset:32768
  ds_read_b64_tr_b16 a[194:195], v224 offset:36864
  v_mfma_f32_16x16x32_bf16 v[164:167], a[196:199], a[28:31], v[164:167]
  v_mfma_f32_16x16x32_bf16 v[196:199], a[196:199], a[32:35], v[196:199]
  s_waitcnt lgkmcnt(4)
  v_mfma_f32_16x16x32_bf16 v[128:131], a[224:227], a[36:39], v[128:131]
  ds_read_b64_tr_b16 a[196:197], v225 offset:32768
  ds_read_b64_tr_b16 a[198:199], v225 offset:36864
  v_mfma_f32_16x16x32_bf16 v[160:163], a[224:227], a[40:43], v[160:163]
  v_mfma_f32_16x16x32_bf16 v[192:195], a[224:227], a[44:47], v[192:195]
  s_waitcnt lgkmcnt(4)
  v_mfma_f32_16x16x32_bf16 v[132:135], a[228:231], a[36:39], v[132:135]
  ds_read_b64_tr_b16 a[224:225], v224 offset:40960
  ds_read_b64_tr_b16 a[226:227], v224 offset:45056
  v_mfma_f32_16x16x32_bf16 v[164:167], a[228:231], a[40:43], v[164:167]
  v_mfma_f32_16x16x32_bf16 v[196:199], a[228:231], a[44:47], v[196:199]
  s_waitcnt lgkmcnt(4)
  v_mfma_f32_16x16x32_bf16 v[128:131], a[192:195], a[48:51], v[128:131]
  ds_read_b64_tr_b16 a[228:229], v225 offset:40960
  ds_read_b64_tr_b16 a[230:231], v225 offset:45056
  v_mfma_f32_16x16x32_bf16 v[160:163], a[192:195], a[52:55], v[160:163]
  v_mfma_f32_16x16x32_bf16 v[192:195], a[192:195], a[56:59], v[192:195]
  s_waitcnt lgkmcnt(4)
  v_mfma_f32_16x16x32_bf16 v[132:135], a[196:199], a[48:51], v[132:135]
  ds_read_b64_tr_b16 a[192:193], v224 offset:49152
  ds_read_b64_tr_b16 a[194:195], v224 offset:53248
  v_mfma_f32_16x16x32_bf16 v[164:167], a[196:199], a[52:55], v[164:167]
  v_mfma_f32_16x16x32_bf16 v[196:199], a[196:199], a[56:59], v[196:199]
  s_waitcnt lgkmcnt(4)
  v_mfma_f32_16x16x32_bf16 v[128:131], a[224:227], a[60:63], v[128:131]
  ds_read_b64_tr_b16 a[196:197], v225 offset:49152
  ds_read_b64_tr_b16 a[198:199], v225 offset:53248
  v_mfma_f32_16x16x32_bf16 v[160:163], a[224:227], a[64:67], v[160:163]
  v_mfma_f32_16x16x32_bf16 v[192:195], a[224:227], a[68:71], v[192:195]
  s_waitcnt lgkmcnt(4)
  v_mfma_f32_16x16x32_bf16 v[132:135], a[228:231], a[60:63], v[132:135]
  ds_read_b64_tr_b16 a[224:225], v224 offset:57344
  ds_read_b64_tr_b16 a[226:227], v224 offset:61440
  v_mfma_f32_16x16x32_bf16 v[164:167], a[228:231], a[64:67], v[164:167]
  v_mfma_f32_16x16x32_bf16 v[196:199], a[228:231], a[68:71], v[196:199]
  s_waitcnt lgkmcnt(4)
  v_mfma_f32_16x16x32_bf16 v[128:131], a[192:195], a[72:75], v[128:131]
  ds_read_b64_tr_b16 a[228:229], v225 offset:57344
  ds_read_b64_tr_b16 a[230:231], v225 offset:61440
  v_mfma_f32_16x16x32_bf16 v[160:163], a[192:195], a[76:79], v[160:163]
  v_mfma_f32_16x16x32_bf16 v[192:195], a[192:195], a[80:83], v[192:195]
  s_waitcnt lgkmcnt(4)
  v_mfma_f32_16x16x32_bf16 v[132:135], a[196:199], a[72:75], v[132:135]
  v_mfma_f32_16x16x32_bf16 v[164:167], a[196:199], a[76:79], v[164:167]
  v_mfma_f32_16x16x32_bf16 v[196:199], a[196:199], a[80:83], v[196:199]
  s_waitcnt lgkmcnt(2)
  v_mfma_f32_16x16x32_bf16 v[128:131], a[224:227], a[84:87], v[128:131]
  v_mfma_f32_16x16x32_bf16 v[160:163], a[224:227], a[88:91], v[160:163]
  v_mfma_f32_16x16x32_bf16 v[192:195], a[224:227], a[92:95], v[192:195]
  s_waitcnt lgkmcnt(0)
  v_mfma_f32_16x16x32_bf16 v[132:135], a[228:231], a[84:87], v[132:135]
  v_mfma_f32_16x16x32_bf16 v[164:167], a[228:231], a[88:91], v[164:167]
  v_mfma_f32_16x16x32_bf16 v[196:199], a[228:231], a[92:95], v[196:199]
  s_lshl_b32 s74, s47, 7
  s_add_u32 s74, s74, 0x8000
  s_lshl_b32 s75, s44, 4
  s_add_u32 s75, s75, s74
  v_mov_b32_e32 v27, s75
  v_add_u32_e32 v26, s74, v0
  s_waitcnt vmcnt(0) lgkmcnt(0)
  s_barrier
  s_cmp_lg_u32 s36, 0
  s_cbranch_scc1 .Lk2sync%=
  s_mov_b64 exec, 1
  v_mov_b32_e32 v250, s82
  v_mov_b32_e32 v251, s83
  global_store_dwordx2 v27, v[250:251], s[92:93] sc1
  s_mov_b64 exec, -1
  s_nop 0
  s_mov_b64 exec, 0xf
  s_mov_b32 s73, 0x20000
.Lxwa%=:
  global_load_dwordx2 v[246:247], v26, s[92:93] sc1
  s_waitcnt vmcnt(0)
.Lxwc%=:
  v_cmp_ne_u64_e32 vcc, s[82:83], v[246:247]
  s_sub_u32 s73, s73, 1
  s_cbranch_scc1 .Lxwb%=
  s_nop 3
  s_cbranch_vccnz .Lxwa%=
.Lxwb%=:
  s_mov_b64 exec, -1
.Lk2sync%=:
  s_barrier
  s_mul_i32 s74, s44, 36864
  s_add_u32 s74, s74, s76
  s_add_u32 s75, s74, 12288
  s_add_u32 s76, s74, 24576
  buffer_load_dwordx4 v[32:35], v0, s[28:31], s74 offen offset:0 sc1
  buffer_load_dwordx4 v[36:39], v0, s[28:31], s75 offen offset:0 sc1
  buffer_load_dwordx4 v[40:43], v0, s[28:31], s76 offen offset:0 sc1
  buffer_load_dwordx4 v[44:47], v0, s[28:31], s74 offen offset:1024 sc1
  buffer_load_dwordx4 v[48:51], v0, s[28:31], s75 offen offset:1024 sc1
  buffer_load_dwordx4 v[52:55], v0, s[28:31], s76 offen offset:1024 sc1
  buffer_load_dwordx4 v[56:59], v0, s[28:31], s74 offen offset:2048 sc1
  buffer_load_dwordx4 v[60:63], v0, s[28:31], s75 offen offset:2048 sc1
  buffer_load_dwordx4 v[64:67], v0, s[28:31], s76 offen offset:2048 sc1
  s_waitcnt vmcnt(6)
  v_mfma_f32_16x16x32_bf16 v[128:131], v[22:25], v[32:35], v[128:131]
  v_mfma_f32_16x16x32_bf16 v[132:135], v[20:23], v[32:35], v[132:135]
  v_mfma_f32_16x16x32_bf16 v[128:131], v[22:25], v[36:39], v[128:131]
  v_mfma_f32_16x16x32_bf16 v[132:135], v[20:23], v[36:39], v[132:135]
  v_mfma_f32_16x16x32_bf16 v[128:131], v[22:25], v[40:43], v[128:131]
  v_mfma_f32_16x16x32_bf16 v[132:135], v[20:23], v[40:43], v[132:135]
  s_waitcnt vmcnt(3)
  v_mfma_f32_16x16x32_bf16 v[160:163], v[22:25], v[44:47], v[160:163]
  v_mfma_f32_16x16x32_bf16 v[164:167], v[20:23], v[44:47], v[164:167]
  v_mfma_f32_16x16x32_bf16 v[160:163], v[22:25], v[48:51], v[160:163]
  v_mfma_f32_16x16x32_bf16 v[164:167], v[20:23], v[48:51], v[164:167]
  v_mfma_f32_16x16x32_bf16 v[160:163], v[22:25], v[52:55], v[160:163]
  v_mfma_f32_16x16x32_bf16 v[164:167], v[20:23], v[52:55], v[164:167]
  s_waitcnt vmcnt(0)
  v_mfma_f32_16x16x32_bf16 v[192:195], v[22:25], v[56:59], v[192:195]
  v_mfma_f32_16x16x32_bf16 v[196:199], v[20:23], v[56:59], v[196:199]
  v_mfma_f32_16x16x32_bf16 v[192:195], v[22:25], v[60:63], v[192:195]
  v_mfma_f32_16x16x32_bf16 v[196:199], v[20:23], v[60:63], v[196:199]
  v_mfma_f32_16x16x32_bf16 v[192:195], v[22:25], v[64:67], v[192:195]
  v_mfma_f32_16x16x32_bf16 v[196:199], v[20:23], v[64:67], v[196:199]
  s_nop 1
  v_cvt_pk_bf16_f32 v232, v128, v129
  v_cvt_pk_bf16_f32 v233, v130, v131
  v_cvt_pk_bf16_f32 v234, v132, v133
  v_cvt_pk_bf16_f32 v235, v134, v135
  s_mov_b32 s77, 0
  s_nop 0
  v_permlane16_swap_b32_e32 v232, v234
  v_permlane16_swap_b32_e32 v233, v235
  buffer_store_dwordx4 v[232:235], v1, s[24:27], s77 offen
  v_cvt_pk_bf16_f32 v236, v160, v161
  v_cvt_pk_bf16_f32 v237, v162, v163
  v_cvt_pk_bf16_f32 v238, v164, v165
  v_cvt_pk_bf16_f32 v239, v166, v167
  s_mov_b32 s77, 40960
  s_nop 0
  v_permlane16_swap_b32_e32 v236, v238
  v_permlane16_swap_b32_e32 v237, v239
  buffer_store_dwordx4 v[236:239], v1, s[24:27], s77 offen
  v_cvt_pk_bf16_f32 v240, v192, v193
  v_cvt_pk_bf16_f32 v241, v194, v195
  v_cvt_pk_bf16_f32 v242, v196, v197
  v_cvt_pk_bf16_f32 v243, v198, v199
  s_mov_b32 s77, 81920
  s_nop 0
  v_permlane16_swap_b32_e32 v240, v242
  v_permlane16_swap_b32_e32 v241, v243
  buffer_store_dwordx4 v[240:243], v1, s[24:27], s77 offen
  s_waitcnt vmcnt(0) lgkmcnt(0)
.Ldone%=:
  s_waitcnt vmcnt(0) lgkmcnt(0)
)ASM"
      :
      : [ka] "s"(ka), [tag] "s"(tag), [wid] "s"(wid), [gid] "s"(g)
      : "v0", "v1", "v2", "v3", "v4", "v5", "v6", "v7", "v8", "v9", "v10", "v11", "v12", "v13", "v14", "v15", "v16", "v17", "v18", "v19", "v20", "v21", "v22", "v23",
      "v24", "v25", "v26", "v27", "v28", "v29", "v30", "v31", "v32", "v33", "v34", "v35", "v36", "v37", "v38", "v39", "v40", "v41", "v42", "v43", "v44", "v45", "v46", "v47",
      "v48", "v49", "v50", "v51", "v52", "v53", "v54", "v55", "v56", "v57", "v58", "v59", "v60", "v61", "v62", "v63", "v64", "v65", "v66", "v67", "v68", "v69", "v70", "v71",
      "v72", "v73", "v74", "v75", "v76", "v77", "v78", "v79", "v80", "v81", "v82", "v83", "v84", "v85", "v86", "v87", "v88", "v89", "v90", "v91", "v92", "v93", "v94", "v95",
      "v96", "v97", "v98", "v99", "v100", "v101", "v102", "v103", "v104", "v105", "v106", "v107", "v108", "v109", "v110", "v111", "v112", "v113", "v114", "v115", "v116", "v117", "v118", "v119",
      "v120", "v121", "v122", "v123", "v124", "v125", "v126", "v127", "v128", "v129", "v130", "v131", "v132", "v133", "v134", "v135", "v136", "v137", "v138", "v139", "v140", "v141", "v142", "v143",
      "v144", "v145", "v146", "v147", "v148", "v149", "v150", "v151", "v152", "v153", "v154", "v155", "v156", "v157", "v158", "v159", "v160", "v161", "v162", "v163", "v164", "v165", "v166", "v167",
      "v168", "v169", "v170", "v171", "v172", "v173", "v174", "v175", "v176", "v177", "v178", "v179", "v180", "v181", "v182", "v183", "v184", "v185", "v186", "v187", "v188", "v189", "v190", "v191",
      "v192", "v193", "v194", "v195", "v196", "v197", "v198", "v199", "v200", "v201", "v202", "v203", "v204", "v205", "v206", "v207", "v208", "v209", "v210", "v211", "v212", "v213", "v214", "v215",
      "v216", "v217", "v218", "v219", "v220", "v221", "v222", "v223", "v224", "v225", "v226", "v227", "v228", "v229", "v230", "v231", "v232", "v233", "v234", "v235", "v236", "v237", "v238", "v239",
      "v240", "v241", "v242", "v243", "v244", "v245", "v246", "v247", "v248", "v249", "v250", "v251", "v252", "v253", "v254", "v255", "a0", "a1", "a2", "a3", "a4", "a5", "a6", "a7",
      "a8", "a9", "a10", "a11", "a12", "a13", "a14", "a15", "a16", "a17", "a18", "a19", "a20", "a21", "a22", "a23", "a24", "a25", "a26", "a27", "a28", "a29", "a30", "a31",
      "a32", "a33", "a34", "a35", "a36", "a37", "a38", "a39", "a40", "a41", "a42", "a43", "a44", "a45", "a46", "a47", "a48", "a49", "a50", "a51", "a52", "a53", "a54", "a55",
      "a56", "a57", "a58", "a59", "a60", "a61", "a62", "a63", "a64", "a65", "a66", "a67", "a68", "a69", "a70", "a71", "a72", "a73", "a74", "a75", "a76", "a77", "a78", "a79",
      "a80", "a81", "a82", "a83", "a84", "a85", "a86", "a87", "a88", "a89", "a90", "a91", "a92", "a93", "a94", "a95", "a96", "a97", "a98", "a99", "a100", "a101", "a102", "a103",
      "a104", "a105", "a106", "a107", "a108", "a109", "a110", "a111", "a112", "a113", "a114", "a115", "a116", "a117", "a118", "a119", "a120", "a121", "a122", "a123", "a124", "a125", "a126", "a127",
      "a128", "a129", "a130", "a131", "a132", "a133", "a134", "a135", "a136", "a137", "a138", "a139", "a140", "a141", "a142", "a143", "a144", "a145", "a146", "a147", "a148", "a149", "a150", "a151",
      "a152", "a153", "a154", "a155", "a156", "a157", "a158", "a159", "a160", "a161", "a162", "a163", "a164", "a165", "a166", "a167", "a168", "a169", "a170", "a171", "a172", "a173", "a174", "a175",
      "a176", "a177", "a178", "a179", "a180", "a181", "a182", "a183", "a184", "a185", "a186", "a187", "a188", "a189", "a190", "a191", "a192", "a193", "a194", "a195", "a196", "a197", "a198", "a199",
      "a200", "a201", "a202", "a203", "a204", "a205", "a206", "a207", "a208", "a209", "a210", "a211", "a212", "a213", "a214", "a215", "a216", "a217", "a218", "a219", "a220", "a221", "a222", "a223",
      "a224", "a225", "a226", "a227", "a228", "a229", "a230", "a231", "a232", "a233", "a234", "a235", "a236", "a237", "a238", "a239", "a240", "a241", "a242", "a243", "a244", "a245", "a246", "a247",
      "a248", "a249", "a250", "a251", "a252", "a253", "a254", "a255", "s16", "s17", "s18", "s19", "s20", "s21", "s22", "s23", "s24", "s25", "s26", "s27", "s28", "s29", "s30", "s31",
      "s36", "s37", "s38", "s39", "s40", "s41", "s42", "s43", "s44", "s45", "s46", "s47", "s48", "s49", "s50", "s51", "s52", "s53", "s54", "s55", "s56", "s57", "s58", "s59",
      "s60", "s61", "s62", "s63", "s64", "s65", "s66", "s67", "s68", "s69", "s70", "s71", "s72", "s73", "s74", "s75", "s76", "s77", "s78", "s79", "s80", "s81", "s82", "s83",
      "s84", "s85", "s86", "s87", "s88", "s89", "s90", "s91", "s92", "s93", "s94", "s95", "s96", "s97", "s98", "s99", "vcc", "scc", "memory");
  }
}

extern "C" void kernel_launch(void* const* d_in, const int* in_sizes, int n_in, void* d_out, int out_size, void* d_ws, size_t ws_size, hipStream_t stream) {
  (void)in_sizes; (void)n_in; (void)out_size; (void)ws_size;
  (void)hipFuncSetAttribute((const void*)attn_kernel, hipFuncAttributeMaxDynamicSharedMemorySize, 163840);
  attn_kernel<<<256, 256, 163840, stream>>>((const u16*)d_in[0], (const u16*)d_in[1], (const u16*)d_in[2], (const u16*)d_in[3],
                                            (const u16*)d_in[5], (const u16*)d_in[6], (const u16*)d_in[7], (const u16*)d_in[8],
                                            (const u16*)d_in[4], (const u16*)d_in[9], (u16*)d_ws, (u16*)d_out);
}
```
